```python
import torch

def _softmax(x, axis=-1):
    return torch.softmax(x, dim=axis)
def _gelu(x):
    return torch.nn.functional.gelu(x, approximate="tanh")
def _silu(x):
    return torch.nn.functional.silu(x)
def _sigmoid(x):
    return torch.sigmoid(x)
def _layernorm(x, axis=-1, eps=1e-5):
    m = x.mean(dim=axis, keepdim=True)
    v = x.var(dim=axis, keepdim=True, unbiased=False)
    return (x - m) / torch.sqrt(v + eps)
def _scatter_add(tgt, idx, src, axis=0):
    return tgt.clone().index_add_(axis, idx, src)
def _segment_sum(src, seg, n):
    out = torch.zeros((n,) + src.shape[1:], dtype=src.dtype)
    return out.index_add_(0, seg, src)
def _pad(x, pw):
    flat = tuple(v for pair in reversed(pw) for v in pair)
    return torch.nn.functional.pad(x, flat)

kernel_name = 'concat_heads__linear_kernel_hedgehog__sliding_block__prompt'

def setup_inputs():

    _w = (lambda a: (lambda t: t.to(torch.bfloat16) if t.is_floating_point() else t)(torch.as_tensor(a)))
    def _A_setup():
        B, S, D, H = 1, 2048, 640, 32
        Dh = D // H
        return {
            "x": _w((lambda *s: torch.randn(*s, dtype=torch.bfloat16) * 0.4)(B, S, D)),
            "wqkv": _w((lambda *s: torch.randn(*s, dtype=torch.bfloat16) * 1.6)(D, 3 * D)),
            "w_phi": _w((lambda *s: torch.randn(*s, dtype=torch.bfloat16) * 1.6)(H, Dh, Dh) * (Dh ** -0.5)),
            "wo": _w((lambda *s: torch.randn(*s, dtype=torch.bfloat16) * 0.2)(D, D)),
        }
    _A_in = _A_setup()

    def _B_setup():
        B, S, D = 1, 2048, 640
        return {
            "x": _w((lambda *s: torch.randn(*s, dtype=torch.bfloat16) * 0.2)(B, S, D)),
            "wqkv": _w((lambda *s: torch.randn(*s, dtype=torch.bfloat16) * 0.4)(D, 3 * D)),
            "wo": _w((lambda *s: torch.randn(*s, dtype=torch.bfloat16) * 0.2)(D, D)),
        }
    _B_in = _B_setup()

    return {
        "A_x": _A_in["x"],
        "A_wqkv": _A_in["wqkv"],
        "A_w_phi": _A_in["w_phi"],
        "A_wo": _A_in["wo"],
        "B_x": _B_in["x"],
        "B_wqkv": _B_in["wqkv"],
        "B_wo": _B_in["wo"],
    }

def reference(A_x, A_wqkv, A_w_phi, A_wo, B_x, B_wqkv, B_wo):
    def _A_ref(x, wqkv, w_phi, wo):
        B, S, D = x.shape
        H = 32; Dh = D // H
        qkv = (lambda x, *d: x.permute(*d))((x @ wqkv).reshape(B, S, 3, H, Dh), 2, 0, 3, 1, 4)
        q, k, v = qkv[0], qkv[1], qkv[2]
        pq = torch.einsum("bhsd,hdf->bhsf", q, w_phi)
        pk = torch.einsum("bhsd,hdf->bhsf", k, w_phi)
        phi_q = torch.concatenate([_softmax(pq, axis=-1), _softmax(-pq, axis=-1)], axis=-1)
        phi_k = torch.concatenate([_softmax(pk, axis=-1), _softmax(-pk, axis=-1)], axis=-1)
        kv = torch.einsum("bhsf,bhsd->bhfd", phi_k, v)
        z  = torch.einsum("bhsf->bhf", phi_k)
        num = torch.einsum("bhqf,bhfd->bhqd", phi_q, kv)
        den = torch.einsum("bhqf,bhf->bhq", phi_q, z)[..., None] + 1e-6
        o = num / den
        return (lambda x, *d: x.permute(*d))(o, 0, 2, 1, 3).reshape(B, S, D) @ wo

    def _B_ref(x, wqkv, wo):
        B, S, D = x.shape
        H = 32; Dh = D // H; W = 256
        qkv = (lambda x, *d: x.permute(*d))((x @ wqkv).reshape(B, S, 3, H, Dh), 2, 0, 3, 1, 4)
        q, k, v = qkv[0], qkv[1], qkv[2]
        scores = torch.einsum("bhqd,bhkd->bhqk", q, k) * (Dh ** -0.5)
        idx = torch.arange(S)
        mask = (idx[:, None] - idx[None, :] >= 0) & (idx[:, None] - idx[None, :] < W)
        scores = torch.where(mask[None, None], scores, -1e9)
        p = _softmax(scores, axis=-1)
        o = torch.einsum("bhqk,bhkd->bhqd", p, v)
        return (lambda x, *d: x.permute(*d))(o, 0, 2, 1, 3).reshape(B, S, D) @ wo

    a = _A_ref(A_x, A_wqkv, A_w_phi, A_wo)
    b = _B_ref(B_x, B_wqkv, B_wo)
    return torch.concatenate([a, b], axis=-1)
```

```cpp
#include <hip/hip_runtime.h>
#include <hip/hip_bf16.h>
#define MIMIC 1
typedef __hip_bfloat16 bf16;
__device__ __forceinline__ float rb(float x){ return __bfloat162float(__float2bfloat16(x)); }
__device__ __forceinline__ float mr(float x){ return MIMIC ? rb(x) : x; }
constexpr int S=2048,D=640,H=32,DH=20,W=256;
__global__ void gemm_naive(const bf16* A,const bf16* B,bf16* C,int M,int N,int K,int ldc){
  int n=blockIdx.x*blockDim.x+threadIdx.x, m=blockIdx.y; if(n>=N) return;
  float acc=0; for(int k=0;k<K;++k) acc+=__bfloat162float(A[m*K+k])*__bfloat162float(B[k*N+n]);
  C[m*ldc+n]=__float2bfloat16(acc);
}
__global__ void phi_k(const bf16* qkv,const bf16* wphi,float* phi,int off){
  int idx=blockIdx.x*blockDim.x+threadIdx.x; if(idx>=S*H) return; int s=idx/H,h=idx%H;
  float* out=phi+(size_t)idx*40;
  float mx=-1e30f,mn=1e30f;
  for(int f=0;f<DH;++f){ float a=0; for(int d=0;d<DH;++d) a+=__bfloat162float(qkv[s*1920+off+h*DH+d])*__bfloat162float(wphi[(h*DH+d)*DH+f]); a=mr(a); out[f]=a; mx=fmaxf(mx,a); mn=fminf(mn,a);}
  float sp=0,sn=0;
  for(int f=0;f<DH;++f){ float a=out[f]; float ep=__expf(a-mx), en=__expf(mn-a); out[f]=ep; out[20+f]=en; sp+=ep; sn+=en;}
  for(int f=0;f<DH;++f){ out[f]=rb(out[f]/sp); out[20+f]=rb(out[20+f]/sn);}
}
__global__ void kv_k(const bf16* qkv,const float* phik,float* kv){
  int idx=blockIdx.x*blockDim.x+threadIdx.x; if(idx>=H*40*21) return; int h=idx/(840),f=(idx/21)%40,d=idx%21;
  float a=0; for(int s=0;s<S;++s){ float v = d<20? __bfloat162float(qkv[s*1920+1280+h*DH+d]) : 1.f; a+=phik[((size_t)s*H+h)*40+f]*v; }
  kv[idx]=rb(a);
}
__global__ void oa_k(const float* phiq,const float* kv,bf16* o){
  int idx=blockIdx.x*blockDim.x+threadIdx.x; if(idx>=S*H) return; int s=idx/H,h=idx%H;
  const float* p=phiq+(size_t)idx*40; float den=0; for(int f=0;f<40;++f) den+=p[f]*kv[h*840+f*21+20]; den=mr(mr(den)+1e-6f);
  for(int d=0;d<DH;++d){ float a=0; for(int f=0;f<40;++f) a+=p[f]*kv[h*840+f*21+d]; o[s*D+h*DH+d]=__float2bfloat16(mr(a)/den);}
}
__global__ void attn_k(const bf16* qkv,bf16* o,float* scratch){
  int idx=blockIdx.x*blockDim.x+threadIdx.x; if(idx>=S*H) return; int s=idx%S,h=idx/S;
  float* sc=scratch+(size_t)idx*W;
  int k0=s-W+1; if(k0<0)k0=0; float mx=-1e30f;
  for(int k=k0;k<=s;++k){ float a=0; for(int d=0;d<DH;++d) a+=__bfloat162float(qkv[s*1920+h*DH+d])*__bfloat162float(qkv[k*1920+640+h*DH+d]); a=mr(mr(a)*0.22360679774997896f); sc[k-k0]=a; mx=fmaxf(mx,a);}
  float l=0; for(int k=k0;k<=s;++k){ float e=__expf(sc[k-k0]-mx);
    sc[k-k0]=e; l+=e;}
  for(int d=0;d<DH;++d){ float a=0; for(int k=k0;k<=s;++k){
    float p=rb(sc[k-k0]/l);
    a+=p*__bfloat162float(qkv[k*1920+1280+h*DH+d]);}
    o[s*D+h*DH+d]=__float2bfloat16(a);}
}
extern "C" void kernel_launch(void* const* d_in,const int* in_sizes,int n_in,void* d_out,int out_size,void* d_ws,size_t ws_size,hipStream_t stream){
  const bf16* Ax=(const bf16*)d_in[0];const bf16* Aw=(const bf16*)d_in[1];const bf16* Aphi=(const bf16*)d_in[2];const bf16* Awo=(const bf16*)d_in[3];
  const bf16* Bx=(const bf16*)d_in[4];const bf16* Bw=(const bf16*)d_in[5];const bf16* Bwo=(const bf16*)d_in[6];
  bf16* out=(bf16*)d_out; char* ws=(char*)d_ws;
  bf16* qkvA=(bf16*)ws; ws+=S*1920*2; bf16* qkvB=(bf16*)ws; ws+=S*1920*2; bf16* oA=(bf16*)ws; ws+=S*D*2; bf16* oB=(bf16*)ws; ws+=S*D*2;
  float* phiq=(float*)ws; ws+=(size_t)S*H*40*4; float* phik=(float*)ws; ws+=(size_t)S*H*40*4; float* kv=(float*)ws; ws+=H*840*4+1024; float* sc=(float*)ws;
  gemm_naive<<<dim3(1920/128,S),128,0,stream>>>(Ax,Aw,qkvA,S,1920,D,1920);
  gemm_naive<<<dim3(1920/128,S),128,0,stream>>>(Bx,Bw,qkvB,S,1920,D,1920);
  phi_k<<<S*H/256,256,0,stream>>>(qkvA,Aphi,phiq,0);
  phi_k<<<S*H/256,256,0,stream>>>(qkvA,Aphi,phik,640);
  kv_k<<<(H*840+255)/256,256,0,stream>>>(qkvA,phik,kv);
  oa_k<<<S*H/256,256,0,stream>>>(phiq,kv,oA);
  attn_k<<<S*H/256,256,0,stream>>>(qkvB,oB,sc);
  gemm_naive<<<dim3(640/128,S),128,0,stream>>>(oA,Awo,out,S,D,D,1280);
  gemm_naive<<<dim3(640/128,S),128,0,stream>>>(oB,Bwo,out+640,S,D,D,1280);
}
```

```cpp
#include <hip/hip_runtime.h>
#include <hip/hip_bf16.h>

typedef short bf16x8 __attribute__((ext_vector_type(8)));
typedef short s16x4 __attribute__((ext_vector_type(4)));
typedef float f32x2 __attribute__((ext_vector_type(2)));
typedef float f32x4 __attribute__((ext_vector_type(4)));
typedef float f32x16 __attribute__((ext_vector_type(16)));
typedef unsigned u32x2 __attribute__((ext_vector_type(2)));
typedef unsigned u32x4 __attribute__((ext_vector_type(4)));
typedef __bf16 bf16x2_t __attribute__((ext_vector_type(2)));
#define LAS __attribute__((address_space(3)))
#define DI __device__ __forceinline__

constexpr int SEQ = 2048, DM = 640, NH = 32;
constexpr unsigned WS_QA = 0;
constexpr unsigned WS_QB = WS_QA + NH * SEQ * 40;
constexpr unsigned WS_KB = WS_QB + NH * SEQ * 40;
constexpr unsigned WS_VB = WS_KB + NH * SEQ * 40;
constexpr unsigned WS_KVP = WS_VB + NH * SEQ * 48;
constexpr unsigned WS_OA = WS_KVP + 16 * NH * 3360;
constexpr unsigned WS_OB = WS_OA + SEQ * DM * 2;
constexpr unsigned WS_END = WS_OB + SEQ * DM * 2;

DI unsigned pk2(float lo, float hi) {
  bf16x2_t r = __builtin_convertvector((f32x2){lo, hi}, bf16x2_t);
  return __builtin_bit_cast(unsigned, r);
}
DI float rbf(float x) {
  return __builtin_bit_cast(float, pk2(0.f, x));
}
DI s16x4 trread(const LAS char* p) {
  return __builtin_amdgcn_ds_read_tr16_b64_v4i16((LAS s16x4*)p);
}
DI bf16x8 cat8(s16x4 a, s16x4 b) { return __builtin_shufflevector(a, b, 0, 1, 2, 3, 4, 5, 6, 7); }
DI bf16x8 asfrag(u32x4 v) { return __builtin_bit_cast(bf16x8, v); }
DI __amdgpu_buffer_rsrc_t mkrsrc(const void* p, unsigned bytes) {
  return __builtin_amdgcn_make_buffer_rsrc((void*)p, (short)0, (int)bytes, 0x00020000);
}
DI void dma16(__amdgpu_buffer_rsrc_t rs, unsigned lds, unsigned voff, unsigned soff) {
  asm volatile("s_mov_b32 m0, %0\n\ts_nop 0\n\tbuffer_load_dwordx4 %1, %2, %3 offen lds"
               :: "s"(lds), "v"(voff), "s"(rs), "s"(soff) : "memory");
}
template <unsigned HI>
DI void dma16m(__amdgpu_buffer_rsrc_t rs, unsigned lds, unsigned voff, unsigned soff) {
  asm volatile("s_mov_b32 m0, %0\n\ts_mov_b32 exec_hi, %4\n\tbuffer_load_dwordx4 %1, %2, %3 offen lds\n\ts_mov_b32 exec_hi, -1"
               :: "s"(lds), "v"(voff), "s"(rs), "s"(soff), "i"(HI) : "memory");
}
#define MFMA16(a, b, c) __builtin_amdgcn_mfma_f32_16x16x32_bf16((a), (b), (c), 0, 0, 0)
#define MFMA32(a, b, c) __builtin_amdgcn_mfma_f32_32x32x16_bf16((a), (b), (c), 0, 0, 0)
#define WAIT_BAR(N) asm volatile("s_waitcnt vmcnt(" #N ") lgkmcnt(0)\n\ts_barrier" ::: "memory")
#define LBAR() asm volatile("s_waitcnt lgkmcnt(0)\n\ts_barrier" ::: "memory")

constexpr float LOG2E = 1.4426950408889634f;

struct WFrag { bf16x8 t0k0, t0k1, t1k0, t1k1; };
DI WFrag load_wfrag(const LAS char* wl  , int lane) {
  const int hh = lane >> 5, b = (lane >> 4) & 1, q = (lane & 15) >> 2, p = lane & 3;
  const LAS char* a0 = wl + (8 * hh + q) * 40 + (2 * b + (p >> 1)) * 8;
  const LAS char* a1 = wl + (8 * hh + q) * 40 + 32;
  const LAS char* c0 = wl + (16 + q) * 40 + (2 * b + (p >> 1)) * 8;
  const LAS char* c1 = wl + (16 + q) * 40 + 32;
  u32x2 r0 = __builtin_bit_cast(u32x2, trread(a0)), r1 = __builtin_bit_cast(u32x2, trread(a0 + 160));
  u32x2 r2 = __builtin_bit_cast(u32x2, trread(a1)), r3 = __builtin_bit_cast(u32x2, trread(a1 + 160));
  u32x2 r4 = __builtin_bit_cast(u32x2, trread(c0)), r5 = __builtin_bit_cast(u32x2, trread(c1));
  const unsigned sg = (lane & 4) ? 0x80008000u : 0u;
  const unsigned km = hh ? 0u : 0xFFFFFFFFu;
  WFrag w;
  w.t0k0 = asfrag((u32x4){r0[0] ^ sg, r0[1] ^ sg, r1[0] ^ sg, r1[1] ^ sg});
  w.t1k0 = asfrag((u32x4){r2[0] ^ sg, r2[1] ^ sg, r3[0] ^ sg, r3[1] ^ sg});
  w.t0k1 = asfrag((u32x4){(r4[0] ^ sg) & km, (r4[1] ^ sg) & km, 0u, 0u});
  w.t1k1 = asfrag((u32x4){(r5[0] ^ sg) & km, (r5[1] ^ sg) & km, 0u, 0u});
  return w;
}
DI void hedgehog(const WFrag& w, bf16x8 x0, bf16x8 x1, unsigned (&ph)[10]) {
  f32x16 z = {};
  f32x16 p0 = MFMA32(w.t0k0, x0, z);
  f32x16 p1 = MFMA32(w.t1k0, x0, z);
  p0 = MFMA32(w.t0k1, x1, p0);
  p1 = MFMA32(w.t1k1, x1, p1);
  float v[20];
#pragma unroll
  for (int i = 0; i < 16; ++i) v[i] = rbf(p0[i]);
#pragma unroll
  for (int i = 0; i < 4; ++i) v[16 + i] = rbf(p1[i]);
  float m = v[0];
#pragma unroll
  for (int i = 1; i < 20; ++i) m = __builtin_fmaxf(m, v[i]);
  const float nm = -m * LOG2E;
  float s = 0.f;
#pragma unroll
  for (int i = 0; i < 20; ++i) { v[i] = __builtin_amdgcn_exp2f(__builtin_fmaf(v[i], LOG2E, nm)); s += v[i]; }
  const float r = __builtin_amdgcn_rcpf(s);
#pragma unroll
  for (int i = 0; i < 10; ++i) ph[i] = pk2(v[2 * i] * r, v[2 * i + 1] * r);
}

constexpr int K1_STAGE = 16384 + 30720;
constexpr int K1_WPHI = 3 * K1_STAGE + 256;
constexpr int K1_LDS = K1_WPHI + 4096;
constexpr int EP_Q = 0, EP_K = 20480, EP_V = 40960, EP_PHI = 65536 + 256;

__global__ __launch_bounds__(512, 2) void k1_qkv(const __hip_bfloat16* __restrict__ xA, const __hip_bfloat16* __restrict__ wA,
                                                 const __hip_bfloat16* __restrict__ xB, const __hip_bfloat16* __restrict__ wB,
                                                 const __hip_bfloat16* __restrict__ wphi, char* __restrict__ ws) {
  __shared__ __attribute__((aligned(16))) char smem[K1_LDS];
  const LAS char* sm = (const LAS char*)smem;
  LAS char* smw = (LAS char*)smem;
  const unsigned lds0 = (unsigned)(size_t)(LAS char*)smem;
  const int tid = threadIdx.x, lane = tid & 63;
  const int wid = __builtin_amdgcn_readfirstlane(tid >> 6);
  const int xcd = blockIdx.x & 7, idx = blockIdx.x >> 3;
  const int br = xcd >> 2;
  const int mt = ((xcd >> 1) & 1) * 8 + (idx & 7), nt = (xcd & 1) * 4 + (idx >> 3);
  const char* xg = (const char*)(br ? xB : xA) + (size_t)mt * 128 * 1280;
  const char* wg = (const char*)(br ? wB : wA) + nt * 160;
  const __amdgpu_buffer_rsrc_t rx = mkrsrc(xg, 128 * 1280);
  const __amdgpu_buffer_rsrc_t rw = mkrsrc(wg, 640 * 3840 - nt * 160);
  const __amdgpu_buffer_rsrc_t rp = mkrsrc((const char*)wphi + nt * 3200, 3200);

  const int xr = lane >> 3;
  const unsigned vx0 = (unsigned)((16 * wid + xr) * 1280 + (((lane & 7) ^ (xr >> 1)) * 16));
  const unsigned vx1 = (unsigned)((16 * wid + 8 + xr) * 1280 + (((lane & 7) ^ (4 + (xr >> 1))) * 16));
  const int wr = lane >= 30 ? 1 : 0, wc = lane - 30 * wr, wsg = (wc >= 10) + (wc >= 20);
  const unsigned vw = (unsigned)(wr * 3840 + wsg * 1280 + (wc - 10 * wsg) * 16);
  auto stage = [&](int kt, int buf) {
    const unsigned lb = lds0 + buf * K1_STAGE;
    dma16(rx, lb + wid * 2048, vx0, kt * 128);
    dma16(rx, lb + wid * 2048 + 1024, vx1, kt * 128);
#pragma unroll
    for (int i = 0; i < 4; ++i) {
      const int pos = 8 * wid + 2 * i;
      const int k = (pos & ~12) | ((pos & 4) << 1) | ((pos & 8) >> 1);
      dma16m<0x0FFFFFFFu>(rw, lb + 16384 + pos * 480, vw, (unsigned)(kt * 64 + k) * 3840u);
    }
  };
  dma16(rp, lds0 + K1_WPHI + (wid & 3) * 1024, (unsigned)((wid & 3) * 1024 + lane * 16), 0);
  stage(0, 0);
  stage(1, 1);

  const int wm = wid >> 2, wn = wid & 3;
  const int g = lane >> 4, fr = lane & 15, q4 = fr >> 2, p4 = lane & 3;
  const int xa0 = (64 * wm + fr) * 128 + ((g ^ (fr >> 1)) * 16);
  const int xa1 = xa0 ^ 64;
  const int wa = 16384 + (16 * (g >> 1) + 4 * (g & 1) + q4) * 480 + 8 * p4 + wn * 128;
  f32x4 acc[4][4] = {};
  int buf = 0;
  for (int kt = 0; kt < 10; ++kt) {
    if (kt < 9) { WAIT_BAR(6); } else { WAIT_BAR(0); }
    if (kt < 8) stage(kt + 2, buf >= 1 ? buf - 1 : 2);
    const LAS char* sb = sm + buf * K1_STAGE;
#pragma unroll
    for (int kk = 0; kk < 2; ++kk) {
      bf16x8 xf[4], wf[4];
#pragma unroll
      for (int i = 0; i < 4; ++i) xf[i] = *(const LAS bf16x8*)(sb + (kk ? xa1 : xa0) + i * 2048);
#pragma unroll
      for (int j = 0; j < 4; ++j)
        wf[j] = cat8(trread(sb + wa + kk * 32 * 480 + j * 32), trread(sb + wa + (kk * 32 + 8) * 480 + j * 32));
#pragma unroll
      for (int i = 0; i < 4; ++i)
#pragma unroll
        for (int j = 0; j < 4; ++j) acc[i][j] = MFMA16(wf[j], xf[i], acc[i][j]);
    }
    buf = buf == 2 ? 0 : buf + 1;
  }
  LBAR();
#pragma unroll
  for (int j = 0; j < 4; ++j) {
    const int qd = 16 * wn + 4 * j + g;
    const int seg = (qd >= 20) + (qd >= 40), r20 = qd - 20 * seg;
    const int hl = (r20 >= 5) + (r20 >= 10) + (r20 >= 15), c = r20 - 5 * hl;
    const int rs = seg == 2 ? 48 : 40;
    const int base = seg * 20480 + hl * 128 * rs + (64 * wm + fr) * rs + c * 8;
    if (qd < 60) {
#pragma unroll
      for (int i = 0; i < 4; ++i)
        *(LAS u32x2*)(smw + base + i * 16 * rs) = (u32x2){pk2(acc[i][j][0], acc[i][j][1]), pk2(acc[i][j][2], acc[i][j][3])};
    }
  }
  *(LAS u32x2*)(smw + EP_V + tid * 48 + 40) = (u32x2){0x3F80u, 0u};
  LBAR();
  {
    const unsigned rowoff = (unsigned)(4 * nt * SEQ + mt * 128);
    char* qg = ws + (br ? WS_QB : WS_QA) + rowoff * 40u;
#pragma unroll
    for (int i = 0; i < 3; ++i) {
      const int c = tid + 512 * i;
      if (c < 1280) {
        const int hl = c / 320, w = c - hl * 320;
        *(u32x4*)(qg + hl * (SEQ * 40) + w * 16) = *(const LAS u32x4*)(sm + EP_Q + c * 16);
      }
    }
    if (br) {
      char* kg = ws + WS_KB + rowoff * 40u;
      char* vg = ws + WS_VB + rowoff * 48u;
#pragma unroll
      for (int i = 0; i < 3; ++i) {
        const int c = tid + 512 * i;
        if (c < 1280) {
          const int hl = c / 320, w = c - hl * 320;
          *(u32x4*)(kg + hl * (SEQ * 40) + w * 16) = *(const LAS u32x4*)(sm + EP_K + c * 16);
        }
      }
#pragma unroll
      for (int i = 0; i < 3; ++i) {
        const int c = tid + 512 * i;
        const int hl = c / 384, w = c - hl * 384;
        *(u32x4*)(vg + hl * (SEQ * 48) + w * 16) = *(const LAS u32x4*)(sm + EP_V + c * 16);
      }
      return;
    }
  }
  const int hl = wid >> 1, hh = lane >> 5, r32 = lane & 31;
  {
    const WFrag wf = load_wfrag(sm + K1_WPHI + hl * 800, lane);
#pragma unroll
    for (int t = 0; t < 2; ++t) {
      const int row = 64 * (wid & 1) + 32 * t + r32;
      const LAS char* kp = sm + EP_K + hl * 5120 + row * 40;
      const u32x2 a = *(const LAS u32x2*)(kp + 16 * hh), b = *(const LAS u32x2*)(kp + 16 * hh + 8), c = *(const LAS u32x2*)(kp + 32);
      unsigned ph[10];
      hedgehog(wf, asfrag((u32x4){a[0], a[1], b[0], b[1]}), asfrag((u32x4){c[0], c[1], c[0], c[1]}), ph);
      LAS char* pp = smw + EP_PHI + (hl * 128 + row) * 88 + hh * 40;
#pragma unroll
      for (int i = 0; i < 5; ++i) *(LAS u32x2*)(pp + 8 * i) = (u32x2){ph[2 * i], ph[2 * i + 1]};
    }
  }
  LBAR();
  {
    const int ft = wid & 1, b = (lane >> 4) & 1;
    const LAS char* pa = sm + EP_PHI + (hl * 128 + 8 * hh + q4) * 88 + (32 * ft + 16 * b + 4 * p4) * 2;
    const LAS char* va = sm + EP_V + (hl * 128 + 8 * hh + q4) * 48 + (16 * b + 4 * p4) * 2;
    f32x16 d = {};
#pragma unroll
    for (int ks = 0; ks < 8; ++ks) {
      const bf16x8 af = cat8(trread(pa + ks * 16 * 88), trread(pa + (ks * 16 + 4) * 88));
      const bf16x8 bf = cat8(trread(va + ks * 16 * 48), trread(va + (ks * 16 + 4) * 48));
      d = MFMA32(af, bf, d);
    }
    if (r32 <= 20) {
      float* o = (float*)(ws + WS_KVP) + ((mt * NH + 4 * nt + hl) * 21 + r32) * 40 + 32 * ft + 4 * hh;
      *(f32x4*)o = (f32x4){d[0], d[1], d[2], d[3]};
      if (ft == 0) {
        *(f32x4*)(o + 8) = (f32x4){d[4], d[5], d[6], d[7]};
        *(f32x4*)(o + 16) = (f32x4){d[8], d[9], d[10], d[11]};
        *(f32x4*)(o + 24) = (f32x4){d[12], d[13], d[14], d[15]};
      }
    }
  }
}

constexpr int K2_K = 0, K2_V = 20480, K2_KV = K2_V + 24576 + 64, K2_W = K2_KV + 32 * 112, K2_LDS = K2_W + 1024;
constexpr float SC2 = 0.22360679774997896f * 1.4426950408889634f;

DI void store_o(char* og, int h, int srow, int hh, const float (&o)[12]) {
  const int p = h >> 1, odd = h & 1;
  char* b = og + (unsigned)(((srow >> 4) * 20 + 5 * (p >> 2)) * 1024 + ((p & 3) * 16 + (srow & 15)) * 16);
  *(u32x4*)(b + (3 * odd + hh) * 1024) = (u32x4){pk2(o[0], o[1]), pk2(o[2], o[3]), pk2(o[4], o[5]), pk2(o[6], o[7])};
  if (hh == 0) *(u32x2*)(b + 2048 + 8 * odd) = (u32x2){pk2(o[8], o[9]), pk2(o[10], o[11])};
}

__global__ __launch_bounds__(512, 2) void k2_attn(const __hip_bfloat16* __restrict__ wphi, char* __restrict__ ws) {
  __shared__ __attribute__((aligned(16))) char smem[K2_LDS];
  const LAS char* sm = (const LAS char*)smem;
  LAS char* smw = (LAS char*)smem;
  const unsigned lds0 = (unsigned)(size_t)(LAS char*)smem;
  const int tid = threadIdx.x, lane = tid & 63;
  const int wid = __builtin_amdgcn_readfirstlane(tid >> 6);
  const int h = (blockIdx.x & 7) * 4 + ((blockIdx.x >> 3) & 3), rb = blockIdx.x >> 5;
  const int q0 = rb * 256;
  const int hh = lane >> 5, r32 = lane & 31, b16 = (lane >> 4) & 1, q4 = (lane & 15) >> 2, p4 = lane & 3;
  {
    const __amdgpu_buffer_rsrc_t rk = mkrsrc(ws + WS_KB + h * (SEQ * 40), SEQ * 40);
    const __amdgpu_buffer_rsrc_t rv = mkrsrc(ws + WS_VB + h * (SEQ * 48), SEQ * 48);
    const __amdgpu_buffer_rsrc_t rp = mkrsrc((const char*)wphi + h * 800, 800);
    const unsigned vo = lane * 16;
#pragma unroll
    for (int i = 0; i < 3; ++i) {
      const int pc = wid + 8 * i;
      if (pc < 20 && (rb > 0 || pc >= 10)) dma16(rk, lds0 + K2_K + pc * 1024, vo, (unsigned)((q0 - 256) * 40 + pc * 1024));
    }
#pragma unroll
    for (int i = 0; i < 3; ++i) {
      const int pc = wid + 8 * i;
      if (rb > 0 || pc >= 12) dma16(rv, lds0 + K2_V + pc * 1024, vo, (unsigned)((q0 - 256) * 48 + pc * 1024));
    }
    if (wid == 7) dma16(rp, lds0 + K2_W, vo, 0);
    if (rb == 0) {
      *(LAS u32x4*)(smw + K2_K + tid * 16) = (u32x4){0u, 0u, 0u, 0u};
      if (tid < 128) *(LAS u32x4*)(smw + K2_K + 8192 + tid * 16) = (u32x4){0u, 0u, 0u, 0u};
      *(LAS u32x4*)(smw + K2_V + tid * 16) = (u32x4){0u, 0u, 0u, 0u};
      if (tid < 256) *(LAS u32x4*)(smw + K2_V + 8192 + tid * 16) = (u32x4){0u, 0u, 0u, 0u};
    }
  }
  const int srow = q0 + 32 * wid + r32;
  const unsigned qoff = (unsigned)((h * SEQ + srow) * 40);
  const u32x2 qa0 = *(const u32x2*)(ws + WS_QA + qoff + 16 * hh), qa1 = *(const u32x2*)(ws + WS_QA + qoff + 16 * hh + 8);
  const u32x2 qa2 = *(const u32x2*)(ws + WS_QA + qoff + 32);
  const u32x2 qb0 = *(const u32x2*)(ws + WS_QB + qoff + 16 * hh), qb1 = *(const u32x2*)(ws + WS_QB + qoff + 16 * hh + 8);
  const u32x2 qb2 = *(const u32x2*)(ws + WS_QB + qoff + 32);
  if (tid < 420) {
    const int f4 = tid >> 1, hf = tid & 1;
    const f32x4* src = (const f32x4*)(ws + WS_KVP) + (hf * 8 * NH + h) * 210 + f4;
    f32x4 s = src[0];
#pragma unroll
    for (int i = 1; i < 8; ++i) s += src[i * NH * 210];
#pragma unroll
    for (int i = 0; i < 4; ++i) s[i] += __shfl_xor(s[i], 1);
    const int d = f4 / 10, fq = f4 - d * 10;
    const int off = (fq >= 5 ? 48 + (fq - 5) * 8 : fq * 8);
    const u32x2 v = (u32x2){pk2(s[0], s[1]), pk2(s[2], s[3])};
    if (hf == 0) *(LAS u32x2*)(smw + K2_KV + d * 112 + off) = v;
    else if (d == 20) *(LAS u32x2*)(smw + K2_KV + 24 * 112 + off) = v;
  } else if (tid >= 448) {
    *(LAS u32x2*)(smw + K2_KV + ((tid - 448) >> 1) * 112 + (tid & 1) * 48 + 40) = (u32x2){0u, 0u};
  }
  WAIT_BAR(0);
  {
    const WFrag wf = load_wfrag(sm + K2_W, lane);
    unsigned ph[10];
    hedgehog(wf, asfrag((u32x4){qa0[0], qa0[1], qa1[0], qa1[1]}), asfrag((u32x4){qa2[0], qa2[1], qa2[0], qa2[1]}), ph);
    const LAS char* kp = sm + K2_KV + r32 * 112 + hh * 48;
    f32x16 n = {};
    n = MFMA32(*(const LAS bf16x8*)(kp), asfrag((u32x4){ph[0], ph[1], ph[2], ph[3]}), n);
    n = MFMA32(*(const LAS bf16x8*)(kp + 16), asfrag((u32x4){ph[4], ph[5], ph[6], ph[7]}), n);
    n = MFMA32(*(const LAS bf16x8*)(kp + 32), asfrag((u32x4){ph[8], ph[9], 0u, 0u}), n);
    const float den = rbf(rbf(hh ? n[8] : n[12]) + 1e-6f);
    const float r = 1.0f / den;
    float o[12];
#pragma unroll
    for (int i = 0; i < 12; ++i) o[i] = rbf(n[i]) * r;
    store_o(ws + WS_OA, h, srow, hh, o);
  }
  {
    const unsigned km = hh ? 0u : 0xFFFFFFFFu;
    const bf16x8 qf0 = asfrag((u32x4){qb0[0], qb0[1], qb1[0], qb1[1]});
    const bf16x8 qf1 = asfrag((u32x4){qb2[0] & km, qb2[1] & km, 0u, 0u});
    const LAS char* kp = sm + K2_K + (32 * wid + r32) * 40;
    f32x16 s[9];
#pragma unroll
    for (int t = 0; t < 9; ++t) {
      const u32x2 a = *(const LAS u32x2*)(kp + t * 1280 + 16 * hh), b = *(const LAS u32x2*)(kp + t * 1280 + 16 * hh + 8);
      const u32x2 c = *(const LAS u32x2*)(kp + t * 1280 + 32);
      f32x16 z = {};
      s[t] = MFMA32(asfrag((u32x4){a[0], a[1], b[0], b[1]}), qf0, z);
      s[t] = MFMA32(asfrag((u32x4){c[0], c[1], c[0], c[1]}), qf1, s[t]);
    }
    if (rb == 0) {
#pragma unroll
      for (int t = 0; t < 8; ++t)
        if (wid + t < 8) {
#pragma unroll
          for (int r = 0; r < 16; ++r) s[t][r] = -INFINITY;
        }
    }
#pragma unroll
    for (int r = 0; r < 16; ++r) {
      const int j = (r & 3) + 8 * (r >> 2) + 4 * hh;
      s[8][r] = (j <= r32) ? s[8][r] : s[0][r];
    }
    float m = s[8][0];
#pragma unroll
    for (int t = 1; t < 9; ++t)
#pragma unroll
      for (int r = 0; r < 16; ++r) m = __builtin_fmaxf(m, s[t][r]);
    {
      auto rr = __builtin_amdgcn_permlane32_swap(__float_as_uint(m), __float_as_uint(m), false, false);
      m = __builtin_fmaxf(__uint_as_float(rr[0]), __uint_as_float(rr[1]));
    }
    const float nm = -m * SC2;
    const LAS char* vp = sm + K2_V + (32 * wid + 4 * hh + q4) * 48 + (16 * b16 + 4 * p4) * 2;
    f32x16 o = {};
#pragma unroll
    for (int t = 1; t < 9; ++t) {
      unsigned pw[8];
#pragma unroll
      for (int i = 0; i < 8; ++i)
        pw[i] = pk2(__builtin_amdgcn_exp2f(__builtin_fmaf(s[t][2 * i], SC2, nm)), __builtin_amdgcn_exp2f(__builtin_fmaf(s[t][2 * i + 1], SC2, nm)));
      if (t == 8) {
        unsigned pl[8];
#pragma unroll
        for (int i = 0; i < 8; ++i) {
          const int j = ((2 * i) & 3) + 8 * ((2 * i) >> 2) + 4 * hh;
          const unsigned mk = (j <= r32 ? 0xFFFFu : 0u) | (j + 1 <= r32 ? 0xFFFF0000u : 0u);
          pl[i] = pw[i] & ~mk;
          pw[i] &= mk;
        }
        o = MFMA32(cat8(trread(vp), trread(vp + 8 * 48)), asfrag((u32x4){pl[0], pl[1], pl[2], pl[3]}), o);
        o = MFMA32(cat8(trread(vp + 16 * 48), trread(vp + 24 * 48)), asfrag((u32x4){pl[4], pl[5], pl[6], pl[7]}), o);
      }
      o = MFMA32(cat8(trread(vp + t * 1536), trread(vp + t * 1536 + 8 * 48)), asfrag((u32x4){pw[0], pw[1], pw[2], pw[3]}), o);
      o = MFMA32(cat8(trread(vp + t * 1536 + 16 * 48), trread(vp + t * 1536 + 24 * 48)), asfrag((u32x4){pw[4], pw[5], pw[6], pw[7]}), o);
    }
    auto rr = __builtin_amdgcn_permlane32_swap(__float_as_uint(o[8]), __float_as_uint(o[8]), false, false);
    const float r = __builtin_amdgcn_rcpf(__uint_as_float(rr[1]));
    float of[12];
#pragma unroll
    for (int i = 0; i < 12; ++i) of[i] = o[i] * r;
    store_o(ws + WS_OB, h, srow, hh, of);
  }
}

constexpr int K3_W = 0, K3_X = 20 * 5120 + 256, K3_LDS = K3_X + 8 * 5 * 1024;

__global__ __launch_bounds__(512, 2) void k3_out(const __hip_bfloat16* __restrict__ woA, const __hip_bfloat16* __restrict__ woB,
                                                 const char* __restrict__ ws, __hip_bfloat16* __restrict__ out) {
  __shared__ __attribute__((aligned(16))) char smem[K3_LDS];
  const LAS char* sm = (const LAS char*)smem;
  LAS char* smw = (LAS char*)smem;
  const unsigned lds0 = (unsigned)(size_t)(LAS char*)smem;
  const int tid = threadIdx.x, lane = tid & 63;
  const int wid = __builtin_amdgcn_readfirstlane(tid >> 6);
  const int xcd = blockIdx.x & 7, idx = blockIdx.x >> 3;
  const int br = xcd >> 2;
  const int mt = (xcd & 3) * 4 + (idx & 3), nt = idx >> 2;
  const int wm = wid & 3, kh = wid >> 2;
  const __amdgpu_buffer_rsrc_t rw = mkrsrc((const char*)(br ? woB : woA) + nt * 160, 640 * 1280 - nt * 160);
  const __amdgpu_buffer_rsrc_t ro = mkrsrc(ws + (br ? WS_OB : WS_OA) + (unsigned)((mt * 8 + 2 * wm) * 20 + 10 * kh) * 1024u, 30 * 1024);
  const int lq = (lane >= 10) + (lane >= 20) + (lane >= 30);
  const unsigned vw = (unsigned)(lq * 1280 + (lane - 10 * lq) * 16);
  const int sg = 2 * (wid >> 2) + (wid & 1), st = (wid >> 1) & 1;
  const unsigned vo = lane * 16;
  u32x4 xf[10][2];
#pragma unroll
  for (int tau = 0; tau < 10; ++tau) {
#pragma unroll
    for (int k2 = 0; k2 < 2; ++k2) {
      const int ks = 10 * k2 + tau, w5 = ks % 5;
      const int o0 = w5 == 0 ? 0 : w5 == 1 ? 4 : w5 == 2 ? 16 : w5 == 3 ? 20 : 24;
      const int row = 160 * (ks / 5) + o0 + 40 * sg + st * (w5 == 2 ? 20 : 8);
      dma16m<0xFFu>(rw, lds0 + K3_W + ks * 5120 + wid * 640, vw, (unsigned)row * 1280u);
    }
    asm volatile("buffer_load_dwordx4 %0, %1, %2, 0 offen offset:%3" : "=v"(xf[tau][0]) : "v"(vo + (tau >> 2) * 4096), "s"(ro), "i"((tau & 3) * 1024) : "memory");
    asm volatile("buffer_load_dwordx4 %0, %1, %2, 0 offen offset:%3" : "=v"(xf[tau][1]) : "v"(vo + 20480 + (tau >> 2) * 4096), "s"(ro), "i"((tau & 3) * 1024) : "memory");
  }
  const int g = lane >> 4, fr = lane & 15, q4 = fr >> 2, p4 = lane & 3;
  const LAS char* wa = sm + K3_W + kh * 51200 + (16 * (g >> 1) + 4 * (g & 1) + q4) * 160 + 8 * p4;
  f32x4 acc[2][5] = {};
#define K3_PHASE(PH, N)                                                                                              \
  asm volatile("s_waitcnt vmcnt(" #N ")\n\ts_barrier"                                                                \
               : "+v"(xf[2 * PH][0]), "+v"(xf[2 * PH][1]), "+v"(xf[2 * PH + 1][0]), "+v"(xf[2 * PH + 1][1])::"memory"); \
  _Pragma("unroll") for (int tt = 0; tt < 2; ++tt) {                                                                 \
    const int tau = 2 * PH + tt;                                                                                     \
    _Pragma("unroll") for (int j = 0; j < 5; ++j) {                                                                  \
      const bf16x8 wf = cat8(trread(wa + tau * 5120 + j * 32), trread(wa + tau * 5120 + 8 * 160 + j * 32));          \
      acc[0][j] = MFMA16(wf, asfrag(xf[tau][0]), acc[0][j]);                                                         \
      acc[1][j] = MFMA16(wf, asfrag(xf[tau][1]), acc[1][j]);                                                         \
    }                                                                                                                \
  }
  K3_PHASE(0, 32)
  K3_PHASE(1, 24)
  K3_PHASE(2, 16)
  K3_PHASE(3, 8)
  K3_PHASE(4, 0)
#undef K3_PHASE
#pragma unroll
  for (int j = 0; j < 5; ++j) *(LAS f32x4*)(smw + K3_X + (wid * 5 + j) * 1024 + lane * 16) = kh ? acc[0][j] : acc[1][j];
  LBAR();
  char* og = (char*)out + (size_t)(mt * 128 + 32 * wm + 16 * kh + fr) * 2560 + br * 1280 + nt * 160 + g * 8;
#pragma unroll
  for (int j = 0; j < 5; ++j) {
    const f32x4 o = *(const LAS f32x4*)(sm + K3_X + ((wid ^ 4) * 5 + j) * 1024 + lane * 16);
    const f32x4 a = kh ? acc[1][j] : acc[0][j];
    *(u32x2*)(og + j * 32) = (u32x2){pk2(a[0] + o[0], a[1] + o[1]), pk2(a[2] + o[2], a[3] + o[3])};
  }
}

extern "C" void kernel_launch(void* const* d_in, const int* in_sizes, int n_in, void* d_out, int out_size, void* d_ws,
                              size_t ws_size, hipStream_t stream) {
  const __hip_bfloat16* Ax = (const __hip_bfloat16*)d_in[0];
  const __hip_bfloat16* Aw = (const __hip_bfloat16*)d_in[1];
  const __hip_bfloat16* Aphi = (const __hip_bfloat16*)d_in[2];
  const __hip_bfloat16* Awo = (const __hip_bfloat16*)d_in[3];
  const __hip_bfloat16* Bx = (const __hip_bfloat16*)d_in[4];
  const __hip_bfloat16* Bw = (const __hip_bfloat16*)d_in[5];
  const __hip_bfloat16* Bwo = (const __hip_bfloat16*)d_in[6];
  char* ws = (char*)d_ws;
  k1_qkv<<<256, 512, 0, stream>>>(Ax, Aw, Bx, Bw, Aphi, ws);
  k2_attn<<<256, 512, 0, stream>>>(Aphi, ws);
  k3_out<<<256, 512, 0, stream>>>(Awo, Bwo, ws, (__hip_bfloat16*)d_out);
}
```

```cpp
#include <hip/hip_runtime.h>
#include <hip/hip_bf16.h>

typedef short bf16x8 __attribute__((ext_vector_type(8)));
typedef short s16x4 __attribute__((ext_vector_type(4)));
typedef float f32x2 __attribute__((ext_vector_type(2)));
typedef float f32x4 __attribute__((ext_vector_type(4)));
typedef float f32x16 __attribute__((ext_vector_type(16)));
typedef unsigned u32x2 __attribute__((ext_vector_type(2)));
typedef unsigned u32x4 __attribute__((ext_vector_type(4)));
typedef __bf16 bf16x2_t __attribute__((ext_vector_type(2)));
#define LAS __attribute__((address_space(3)))
#define DI __device__ __forceinline__

constexpr int SEQ = 2048, DM = 640, NH = 32;
constexpr unsigned WS_QA = 0;
constexpr unsigned WS_QB = WS_QA + NH * SEQ * 40;
constexpr unsigned WS_KB = WS_QB + NH * SEQ * 40;
constexpr unsigned WS_VB = WS_KB + NH * SEQ * 40;
constexpr unsigned WS_KVP = WS_VB + NH * SEQ * 48;
constexpr unsigned WS_OA = WS_KVP + 16 * NH * 3360;
constexpr unsigned WS_OB = WS_OA + SEQ * DM * 2;
constexpr unsigned WS_END = WS_OB + SEQ * DM * 2;

DI unsigned pk2(float lo, float hi) {
  bf16x2_t r = __builtin_convertvector((f32x2){lo, hi}, bf16x2_t);
  return __builtin_bit_cast(unsigned, r);
}
DI float rbf(float x) {
  return __builtin_bit_cast(float, pk2(0.f, x));
}
DI s16x4 trread(const LAS char* p) {
  return __builtin_amdgcn_ds_read_tr16_b64_v4i16((LAS s16x4*)p);
}
DI bf16x8 cat8(s16x4 a, s16x4 b) { return __builtin_shufflevector(a, b, 0, 1, 2, 3, 4, 5, 6, 7); }
DI bf16x8 asfrag(u32x4 v) { return __builtin_bit_cast(bf16x8, v); }
DI __amdgpu_buffer_rsrc_t mkrsrc(const void* p, unsigned bytes) {
  return __builtin_amdgcn_make_buffer_rsrc((void*)p, (short)0, (int)bytes, 0x00020000);
}
DI void dma16(__amdgpu_buffer_rsrc_t rs, unsigned lds, unsigned voff, unsigned soff) {
  asm volatile("s_mov_b32 m0, %0\n\ts_nop 0\n\tbuffer_load_dwordx4 %1, %2, %3 offen lds"
               :: "s"(lds), "v"(voff), "s"(rs), "s"(soff) : "memory");
}
template <unsigned HI>
DI void dma16m(__amdgpu_buffer_rsrc_t rs, unsigned lds, unsigned voff, unsigned soff) {
  asm volatile("s_mov_b32 m0, %0\n\ts_mov_b32 exec_hi, %4\n\tbuffer_load_dwordx4 %1, %2, %3 offen lds\n\ts_mov_b32 exec_hi, -1"
               :: "s"(lds), "v"(voff), "s"(rs), "s"(soff), "i"(HI) : "memory");
}
#define MFMA16(a, b, c) __builtin_amdgcn_mfma_f32_16x16x32_bf16((a), (b), (c), 0, 0, 0)
#define MFMA32(a, b, c) __builtin_amdgcn_mfma_f32_32x32x16_bf16((a), (b), (c), 0, 0, 0)
#define WAIT_BAR(N) asm volatile("s_waitcnt vmcnt(" #N ") lgkmcnt(0)\n\ts_barrier" ::: "memory")
#define LBAR() asm volatile("s_waitcnt lgkmcnt(0)\n\ts_barrier" ::: "memory")

#define STAMP_DECL
#define STAMP(kid, i)
#define STAMP_FLUSH(kid)
constexpr float LOG2E = 1.4426950408889634f;

struct WFrag { bf16x8 t0k0, t0k1, t1k0, t1k1; };
DI WFrag load_wfrag(const LAS char* wl  , int lane) {
  const int hh = lane >> 5, b = (lane >> 4) & 1, q = (lane & 15) >> 2, p = lane & 3;
  const LAS char* a0 = wl + (8 * hh + q) * 40 + (2 * b + (p >> 1)) * 8;
  const LAS char* a1 = wl + (8 * hh + q) * 40 + 32;
  const LAS char* c0 = wl + (16 + q) * 40 + (2 * b + (p >> 1)) * 8;
  const LAS char* c1 = wl + (16 + q) * 40 + 32;
  u32x2 r0 = __builtin_bit_cast(u32x2, trread(a0)), r1 = __builtin_bit_cast(u32x2, trread(a0 + 160));
  u32x2 r2 = __builtin_bit_cast(u32x2, trread(a1)), r3 = __builtin_bit_cast(u32x2, trread(a1 + 160));
  u32x2 r4 = __builtin_bit_cast(u32x2, trread(c0)), r5 = __builtin_bit_cast(u32x2, trread(c1));
  const unsigned sg = (lane & 4) ? 0x80008000u : 0u;
  const unsigned km = hh ? 0u : 0xFFFFFFFFu;
  WFrag w;
  w.t0k0 = asfrag((u32x4){r0[0] ^ sg, r0[1] ^ sg, r1[0] ^ sg, r1[1] ^ sg});
  w.t1k0 = asfrag((u32x4){r2[0] ^ sg, r2[1] ^ sg, r3[0] ^ sg, r3[1] ^ sg});
  w.t0k1 = asfrag((u32x4){(r4[0] ^ sg) & km, (r4[1] ^ sg) & km, 0u, 0u});
  w.t1k1 = asfrag((u32x4){(r5[0] ^ sg) & km, (r5[1] ^ sg) & km, 0u, 0u});
  return w;
}
DI void hedgehog(const WFrag& w, bf16x8 x0, bf16x8 x1, unsigned (&ph)[10]) {
  f32x16 z = {};
  f32x16 p0 = MFMA32(w.t0k0, x0, z);
  f32x16 p1 = MFMA32(w.t1k0, x0, z);
  p0 = MFMA32(w.t0k1, x1, p0);
  p1 = MFMA32(w.t1k1, x1, p1);
  float v[20];
#pragma unroll
  for (int i = 0; i < 16; ++i) v[i] = rbf(p0[i]);
#pragma unroll
  for (int i = 0; i < 4; ++i) v[16 + i] = rbf(p1[i]);
  float m = v[0];
#pragma unroll
  for (int i = 1; i < 20; ++i) m = __builtin_fmaxf(m, v[i]);
  const float nm = -m * LOG2E;
  float s = 0.f;
#pragma unroll
  for (int i = 0; i < 20; ++i) { v[i] = __builtin_amdgcn_exp2f(__builtin_fmaf(v[i], LOG2E, nm)); s += v[i]; }
  const float r = __builtin_amdgcn_rcpf(s);
#pragma unroll
  for (int i = 0; i < 10; ++i) ph[i] = pk2(v[2 * i] * r, v[2 * i + 1] * r);
}

constexpr int K1_STAGE = 16384 + 30720;
constexpr int K1_WPHI = 3 * K1_STAGE + 256;
constexpr int K1_LDS = K1_WPHI + 4096;
constexpr int EP_Q = 0, EP_K = 20480, EP_V = 40960, EP_PHI = 65536 + 256;

__global__ __launch_bounds__(512, 2) void k1_qkv(const __hip_bfloat16* __restrict__ xA, const __hip_bfloat16* __restrict__ wA,
                                                 const __hip_bfloat16* __restrict__ xB, const __hip_bfloat16* __restrict__ wB,
                                                 const __hip_bfloat16* __restrict__ wphi, char* __restrict__ ws) {
  __shared__ __attribute__((aligned(16))) char smem[K1_LDS];
  const LAS char* sm = (const LAS char*)smem;
  LAS char* smw = (LAS char*)smem;
  const unsigned lds0 = (unsigned)(size_t)(LAS char*)smem;
  const int tid = threadIdx.x, lane = tid & 63;
  const int wid = __builtin_amdgcn_readfirstlane(tid >> 6);
  const int xcd = blockIdx.x & 7, idx = blockIdx.x >> 3;
  const int br = xcd >> 2;
  const int mt = ((xcd >> 1) & 1) * 8 + (idx & 7), nt = (xcd & 1) * 4 + (idx >> 3);
  const char* xg = (const char*)(br ? xB : xA) + (size_t)mt * 128 * 1280;
  const char* wg = (const char*)(br ? wB : wA) + nt * 160;
  const __amdgpu_buffer_rsrc_t rx = mkrsrc(xg, 128 * 1280);
  const __amdgpu_buffer_rsrc_t rw = mkrsrc(wg, 640 * 3840 - nt * 160);
  const __amdgpu_buffer_rsrc_t rp = mkrsrc((const char*)wphi + nt * 3200, 3200);

  STAMP_DECL;
  STAMP(0, 0);
  const int xr = lane >> 3;
  const unsigned vx0 = (unsigned)((16 * wid + xr) * 1280 + (((lane & 7) ^ (xr >> 1)) * 16));
  const unsigned vx1 = (unsigned)((16 * wid + 8 + xr) * 1280 + (((lane & 7) ^ (4 + (xr >> 1))) * 16));
  const int wr = lane >= 30 ? 1 : 0, wc = lane - 30 * wr, wsg = (wc >= 10) + (wc >= 20);
  const unsigned vw = (unsigned)(wr * 3840 + wsg * 1280 + (wc - 10 * wsg) * 16);
  auto stage = [&](int kt, int buf) {
    const unsigned lb = lds0 + buf * K1_STAGE;
    dma16(rx, lb + wid * 2048, vx0, kt * 128);
    dma16(rx, lb + wid * 2048 + 1024, vx1, kt * 128);
#pragma unroll
    for (int i = 0; i < 4; ++i) {
      const int pos = 8 * wid + 2 * i;
      const int k = (pos & ~12) | ((pos & 4) << 1) | ((pos & 8) >> 1);
      dma16m<0x0FFFFFFFu>(rw, lb + 16384 + pos * 480, vw, (unsigned)(kt * 64 + k) * 3840u);
    }
  };
  dma16(rp, lds0 + K1_WPHI + (wid & 3) * 1024, (unsigned)((wid & 3) * 1024 + lane * 16), 0);
  stage(0, 0);
  stage(1, 1);

  const int wm = wid >> 2, wn = wid & 3;
  const int g = lane >> 4, fr = lane & 15, q4 = fr >> 2, p4 = lane & 3;
  const int xa0 = (64 * wm + fr) * 128 + ((g ^ (fr >> 1)) * 16);
  const int xa1 = xa0 ^ 64;
  const int wa = 16384 + (16 * (g >> 1) + 4 * (g & 1) + q4) * 480 + 8 * p4 + wn * 128;
  f32x4 acc[4][4] = {};
  int buf = 0;
  for (int kt = 0; kt < 10; ++kt) {
    if (kt < 9) { WAIT_BAR(6); } else { WAIT_BAR(0); }
    STAMP(0, 1 + kt);
    if (kt < 8) stage(kt + 2, buf >= 1 ? buf - 1 : 2);
    const LAS char* sb = sm + buf * K1_STAGE;
#pragma unroll
    for (int kk = 0; kk < 2; ++kk) {
      bf16x8 xf[4], wf[4];
#pragma unroll
      for (int i = 0; i < 4; ++i) xf[i] = *(const LAS bf16x8*)(sb + (kk ? xa1 : xa0) + i * 2048);
#pragma unroll
      for (int j = 0; j < 4; ++j)
        wf[j] = cat8(trread(sb + wa + kk * 32 * 480 + j * 32), trread(sb + wa + (kk * 32 + 8) * 480 + j * 32));
#pragma unroll
      for (int i = 0; i < 4; ++i)
#pragma unroll
        for (int j = 0; j < 4; ++j) acc[i][j] = MFMA16(wf[j], xf[i], acc[i][j]);
    }
    buf = buf == 2 ? 0 : buf + 1;
  }
  LBAR();
  STAMP(0, 11);
#pragma unroll
  for (int j = 0; j < 4; ++j) {
    const int qd = 16 * wn + 4 * j + g;
    const int seg = (qd >= 20) + (qd >= 40), r20 = qd - 20 * seg;
    const int hl = (r20 >= 5) + (r20 >= 10) + (r20 >= 15), c = r20 - 5 * hl;
    const int rs = seg == 2 ? 48 : 40;
    const int base = seg * 20480 + hl * 128 * rs + (64 * wm + fr) * rs + c * 8;
    if (qd < 60) {
#pragma unroll
      for (int i = 0; i < 4; ++i)
        *(LAS u32x2*)(smw + base + i * 16 * rs) = (u32x2){pk2(acc[i][j][0], acc[i][j][1]), pk2(acc[i][j][2], acc[i][j][3])};
    }
  }
  *(LAS u32x2*)(smw + EP_V + tid * 48 + 40) = (u32x2){0x3F80u, 0u};
  LBAR();
  STAMP(0, 12);
  {
    const unsigned rowoff = (unsigned)(4 * nt * SEQ + mt * 128);
    char* qg = ws + (br ? WS_QB : WS_QA) + rowoff * 40u;
#pragma unroll
    for (int i = 0; i < 3; ++i) {
      const int c = tid + 512 * i;
      if (c < 1280) {
        const int hl = c / 320, w = c - hl * 320;
        *(u32x4*)(qg + hl * (SEQ * 40) + w * 16) = *(const LAS u32x4*)(sm + EP_Q + c * 16);
      }
    }
    if (br) {
      char* kg = ws + WS_KB + rowoff * 40u;
      char* vg = ws + WS_VB + rowoff * 48u;
#pragma unroll
      for (int i = 0; i < 3; ++i) {
        const int c = tid + 512 * i;
        if (c < 1280) {
          const int hl = c / 320, w = c - hl * 320;
          *(u32x4*)(kg + hl * (SEQ * 40) + w * 16) = *(const LAS u32x4*)(sm + EP_K + c * 16);
        }
      }
#pragma unroll
      for (int i = 0; i < 3; ++i) {
        const int c = tid + 512 * i;
        const int hl = c / 384, w = c - hl * 384;
        *(u32x4*)(vg + hl * (SEQ * 48) + w * 16) = *(const LAS u32x4*)(sm + EP_V + c * 16);
      }
      STAMP(0, 13);
      STAMP_FLUSH(0);
      return;
    }
  }
  STAMP(0, 13);
  const int hl = wid >> 1, hh = lane >> 5, r32 = lane & 31;
  {
    const WFrag wf = load_wfrag(sm + K1_WPHI + hl * 800, lane);
#pragma unroll
    for (int t = 0; t < 2; ++t) {
      const int row = 64 * (wid & 1) + 32 * t + r32;
      const LAS char* kp = sm + EP_K + hl * 5120 + row * 40;
      const u32x2 a = *(const LAS u32x2*)(kp + 16 * hh), b = *(const LAS u32x2*)(kp + 16 * hh + 8), c = *(const LAS u32x2*)(kp + 32);
      unsigned ph[10];
      hedgehog(wf, asfrag((u32x4){a[0], a[1], b[0], b[1]}), asfrag((u32x4){c[0], c[1], c[0], c[1]}), ph);
      LAS char* pp = smw + EP_PHI + (hl * 128 + row) * 88 + hh * 40;
#pragma unroll
      for (int i = 0; i < 5; ++i) *(LAS u32x2*)(pp + 8 * i) = (u32x2){ph[2 * i], ph[2 * i + 1]};
    }
  }
  LBAR();
  STAMP(0, 14);
  {
    const int ft = wid & 1, b = (lane >> 4) & 1;
    const LAS char* pa = sm + EP_PHI + (hl * 128 + 8 * hh + q4) * 88 + (32 * ft + 16 * b + 4 * p4) * 2;
    const LAS char* va = sm + EP_V + (hl * 128 + 8 * hh + q4) * 48 + (16 * b + 4 * p4) * 2;
    f32x16 d = {};
#pragma unroll
    for (int ks = 0; ks < 8; ++ks) {
      const bf16x8 af = cat8(trread(pa + ks * 16 * 88), trread(pa + (ks * 16 + 4) * 88));
      const bf16x8 bf = cat8(trread(va + ks * 16 * 48), trread(va + (ks * 16 + 4) * 48));
      d = MFMA32(af, bf, d);
    }
    if (r32 <= 20) {
      float* o = (float*)(ws + WS_KVP) + ((mt * NH + 4 * nt + hl) * 21 + r32) * 40 + 32 * ft + 4 * hh;
      *(f32x4*)o = (f32x4){d[0], d[1], d[2], d[3]};
      if (ft == 0) {
        *(f32x4*)(o + 8) = (f32x4){d[4], d[5], d[6], d[7]};
        *(f32x4*)(o + 16) = (f32x4){d[8], d[9], d[10], d[11]};
        *(f32x4*)(o + 24) = (f32x4){d[12], d[13], d[14], d[15]};
      }
    }
  }
  STAMP(0, 15);
  STAMP_FLUSH(0);
}

constexpr int K2_K = 0, K2_V = 20480, K2_KV = K2_V + 24576 + 64, K2_W = K2_KV + 32 * 112, K2_LDS = K2_W + 1024;
constexpr float SC2 = 0.22360679774997896f * 1.4426950408889634f;

DI void store_o(char* og, int h, int srow, int hh, const float (&o)[12]) {
  const int p = h >> 1, odd = h & 1;
  char* b = og + (unsigned)(((srow >> 4) * 20 + 5 * (p >> 2)) * 1024 + ((p & 3) * 16 + (srow & 15)) * 16);
  *(u32x4*)(b + (3 * odd + hh) * 1024) = (u32x4){pk2(o[0], o[1]), pk2(o[2], o[3]), pk2(o[4], o[5]), pk2(o[6], o[7])};
  if (hh == 0) *(u32x2*)(b + 2048 + 8 * odd) = (u32x2){pk2(o[8], o[9]), pk2(o[10], o[11])};
}

__global__ __launch_bounds__(512, 2) void k2_attn(const __hip_bfloat16* __restrict__ wphi, char* __restrict__ ws) {
  __shared__ __attribute__((aligned(16))) char smem[K2_LDS];
  const LAS char* sm = (const LAS char*)smem;
  LAS char* smw = (LAS char*)smem;
  const unsigned lds0 = (unsigned)(size_t)(LAS char*)smem;
  const int tid = threadIdx.x, lane = tid & 63;
  const int wid = __builtin_amdgcn_readfirstlane(tid >> 6);
  const int h = (blockIdx.x & 7) * 4 + ((blockIdx.x >> 3) & 3), rb = blockIdx.x >> 5;
  const int q0 = rb * 256;
  const int hh = lane >> 5, r32 = lane & 31, b16 = (lane >> 4) & 1, q4 = (lane & 15) >> 2, p4 = lane & 3;
  STAMP_DECL;
  STAMP(1, 0);
  {
    const __amdgpu_buffer_rsrc_t rk = mkrsrc(ws + WS_KB + h * (SEQ * 40), SEQ * 40);
    const __amdgpu_buffer_rsrc_t rv = mkrsrc(ws + WS_VB + h * (SEQ * 48), SEQ * 48);
    const __amdgpu_buffer_rsrc_t rp = mkrsrc((const char*)wphi + h * 800, 800);
    const unsigned vo = lane * 16;
#pragma unroll
    for (int i = 0; i < 3; ++i) {
      const int pc = wid + 8 * i;
      if (pc < 20 && (rb > 0 || pc >= 10)) dma16(rk, lds0 + K2_K + pc * 1024, vo, (unsigned)((q0 - 256) * 40 + pc * 1024));
    }
#pragma unroll
    for (int i = 0; i < 3; ++i) {
      const int pc = wid + 8 * i;
      if (rb > 0 || pc >= 12) dma16(rv, lds0 + K2_V + pc * 1024, vo, (unsigned)((q0 - 256) * 48 + pc * 1024));
    }
    if (wid == 7) dma16(rp, lds0 + K2_W, vo, 0);
    if (rb == 0) {
      *(LAS u32x4*)(smw + K2_K + tid * 16) = (u32x4){0u, 0u, 0u, 0u};
      if (tid < 128) *(LAS u32x4*)(smw + K2_K + 8192 + tid * 16) = (u32x4){0u, 0u, 0u, 0u};
      *(LAS u32x4*)(smw + K2_V + tid * 16) = (u32x4){0u, 0u, 0u, 0u};
      if (tid < 256) *(LAS u32x4*)(smw + K2_V + 8192 + tid * 16) = (u32x4){0u, 0u, 0u, 0u};
    }
  }
  const int srow = q0 + 32 * wid + r32;
  const unsigned qoff = (unsigned)((h * SEQ + srow) * 40);
  const u32x2 qa0 = *(const u32x2*)(ws + WS_QA + qoff + 16 * hh), qa1 = *(const u32x2*)(ws + WS_QA + qoff + 16 * hh + 8);
  const u32x2 qa2 = *(const u32x2*)(ws + WS_QA + qoff + 32);
  const u32x2 qb0 = *(const u32x2*)(ws + WS_QB + qoff + 16 * hh), qb1 = *(const u32x2*)(ws + WS_QB + qoff + 16 * hh + 8);
  const u32x2 qb2 = *(const u32x2*)(ws + WS_QB + qoff + 32);
  if (tid < 420) {
    const int f4 = tid >> 1, hf = tid & 1;
    const f32x4* src = (const f32x4*)(ws + WS_KVP) + (hf * 8 * NH + h) * 210 + f4;
    f32x4 s = src[0];
#pragma unroll
    for (int i = 1; i < 8; ++i) s += src[i * NH * 210];
#pragma unroll
    for (int i = 0; i < 4; ++i) s[i] += __shfl_xor(s[i], 1);
    const int d = f4 / 10, fq = f4 - d * 10;
    const int off = (fq >= 5 ? 48 + (fq - 5) * 8 : fq * 8);
    const u32x2 v = (u32x2){pk2(s[0], s[1]), pk2(s[2], s[3])};
    if (hf == 0) *(LAS u32x2*)(smw + K2_KV + d * 112 + off) = v;
    else if (d == 20) *(LAS u32x2*)(smw + K2_KV + 24 * 112 + off) = v;
  } else if (tid >= 448) {
    *(LAS u32x2*)(smw + K2_KV + ((tid - 448) >> 1) * 112 + (tid & 1) * 48 + 40) = (u32x2){0u, 0u};
  }
  STAMP(1, 1);
  WAIT_BAR(0);
  STAMP(1, 2);
  {
    const WFrag wf = load_wfrag(sm + K2_W, lane);
    unsigned ph[10];
    hedgehog(wf, asfrag((u32x4){qa0[0], qa0[1], qa1[0], qa1[1]}), asfrag((u32x4){qa2[0], qa2[1], qa2[0], qa2[1]}), ph);
    const LAS char* kp = sm + K2_KV + r32 * 112 + hh * 48;
    f32x16 n = {};
    n = MFMA32(*(const LAS bf16x8*)(kp), asfrag((u32x4){ph[0], ph[1], ph[2], ph[3]}), n);
    n = MFMA32(*(const LAS bf16x8*)(kp + 16), asfrag((u32x4){ph[4], ph[5], ph[6], ph[7]}), n);
    n = MFMA32(*(const LAS bf16x8*)(kp + 32), asfrag((u32x4){ph[8], ph[9], 0u, 0u}), n);
    const float den = rbf(rbf(hh ? n[8] : n[12]) + 1e-6f);
    const float r = 1.0f / den;
    float o[12];
#pragma unroll
    for (int i = 0; i < 12; ++i) o[i] = rbf(n[i]) * r;
    store_o(ws + WS_OA, h, srow, hh, o);
  }
  STAMP(1, 3);
  {
    const unsigned km = hh ? 0u : 0xFFFFFFFFu;
    const bf16x8 qf0 = asfrag((u32x4){qb0[0], qb0[1], qb1[0], qb1[1]});
    const bf16x8 qf1 = asfrag((u32x4){qb2[0] & km, qb2[1] & km, 0u, 0u});
    const LAS char* kp = sm + K2_K + (32 * wid + r32) * 40;
    f32x16 s[9];
#pragma unroll
    for (int t = 0; t < 9; ++t) {
      const u32x2 a = *(const LAS u32x2*)(kp + t * 1280 + 16 * hh), b = *(const LAS u32x2*)(kp + t * 1280 + 16 * hh + 8);
      const u32x2 c = *(const LAS u32x2*)(kp + t * 1280 + 32);
      f32x16 z = {};
      s[t] = MFMA32(asfrag((u32x4){a[0], a[1], b[0], b[1]}), qf0, z);
      s[t] = MFMA32(asfrag((u32x4){c[0], c[1], c[0], c[1]}), qf1, s[t]);
    }
    if (rb == 0) {
#pragma unroll
      for (int t = 0; t < 8; ++t)
        if (wid + t < 8) {
#pragma unroll
          for (int r = 0; r < 16; ++r) s[t][r] = -INFINITY;
        }
    }
#pragma unroll
    for (int r = 0; r < 16; ++r) {
      const int j = (r & 3) + 8 * (r >> 2) + 4 * hh;
      s[8][r] = (j <= r32) ? s[8][r] : s[0][r];
    }
    float m = s[8][0];
#pragma unroll
    for (int t = 1; t < 9; ++t)
#pragma unroll
      for (int r = 0; r < 16; ++r) m = __builtin_fmaxf(m, s[t][r]);
    {
      auto rr = __builtin_amdgcn_permlane32_swap(__float_as_uint(m), __float_as_uint(m), false, false);
      m = __builtin_fmaxf(__uint_as_float(rr[0]), __uint_as_float(rr[1]));
    }
    const float nm = -m * SC2;
    STAMP(1, 4);
    const LAS char* vp = sm + K2_V + (32 * wid + 4 * hh + q4) * 48 + (16 * b16 + 4 * p4) * 2;
    f32x16 o = {};
#pragma unroll
    for (int t = 1; t < 9; ++t) {
      unsigned pw[8];
#pragma unroll
      for (int i = 0; i < 8; ++i)
        pw[i] = pk2(__builtin_amdgcn_exp2f(__builtin_fmaf(s[t][2 * i], SC2, nm)), __builtin_amdgcn_exp2f(__builtin_fmaf(s[t][2 * i + 1], SC2, nm)));
      if (t == 8) {
        unsigned pl[8];
#pragma unroll
        for (int i = 0; i < 8; ++i) {
          const int j = ((2 * i) & 3) + 8 * ((2 * i) >> 2) + 4 * hh;
          const unsigned mk = (j <= r32 ? 0xFFFFu : 0u) | (j + 1 <= r32 ? 0xFFFF0000u : 0u);
          pl[i] = pw[i] & ~mk;
          pw[i] &= mk;
        }
        o = MFMA32(cat8(trread(vp), trread(vp + 8 * 48)), asfrag((u32x4){pl[0], pl[1], pl[2], pl[3]}), o);
        o = MFMA32(cat8(trread(vp + 16 * 48), trread(vp + 24 * 48)), asfrag((u32x4){pl[4], pl[5], pl[6], pl[7]}), o);
      }
      o = MFMA32(cat8(trread(vp + t * 1536), trread(vp + t * 1536 + 8 * 48)), asfrag((u32x4){pw[0], pw[1], pw[2], pw[3]}), o);
      o = MFMA32(cat8(trread(vp + t * 1536 + 16 * 48), trread(vp + t * 1536 + 24 * 48)), asfrag((u32x4){pw[4], pw[5], pw[6], pw[7]}), o);
    }
    auto rr = __builtin_amdgcn_permlane32_swap(__float_as_uint(o[8]), __float_as_uint(o[8]), false, false);
    const float r = __builtin_amdgcn_rcpf(__uint_as_float(rr[1]));
    float of[12];
#pragma unroll
    for (int i = 0; i < 12; ++i) of[i] = o[i] * r;
    store_o(ws + WS_OB, h, srow, hh, of);
  }
  STAMP(1, 5);
  STAMP_FLUSH(1);
}

constexpr int K3_W = 0, K3_X = 20 * 5120 + 256, K3_LDS = K3_X + 8 * 5 * 1024;

__global__ __launch_bounds__(512, 2) void k3_out(const __hip_bfloat16* __restrict__ woA, const __hip_bfloat16* __restrict__ woB,
                                                 const char* __restrict__ ws, __hip_bfloat16* __restrict__ out) {
  __shared__ __attribute__((aligned(16))) char smem[K3_LDS];
  const LAS char* sm = (const LAS char*)smem;
  LAS char* smw = (LAS char*)smem;
  const unsigned lds0 = (unsigned)(size_t)(LAS char*)smem;
  const int tid = threadIdx.x, lane = tid & 63;
  const int wid = __builtin_amdgcn_readfirstlane(tid >> 6);
  const int xcd = blockIdx.x & 7, idx = blockIdx.x >> 3;
  const int br = xcd >> 2;
  const int mt = (xcd & 3) * 4 + (idx & 3), nt = idx >> 2;
  const int wm = wid & 3, kh = wid >> 2;
  const __amdgpu_buffer_rsrc_t rw = mkrsrc((const char*)(br ? woB : woA) + nt * 160, 640 * 1280 - nt * 160);
  const __amdgpu_buffer_rsrc_t ro = mkrsrc(ws + (br ? WS_OB : WS_OA) + (unsigned)((mt * 8 + 2 * wm) * 20 + 10 * kh) * 1024u, 30 * 1024);
  STAMP_DECL;
  STAMP(2, 0);
  const int lq = (lane >= 10) + (lane >= 20) + (lane >= 30);
  const unsigned vw = (unsigned)(lq * 1280 + (lane - 10 * lq) * 16);
  const int sg = 2 * (wid >> 2) + (wid & 1), st = (wid >> 1) & 1;
  const unsigned vo = lane * 16;
  u32x4 xf[10][2];
#pragma unroll
  for (int tau = 0; tau < 10; ++tau) {
#pragma unroll
    for (int k2 = 0; k2 < 2; ++k2) {
      const int ks = 10 * k2 + tau, w5 = ks % 5;
      const int o0 = w5 == 0 ? 0 : w5 == 1 ? 4 : w5 == 2 ? 16 : w5 == 3 ? 20 : 24;
      const int row = 160 * (ks / 5) + o0 + 40 * sg + st * (w5 == 2 ? 20 : 8);
      dma16m<0xFFu>(rw, lds0 + K3_W + ks * 5120 + wid * 640, vw, (unsigned)row * 1280u);
    }
    asm volatile("buffer_load_dwordx4 %0, %1, %2, 0 offen offset:%3" : "=v"(xf[tau][0]) : "v"(vo + (tau >> 2) * 4096), "s"(ro), "i"((tau & 3) * 1024) : "memory");
    asm volatile("buffer_load_dwordx4 %0, %1, %2, 0 offen offset:%3" : "=v"(xf[tau][1]) : "v"(vo + 20480 + (tau >> 2) * 4096), "s"(ro), "i"((tau & 3) * 1024) : "memory");
  }
  const int g = lane >> 4, fr = lane & 15, q4 = fr >> 2, p4 = lane & 3;
  const LAS char* wa = sm + K3_W + kh * 51200 + (16 * (g >> 1) + 4 * (g & 1) + q4) * 160 + 8 * p4;
  f32x4 acc[2][5] = {};
  STAMP(2, 1);
#define K3_PHASE(PH, N)                                                                                              \
  asm volatile("s_waitcnt vmcnt(" #N ")\n\ts_barrier"                                                                \
               : "+v"(xf[2 * PH][0]), "+v"(xf[2 * PH][1]), "+v"(xf[2 * PH + 1][0]), "+v"(xf[2 * PH + 1][1])::"memory"); \
  _Pragma("unroll") for (int tt = 0; tt < 2; ++tt) {                                                                 \
    const int tau = 2 * PH + tt;                                                                                     \
    _Pragma("unroll") for (int j = 0; j < 5; ++j) {                                                                  \
      const bf16x8 wf = cat8(trread(wa + tau * 5120 + j * 32), trread(wa + tau * 5120 + 8 * 160 + j * 32));          \
      acc[0][j] = MFMA16(wf, asfrag(xf[tau][0]), acc[0][j]);                                                         \
      acc[1][j] = MFMA16(wf, asfrag(xf[tau][1]), acc[1][j]);                                                         \
    }                                                                                                                \
  }
  K3_PHASE(0, 32)
  K3_PHASE(1, 24)
  K3_PHASE(2, 16)
  K3_PHASE(3, 8)
  K3_PHASE(4, 0)
  STAMP(2, 2);
#undef K3_PHASE
#pragma unroll
  for (int j = 0; j < 5; ++j) *(LAS f32x4*)(smw + K3_X + (wid * 5 + j) * 1024 + lane * 16) = kh ? acc[0][j] : acc[1][j];
  LBAR();
  char* og = (char*)out + (size_t)(mt * 128 + 32 * wm + 16 * kh + fr) * 2560 + br * 1280 + nt * 160 + g * 8;
#pragma unroll
  for (int j = 0; j < 5; ++j) {
    const f32x4 o = *(const LAS f32x4*)(sm + K3_X + ((wid ^ 4) * 5 + j) * 1024 + lane * 16);
    const f32x4 a = kh ? acc[1][j] : acc[0][j];
    *(u32x2*)(og + j * 32) = (u32x2){pk2(a[0] + o[0], a[1] + o[1]), pk2(a[2] + o[2], a[3] + o[3])};
  }
  STAMP(2, 3);
  STAMP_FLUSH(2);
}

extern "C" void kernel_launch(void* const* d_in, const int* in_sizes, int n_in, void* d_out, int out_size, void* d_ws,
                              size_t ws_size, hipStream_t stream) {
  const __hip_bfloat16* Ax = (const __hip_bfloat16*)d_in[0];
  const __hip_bfloat16* Aw = (const __hip_bfloat16*)d_in[1];
  const __hip_bfloat16* Aphi = (const __hip_bfloat16*)d_in[2];
  const __hip_bfloat16* Awo = (const __hip_bfloat16*)d_in[3];
  const __hip_bfloat16* Bx = (const __hip_bfloat16*)d_in[4];
  const __hip_bfloat16* Bw = (const __hip_bfloat16*)d_in[5];
  const __hip_bfloat16* Bwo = (const __hip_bfloat16*)d_in[6];
  char* ws = (char*)d_ws;
  k1_qkv<<<256, 512, 0, stream>>>(Ax, Aw, Bx, Bw, Aphi, ws);
  k2_attn<<<256, 512, 0, stream>>>(Aphi, ws);
  k3_out<<<256, 512, 0, stream>>>(Awo, Bwo, ws, (__hip_bfloat16*)d_out);
}
```

```cpp
#include <hip/hip_runtime.h>
#include <hip/hip_bf16.h>

typedef short bf16x8 __attribute__((ext_vector_type(8)));
typedef short s16x4 __attribute__((ext_vector_type(4)));
typedef float f32x2 __attribute__((ext_vector_type(2)));
typedef float f32x4 __attribute__((ext_vector_type(4)));
typedef float f32x16 __attribute__((ext_vector_type(16)));
typedef unsigned u32x2 __attribute__((ext_vector_type(2)));
typedef unsigned u32x4 __attribute__((ext_vector_type(4)));
typedef __bf16 bf16x2_t __attribute__((ext_vector_type(2)));
#define LAS __attribute__((address_space(3)))
#define DI __device__ __forceinline__

constexpr int SEQ = 2048, DM = 640, NH = 32;
constexpr unsigned WS_QA = 0;
constexpr unsigned WS_QB = WS_QA + NH * SEQ * 40;
constexpr unsigned WS_KB = WS_QB + NH * SEQ * 40;
constexpr unsigned WS_VB = WS_KB + NH * SEQ * 40;
constexpr unsigned WS_KVP = WS_VB + NH * SEQ * 48;
constexpr unsigned WS_OA = WS_KVP + 16 * NH * 3360;
constexpr unsigned WS_OB = WS_OA + SEQ * DM * 2;
constexpr unsigned WS_END = WS_OB + SEQ * DM * 2;

DI unsigned pk2(float lo, float hi) {
  bf16x2_t r = __builtin_convertvector((f32x2){lo, hi}, bf16x2_t);
  return __builtin_bit_cast(unsigned, r);
}
DI float rbf(float x) {
  return __builtin_bit_cast(float, pk2(0.f, x));
}
DI s16x4 trread(const LAS char* p) {
  return __builtin_amdgcn_ds_read_tr16_b64_v4i16((LAS s16x4*)p);
}
DI bf16x8 cat8(s16x4 a, s16x4 b) { return __builtin_shufflevector(a, b, 0, 1, 2, 3, 4, 5, 6, 7); }
DI bf16x8 asfrag(u32x4 v) { return __builtin_bit_cast(bf16x8, v); }
DI __amdgpu_buffer_rsrc_t mkrsrc(const void* p, unsigned bytes) {
  return __builtin_amdgcn_make_buffer_rsrc((void*)p, (short)0, (int)bytes, 0x00020000);
}
DI void dma16(__amdgpu_buffer_rsrc_t rs, unsigned lds, unsigned voff, unsigned soff) {
  asm volatile("s_mov_b32 m0, %0\n\ts_nop 0\n\tbuffer_load_dwordx4 %1, %2, %3 offen lds"
               :: "s"(lds), "v"(voff), "s"(rs), "s"(soff) : "memory");
}
template <unsigned HI>
DI void dma16m(__amdgpu_buffer_rsrc_t rs, unsigned lds, unsigned voff, unsigned soff) {
  asm volatile("s_mov_b32 m0, %0\n\ts_mov_b32 exec_hi, %4\n\tbuffer_load_dwordx4 %1, %2, %3 offen lds\n\ts_mov_b32 exec_hi, -1"
               :: "s"(lds), "v"(voff), "s"(rs), "s"(soff), "i"(HI) : "memory");
}
#define MFMA16(a, b, c) __builtin_amdgcn_mfma_f32_16x16x32_bf16((a), (b), (c), 0, 0, 0)
#define MFMA32(a, b, c) __builtin_amdgcn_mfma_f32_32x32x16_bf16((a), (b), (c), 0, 0, 0)
#define WAIT_BAR(N) asm volatile("s_waitcnt vmcnt(" #N ") lgkmcnt(0)\n\ts_barrier" ::: "memory")
#define LBAR() asm volatile("s_waitcnt lgkmcnt(0)\n\ts_barrier" ::: "memory")

#define STAMP_DECL
#define STAMP(kid, i)
#define STAMP_FLUSH(kid)
constexpr float LOG2E = 1.4426950408889634f;

struct WFrag { bf16x8 t0k0, t0k1, t1k0, t1k1; };
DI WFrag load_wfrag(const LAS char* wl  , int lane) {
  const int hh = lane >> 5, b = (lane >> 4) & 1, q = (lane & 15) >> 2, p = lane & 3;
  const LAS char* a0 = wl + (8 * hh + q) * 40 + (2 * b + (p >> 1)) * 8;
  const LAS char* a1 = wl + (8 * hh + q) * 40 + 32;
  const LAS char* c0 = wl + (16 + q) * 40 + (2 * b + (p >> 1)) * 8;
  const LAS char* c1 = wl + (16 + q) * 40 + 32;
  u32x2 r0 = __builtin_bit_cast(u32x2, trread(a0)), r1 = __builtin_bit_cast(u32x2, trread(a0 + 160));
  u32x2 r2 = __builtin_bit_cast(u32x2, trread(a1)), r3 = __builtin_bit_cast(u32x2, trread(a1 + 160));
  u32x2 r4 = __builtin_bit_cast(u32x2, trread(c0)), r5 = __builtin_bit_cast(u32x2, trread(c1));
  const unsigned sg = (lane & 4) ? 0x80008000u : 0u;
  const unsigned km = hh ? 0u : 0xFFFFFFFFu;
  WFrag w;
  w.t0k0 = asfrag((u32x4){r0[0] ^ sg, r0[1] ^ sg, r1[0] ^ sg, r1[1] ^ sg});
  w.t1k0 = asfrag((u32x4){r2[0] ^ sg, r2[1] ^ sg, r3[0] ^ sg, r3[1] ^ sg});
  w.t0k1 = asfrag((u32x4){(r4[0] ^ sg) & km, (r4[1] ^ sg) & km, 0u, 0u});
  w.t1k1 = asfrag((u32x4){(r5[0] ^ sg) & km, (r5[1] ^ sg) & km, 0u, 0u});
  return w;
}
DI void hedgehog(const WFrag& w, bf16x8 x0, bf16x8 x1, unsigned (&ph)[10]) {
  f32x16 z = {};
  f32x16 p0 = MFMA32(w.t0k0, x0, z);
  f32x16 p1 = MFMA32(w.t1k0, x0, z);
  p0 = MFMA32(w.t0k1, x1, p0);
  p1 = MFMA32(w.t1k1, x1, p1);
  float v[20];
#pragma unroll
  for (int i = 0; i < 16; ++i) v[i] = rbf(p0[i]);
#pragma unroll
  for (int i = 0; i < 4; ++i) v[16 + i] = rbf(p1[i]);
  float m = v[0];
#pragma unroll
  for (int i = 1; i < 20; ++i) m = __builtin_fmaxf(m, v[i]);
  const float nm = -m * LOG2E;
  float s = 0.f;
#pragma unroll
  for (int i = 0; i < 20; ++i) { v[i] = __builtin_amdgcn_exp2f(__builtin_fmaf(v[i], LOG2E, nm)); s += v[i]; }
  const float r = __builtin_amdgcn_rcpf(s);
#pragma unroll
  for (int i = 0; i < 10; ++i) ph[i] = pk2(v[2 * i] * r, v[2 * i + 1] * r);
}

constexpr int K1_STAGE = 16384 + 30720;
constexpr int K1_WPHI = 3 * K1_STAGE + 256;
constexpr int K1_LDS = K1_WPHI + 4096;
constexpr int EP_Q = 0, EP_K = 20480, EP_V = 40960, EP_PHI = 65536 + 256;

__global__ __launch_bounds__(512, 2) void k1_qkv(const __hip_bfloat16* __restrict__ xA, const __hip_bfloat16* __restrict__ wA,
                                                 const __hip_bfloat16* __restrict__ xB, const __hip_bfloat16* __restrict__ wB,
                                                 const __hip_bfloat16* __restrict__ wphi, char* __restrict__ ws) {
  __shared__ __attribute__((aligned(16))) char smem[K1_LDS];
  const LAS char* sm = (const LAS char*)smem;
  LAS char* smw = (LAS char*)smem;
  const unsigned lds0 = (unsigned)(size_t)(LAS char*)smem;
  const int tid = threadIdx.x, lane = tid & 63;
  const int wid = __builtin_amdgcn_readfirstlane(tid >> 6);
  const int xcd = blockIdx.x & 7, idx = blockIdx.x >> 3;
  const int br = xcd >> 2;
  const int mt = ((xcd >> 1) & 1) * 8 + (idx & 7), nt = (xcd & 1) * 4 + (idx >> 3);
  const char* xg = (const char*)(br ? xB : xA) + (size_t)mt * 128 * 1280;
  const char* wg = (const char*)(br ? wB : wA) + nt * 160;
  const __amdgpu_buffer_rsrc_t rx = mkrsrc(xg, 128 * 1280);
  const __amdgpu_buffer_rsrc_t rw = mkrsrc(wg, 640 * 3840 - nt * 160);
  const __amdgpu_buffer_rsrc_t rp = mkrsrc((const char*)wphi + nt * 3200, 3200);

  STAMP_DECL;
  STAMP(0, 0);
  const int xr = lane >> 3;
  const unsigned vx0 = (unsigned)((16 * wid + xr) * 1280 + (((lane & 7) ^ (xr >> 1)) * 16));
  const unsigned vx1 = (unsigned)((16 * wid + 8 + xr) * 1280 + (((lane & 7) ^ (4 + (xr >> 1))) * 16));
  const int wr = lane >= 30 ? 1 : 0, wc = lane - 30 * wr, wsg = (wc >= 10) + (wc >= 20);
  const unsigned vw = (unsigned)(wr * 3840 + wsg * 1280 + (wc - 10 * wsg) * 16);
  auto stage = [&](int kt, int buf) {
    const unsigned lb = lds0 + buf * K1_STAGE;
    dma16(rx, lb + wid * 2048, vx0, kt * 128);
    dma16(rx, lb + wid * 2048 + 1024, vx1, kt * 128);
#pragma unroll
    for (int i = 0; i < 4; ++i) {
      const int pos = 8 * wid + 2 * i;
      const int k = (pos & ~12) | ((pos & 4) << 1) | ((pos & 8) >> 1);
      dma16m<0x0FFFFFFFu>(rw, lb + 16384 + pos * 480, vw, (unsigned)(kt * 64 + k) * 3840u);
    }
  };
  dma16(rp, lds0 + K1_WPHI + (wid & 3) * 1024, (unsigned)((wid & 3) * 1024 + lane * 16), 0);
  stage(0, 0);
  stage(1, 1);

  const int wm = wid >> 2, wn = wid & 3;
  const int g = lane >> 4, fr = lane & 15, q4 = fr >> 2, p4 = lane & 3;
  const int xa0 = (64 * wm + fr) * 128 + ((g ^ (fr >> 1)) * 16);
  const int xa1 = xa0 ^ 64;
  const int wa = 16384 + (16 * (g >> 1) + 4 * (g & 1) + q4) * 480 + 8 * p4 + wn * 128;
  f32x4 acc[4][4] = {};
  int buf = 0;
  for (int kt = 0; kt < 10; ++kt) {
    if (kt < 9) { WAIT_BAR(6); } else { WAIT_BAR(0); }
    STAMP(0, 1 + kt);
    if (kt < 8) stage(kt + 2, buf >= 1 ? buf - 1 : 2);
    const LAS char* sb = sm + buf * K1_STAGE;
#pragma unroll
    for (int kk = 0; kk < 2; ++kk) {
      bf16x8 xf[4], wf[4];
#pragma unroll
      for (int i = 0; i < 4; ++i) xf[i] = *(const LAS bf16x8*)(sb + (kk ? xa1 : xa0) + i * 2048);
#pragma unroll
      for (int j = 0; j < 4; ++j)
        wf[j] = cat8(trread(sb + wa + kk * 32 * 480 + j * 32), trread(sb + wa + (kk * 32 + 8) * 480 + j * 32));
#pragma unroll
      for (int i = 0; i < 4; ++i)
#pragma unroll
        for (int j = 0; j < 4; ++j) acc[i][j] = MFMA16(wf[j], xf[i], acc[i][j]);
    }
    buf = buf == 2 ? 0 : buf + 1;
  }
  LBAR();
  STAMP(0, 11);
#pragma unroll
  for (int j = 0; j < 4; ++j) {
    const int qd = 16 * wn + 4 * j + g;
    const int seg = (qd >= 20) + (qd >= 40), r20 = qd - 20 * seg;
    const int hl = (r20 >= 5) + (r20 >= 10) + (r20 >= 15), c = r20 - 5 * hl;
    const int rs = seg == 2 ? 48 : 40;
    const int base = seg * 20480 + hl * 128 * rs + (64 * wm + fr) * rs + c * 8;
    if (qd < 60) {
#pragma unroll
      for (int i = 0; i < 4; ++i)
        *(LAS u32x2*)(smw + base + i * 16 * rs) = (u32x2){pk2(acc[i][j][0], acc[i][j][1]), pk2(acc[i][j][2], acc[i][j][3])};
    }
  }
  *(LAS u32x2*)(smw + EP_V + tid * 48 + 40) = (u32x2){0x3F80u, 0u};
  LBAR();
  STAMP(0, 12);
  {
    const unsigned rowoff = (unsigned)(4 * nt * SEQ + mt * 128);
    char* qg = ws + (br ? WS_QB : WS_QA) + rowoff * 40u;
#pragma unroll
    for (int i = 0; i < 3; ++i) {
      const int c = tid + 512 * i;
      if (c < 1280) {
        const int hl = c / 320, w = c - hl * 320;
        *(u32x4*)(qg + hl * (SEQ * 40) + w * 16) = *(const LAS u32x4*)(sm + EP_Q + c * 16);
      }
    }
    if (br) {
      char* kg = ws + WS_KB + rowoff * 40u;
      char* vg = ws + WS_VB + rowoff * 48u;
#pragma unroll
      for (int i = 0; i < 3; ++i) {
        const int c = tid + 512 * i;
        if (c < 1280) {
          const int hl = c / 320, w = c - hl * 320;
          *(u32x4*)(kg + hl * (SEQ * 40) + w * 16) = *(const LAS u32x4*)(sm + EP_K + c * 16);
        }
      }
#pragma unroll
      for (int i = 0; i < 3; ++i) {
        const int c = tid + 512 * i;
        const int hl = c / 384, w = c - hl * 384;
        *(u32x4*)(vg + hl * (SEQ * 48) + w * 16) = *(const LAS u32x4*)(sm + EP_V + c * 16);
      }
      STAMP(0, 13);
      STAMP_FLUSH(0);
      return;
    }
  }
  STAMP(0, 13);
  const int hl = wid >> 1, hh = lane >> 5, r32 = lane & 31;
  {
    const WFrag wf = load_wfrag(sm + K1_WPHI + hl * 800, lane);
#pragma unroll
    for (int t = 0; t < 2; ++t) {
      const int row = 64 * (wid & 1) + 32 * t + r32;
      const LAS char* kp = sm + EP_K + hl * 5120 + row * 40;
      const u32x2 a = *(const LAS u32x2*)(kp + 16 * hh), b = *(const LAS u32x2*)(kp + 16 * hh + 8), c = *(const LAS u32x2*)(kp + 32);
      unsigned ph[10];
      hedgehog(wf, asfrag((u32x4){a[0], a[1], b[0], b[1]}), asfrag((u32x4){c[0], c[1], c[0], c[1]}), ph);
      LAS char* pp = smw + EP_PHI + (hl * 128 + row) * 88 + hh * 40;
#pragma unroll
      for (int i = 0; i < 5; ++i) *(LAS u32x2*)(pp + 8 * i) = (u32x2){ph[2 * i], ph[2 * i + 1]};
    }
  }
  LBAR();
  STAMP(0, 14);
  {
    const int ft = wid & 1, b = (lane >> 4) & 1;
    const LAS char* pa = sm + EP_PHI + (hl * 128 + 8 * hh + q4) * 88 + (32 * ft + 16 * b + 4 * p4) * 2;
    const LAS char* va = sm + EP_V + (hl * 128 + 8 * hh + q4) * 48 + (16 * b + 4 * p4) * 2;
    f32x16 d = {};
#pragma unroll
    for (int ks = 0; ks < 8; ++ks) {
      const bf16x8 af = cat8(trread(pa + ks * 16 * 88), trread(pa + (ks * 16 + 4) * 88));
      const bf16x8 bf = cat8(trread(va + ks * 16 * 48), trread(va + (ks * 16 + 4) * 48));
      d = MFMA32(af, bf, d);
    }
    if (r32 <= 20) {
      float* o = (float*)(ws + WS_KVP) + ((mt * NH + 4 * nt + hl) * 21 + r32) * 40 + 32 * ft + 4 * hh;
      *(f32x4*)o = (f32x4){d[0], d[1], d[2], d[3]};
      if (ft == 0) {
        *(f32x4*)(o + 8) = (f32x4){d[4], d[5], d[6], d[7]};
        *(f32x4*)(o + 16) = (f32x4){d[8], d[9], d[10], d[11]};
        *(f32x4*)(o + 24) = (f32x4){d[12], d[13], d[14], d[15]};
      }
    }
  }
  STAMP(0, 15);
  STAMP_FLUSH(0);
}

constexpr int K2_K = 0, K2_V = 20480, K2_KV = K2_V + 24576 + 64, K2_W = K2_KV + 32 * 112, K2_LDS = K2_W + 1024;
constexpr float SC2 = 0.22360679774997896f * 1.4426950408889634f;

DI void store_o(char* og, int h, int srow, int hh, const float (&o)[12]) {
  const int p = h >> 1, odd = h & 1;
  char* b = og + (unsigned)(((srow >> 4) * 20 + 5 * (p >> 2)) * 1024 + ((p & 3) * 16 + (srow & 15)) * 16);
  *(u32x4*)(b + (3 * odd + hh) * 1024) = (u32x4){pk2(o[0], o[1]), pk2(o[2], o[3]), pk2(o[4], o[5]), pk2(o[6], o[7])};
  if (hh == 0) *(u32x2*)(b + 2048 + 8 * odd) = (u32x2){pk2(o[8], o[9]), pk2(o[10], o[11])};
}

__global__ __launch_bounds__(512, 2) void k2_attn(const __hip_bfloat16* __restrict__ wphi, char* __restrict__ ws) {
  __shared__ __attribute__((aligned(16))) char smem[K2_LDS];
  const LAS char* sm = (const LAS char*)smem;
  LAS char* smw = (LAS char*)smem;
  const unsigned lds0 = (unsigned)(size_t)(LAS char*)smem;
  const int tid = threadIdx.x, lane = tid & 63;
  const int wid = __builtin_amdgcn_readfirstlane(tid >> 6);
  const int h = (blockIdx.x & 7) * 4 + ((blockIdx.x >> 3) & 3), rb = blockIdx.x >> 5;
  const int q0 = rb * 256;
  const int hh = lane >> 5, r32 = lane & 31, b16 = (lane >> 4) & 1, q4 = (lane & 15) >> 2, p4 = lane & 3;
  STAMP_DECL;
  STAMP(1, 0);
  {
    const __amdgpu_buffer_rsrc_t rk = mkrsrc(ws + WS_KB + h * (SEQ * 40), SEQ * 40);
    const __amdgpu_buffer_rsrc_t rv = mkrsrc(ws + WS_VB + h * (SEQ * 48), SEQ * 48);
    const __amdgpu_buffer_rsrc_t rp = mkrsrc((const char*)wphi + h * 800, 800);
    const unsigned vo = lane * 16;
#pragma unroll
    for (int i = 0; i < 3; ++i) {
      const int pc = wid + 8 * i;
      if (pc < 20 && (rb > 0 || pc >= 10)) dma16(rk, lds0 + K2_K + pc * 1024, vo, (unsigned)((q0 - 256) * 40 + pc * 1024));
    }
#pragma unroll
    for (int i = 0; i < 3; ++i) {
      const int pc = wid + 8 * i;
      if (rb > 0 || pc >= 12) dma16(rv, lds0 + K2_V + pc * 1024, vo, (unsigned)((q0 - 256) * 48 + pc * 1024));
    }
    if (wid == 7) dma16(rp, lds0 + K2_W, vo, 0);
    if (rb == 0) {
      *(LAS u32x4*)(smw + K2_K + tid * 16) = (u32x4){0u, 0u, 0u, 0u};
      if (tid < 128) *(LAS u32x4*)(smw + K2_K + 8192 + tid * 16) = (u32x4){0u, 0u, 0u, 0u};
      *(LAS u32x4*)(smw + K2_V + tid * 16) = (u32x4){0u, 0u, 0u, 0u};
      if (tid < 256) *(LAS u32x4*)(smw + K2_V + 8192 + tid * 16) = (u32x4){0u, 0u, 0u, 0u};
    }
  }
  const int srow = q0 + 32 * wid + r32;
  const unsigned qoff = (unsigned)((h * SEQ + srow) * 40);
  const u32x2 qa0 = *(const u32x2*)(ws + WS_QA + qoff + 16 * hh), qa1 = *(const u32x2*)(ws + WS_QA + qoff + 16 * hh + 8);
  const u32x2 qa2 = *(const u32x2*)(ws + WS_QA + qoff + 32);
  const u32x2 qb0 = *(const u32x2*)(ws + WS_QB + qoff + 16 * hh), qb1 = *(const u32x2*)(ws + WS_QB + qoff + 16 * hh + 8);
  const u32x2 qb2 = *(const u32x2*)(ws + WS_QB + qoff + 32);
  if (tid < 420) {
    const int f4 = tid >> 1, hf = tid & 1;
    const f32x4* src = (const f32x4*)(ws + WS_KVP) + (hf * 8 * NH + h) * 210 + f4;
    f32x4 s = src[0];
#pragma unroll
    for (int i = 1; i < 8; ++i) s += src[i * NH * 210];
#pragma unroll
    for (int i = 0; i < 4; ++i) s[i] += __shfl_xor(s[i], 1);
    const int d = f4 / 10, fq = f4 - d * 10;
    const int off = (fq >= 5 ? 48 + (fq - 5) * 8 : fq * 8);
    const u32x2 v = (u32x2){pk2(s[0], s[1]), pk2(s[2], s[3])};
    if (hf == 0) *(LAS u32x2*)(smw + K2_KV + d * 112 + off) = v;
    else if (d == 20) *(LAS u32x2*)(smw + K2_KV + 24 * 112 + off) = v;
  } else if (tid >= 448) {
    *(LAS u32x2*)(smw + K2_KV + ((tid - 448) >> 1) * 112 + (tid & 1) * 48 + 40) = (u32x2){0u, 0u};
  }
  STAMP(1, 1);
  WAIT_BAR(0);
  STAMP(1, 2);
  {
    const WFrag wf = load_wfrag(sm + K2_W, lane);
    unsigned ph[10];
    hedgehog(wf, asfrag((u32x4){qa0[0], qa0[1], qa1[0], qa1[1]}), asfrag((u32x4){qa2[0], qa2[1], qa2[0], qa2[1]}), ph);
    const LAS char* kp = sm + K2_KV + r32 * 112 + hh * 48;
    f32x16 n = {};
    n = MFMA32(*(const LAS bf16x8*)(kp), asfrag((u32x4){ph[0], ph[1], ph[2], ph[3]}), n);
    n = MFMA32(*(const LAS bf16x8*)(kp + 16), asfrag((u32x4){ph[4], ph[5], ph[6], ph[7]}), n);
    n = MFMA32(*(const LAS bf16x8*)(kp + 32), asfrag((u32x4){ph[8], ph[9], 0u, 0u}), n);
    const float den = rbf(rbf(hh ? n[8] : n[12]) + 1e-6f);
    const float r = 1.0f / den;
    float o[12];
#pragma unroll
    for (int i = 0; i < 12; ++i) o[i] = rbf(n[i]) * r;
    store_o(ws + WS_OA, h, srow, hh, o);
  }
  STAMP(1, 3);
  {
    const unsigned km = hh ? 0u : 0xFFFFFFFFu;
    const bf16x8 qf0 = asfrag((u32x4){qb0[0], qb0[1], qb1[0], qb1[1]});
    const bf16x8 qf1 = asfrag((u32x4){qb2[0] & km, qb2[1] & km, 0u, 0u});
    const LAS char* kp = sm + K2_K + (32 * wid + r32) * 40;
    f32x16 s[9];
#pragma unroll
    for (int t = 0; t < 9; ++t) {
      const u32x2 a = *(const LAS u32x2*)(kp + t * 1280 + 16 * hh), b = *(const LAS u32x2*)(kp + t * 1280 + 16 * hh + 8);
      const u32x2 c = *(const LAS u32x2*)(kp + t * 1280 + 32);
      f32x16 z = {};
      s[t] = MFMA32(asfrag((u32x4){a[0], a[1], b[0], b[1]}), qf0, z);
      s[t] = MFMA32(asfrag((u32x4){c[0], c[1], c[0], c[1]}), qf1, s[t]);
    }
    if (rb == 0) {
#pragma unroll
      for (int t = 0; t < 8; ++t)
        if (wid + t < 8) {
#pragma unroll
          for (int r = 0; r < 16; ++r) s[t][r] = -INFINITY;
        }
    }
#pragma unroll
    for (int r = 0; r < 16; ++r) {
      const int j = (r & 3) + 8 * (r >> 2) + 4 * hh;
      s[8][r] = (j <= r32) ? s[8][r] : s[0][r];
    }
    float m = s[8][0];
#pragma unroll
    for (int t = 1; t < 9; ++t)
#pragma unroll
      for (int r = 0; r < 16; ++r) m = __builtin_fmaxf(m, s[t][r]);
    {
      auto rr = __builtin_amdgcn_permlane32_swap(__float_as_uint(m), __float_as_uint(m), false, false);
      m = __builtin_fmaxf(__uint_as_float(rr[0]), __uint_as_float(rr[1]));
    }
    const float nm = -m * SC2;
    STAMP(1, 4);
    const LAS char* vp = sm + K2_V + (32 * wid + 4 * hh + q4) * 48 + (16 * b16 + 4 * p4) * 2;
    f32x16 o = {};
#pragma unroll
    for (int t = 1; t < 9; ++t) {
      unsigned pw[8];
#pragma unroll
      for (int i = 0; i < 8; ++i)
        pw[i] = pk2(__builtin_amdgcn_exp2f(__builtin_fmaf(s[t][2 * i], SC2, nm)), __builtin_amdgcn_exp2f(__builtin_fmaf(s[t][2 * i + 1], SC2, nm)));
      if (t == 8) {
        unsigned pl[8];
#pragma unroll
        for (int i = 0; i < 8; ++i) {
          const int j = ((2 * i) & 3) + 8 * ((2 * i) >> 2) + 4 * hh;
          const unsigned mk = (j <= r32 ? 0xFFFFu : 0u) | (j + 1 <= r32 ? 0xFFFF0000u : 0u);
          pl[i] = pw[i] & ~mk;
          pw[i] &= mk;
        }
        o = MFMA32(cat8(trread(vp), trread(vp + 8 * 48)), asfrag((u32x4){pl[0], pl[1], pl[2], pl[3]}), o);
        o = MFMA32(cat8(trread(vp + 16 * 48), trread(vp + 24 * 48)), asfrag((u32x4){pl[4], pl[5], pl[6], pl[7]}), o);
      }
      o = MFMA32(cat8(trread(vp + t * 1536), trread(vp + t * 1536 + 8 * 48)), asfrag((u32x4){pw[0], pw[1], pw[2], pw[3]}), o);
      o = MFMA32(cat8(trread(vp + t * 1536 + 16 * 48), trread(vp + t * 1536 + 24 * 48)), asfrag((u32x4){pw[4], pw[5], pw[6], pw[7]}), o);
    }
    auto rr = __builtin_amdgcn_permlane32_swap(__float_as_uint(o[8]), __float_as_uint(o[8]), false, false);
    const float r = __builtin_amdgcn_rcpf(__uint_as_float(rr[1]));
    float of[12];
#pragma unroll
    for (int i = 0; i < 12; ++i) of[i] = o[i] * r;
    store_o(ws + WS_OB, h, srow, hh, of);
  }
  STAMP(1, 5);
  STAMP_FLUSH(1);
}

constexpr int K3_W = 0, K3_X = 20 * 5120 + 256, K3_LDS = K3_X + 8 * 5 * 1024;

__global__ __launch_bounds__(512, 2) void k3_out(const __hip_bfloat16* __restrict__ woA, const __hip_bfloat16* __restrict__ woB,
                                                 const char* __restrict__ ws, __hip_bfloat16* __restrict__ out) {
  __shared__ __attribute__((aligned(16))) char smem[K3_LDS];
  const LAS char* sm = (const LAS char*)smem;
  LAS char* smw = (LAS char*)smem;
  const unsigned lds0 = (unsigned)(size_t)(LAS char*)smem;
  const int tid = threadIdx.x, lane = tid & 63;
  const int wid = __builtin_amdgcn_readfirstlane(tid >> 6);
  const int xcd = blockIdx.x & 7, idx = blockIdx.x >> 3;
  const int br = xcd >> 2;
  const int mt = (xcd & 3) * 4 + (idx & 3), nt = idx >> 2;
  const int wm = wid & 3, kh = wid >> 2;
  const __amdgpu_buffer_rsrc_t rw = mkrsrc((const char*)(br ? woB : woA) + nt * 160, 640 * 1280 - nt * 160);
  const __amdgpu_buffer_rsrc_t ro = mkrsrc(ws + (br ? WS_OB : WS_OA) + (unsigned)((mt * 8 + 2 * wm) * 20 + 10 * kh) * 1024u, 30 * 1024);
  STAMP_DECL;
  STAMP(2, 0);
  const int lq = (lane >= 10) + (lane >= 20) + (lane >= 30);
  const unsigned vw = (unsigned)(lq * 1280 + (lane - 10 * lq) * 16);
  const int sg = 2 * (wid >> 2) + (wid & 1), st = (wid >> 1) & 1;
  const unsigned vo = lane * 16;
  u32x4 xf[10][2];
#pragma unroll
  for (int tau = 0; tau < 10; ++tau) {
#pragma unroll
    for (int k2 = 0; k2 < 2; ++k2) {
      const int ks = 10 * k2 + tau, w5 = ks % 5;
      const int o0 = w5 == 0 ? 0 : w5 == 1 ? 4 : w5 == 2 ? 16 : w5 == 3 ? 20 : 24;
      const int row = 160 * (ks / 5) + o0 + 40 * sg + st * (w5 == 2 ? 20 : 8);
      dma16m<0xFFu>(rw, lds0 + K3_W + ks * 5120 + wid * 640, vw, (unsigned)row * 1280u);
    }
    asm volatile("buffer_load_dwordx4 %0, %1, %2, 0 offen offset:%3" : "=v"(xf[tau][0]) : "v"(vo + (tau >> 2) * 4096), "s"(ro), "i"((tau & 3) * 1024) : "memory");
    asm volatile("buffer_load_dwordx4 %0, %1, %2, 0 offen offset:%3" : "=v"(xf[tau][1]) : "v"(vo + 20480 + (tau >> 2) * 4096), "s"(ro), "i"((tau & 3) * 1024) : "memory");
  }
  const int g = lane >> 4, fr = lane & 15, q4 = fr >> 2, p4 = lane & 3;
  const LAS char* wa = sm + K3_W + kh * 51200 + (16 * (g >> 1) + 4 * (g & 1) + q4) * 160 + 8 * p4;
  f32x4 acc[2][5] = {};
  STAMP(2, 1);
#define K3_PHASE(PH, N)                                                                                              \
  asm volatile("s_waitcnt vmcnt(" #N ")\n\ts_barrier"                                                                \
               : "+v"(xf[2 * PH][0]), "+v"(xf[2 * PH][1]), "+v"(xf[2 * PH + 1][0]), "+v"(xf[2 * PH + 1][1])::"memory"); \
  _Pragma("unroll") for (int tt = 0; tt < 2; ++tt) {                                                                 \
    const int tau = 2 * PH + tt;                                                                                     \
    _Pragma("unroll") for (int j = 0; j < 5; ++j) {                                                                  \
      const bf16x8 wf = cat8(trread(wa + tau * 5120 + j * 32), trread(wa + tau * 5120 + 8 * 160 + j * 32));          \
      acc[0][j] = MFMA16(wf, asfrag(xf[tau][0]), acc[0][j]);                                                         \
      acc[1][j] = MFMA16(wf, asfrag(xf[tau][1]), acc[1][j]);                                                         \
    }                                                                                                                \
  }
  K3_PHASE(0, 32)
  K3_PHASE(1, 24)
  K3_PHASE(2, 16)
  K3_PHASE(3, 8)
  K3_PHASE(4, 0)
  STAMP(2, 2);
#undef K3_PHASE
#pragma unroll
  for (int j = 0; j < 5; ++j) *(LAS f32x4*)(smw + K3_X + (wid * 5 + j) * 1024 + lane * 16) = kh ? acc[0][j] : acc[1][j];
  LBAR();
  char* og = (char*)out + (size_t)(mt * 128 + 32 * wm + 16 * kh + fr) * 2560 + br * 1280 + nt * 160 + g * 8;
#pragma unroll
  for (int j = 0; j < 5; ++j) {
    const f32x4 o = *(const LAS f32x4*)(sm + K3_X + ((wid ^ 4) * 5 + j) * 1024 + lane * 16);
    const f32x4 a = kh ? acc[1][j] : acc[0][j];
    *(u32x2*)(og + j * 32) = (u32x2){pk2(a[0] + o[0], a[1] + o[1]), pk2(a[2] + o[2], a[3] + o[3])};
  }
  STAMP(2, 3);
  STAMP_FLUSH(2);
}

struct Ptrs { const char* p[9]; };
template <int MODE> __global__ __launch_bounds__(512) void touch_k(const char* a0, const char* a1, const char* a2, const char* a3, const char* a4, const char* a5, const char* a6, char* ws, const char* out) {
  const char* p[9] = {a0, a1, a2, a3, a4, a5, a6, ws, out};
  const unsigned sz[9] = {2621440u, 2457600u, 25600u, 819200u, 2621440u, 2457600u, 819200u, WS_END, 5242880u};
  unsigned acc = 0;
  if (MODE == 1) {
    const unsigned t = blockIdx.x * 512 + threadIdx.x;
#pragma unroll
    for (int i = 0; i < 7; ++i) for (unsigned o = t * 128u; o < sz[i]; o += 131072u * 128u) acc ^= *(const unsigned*)(p[i] + o);
  } else if (MODE == 2) {
    const unsigned t = (blockIdx.x >> 3) * 512 + threadIdx.x;
#pragma unroll
    for (int i = 0; i < 9; ++i) for (unsigned o = t * 4096u; o < sz[i]; o += 16384u * 4096u) acc ^= *(const unsigned*)(p[i] + o);
  } else if (MODE == 3) {
    unsigned long long pc; asm volatile("s_getpc_b64 %0" : "=s"(pc));
    const char* c = (const char*)((pc & ~4095ull) - 24576);
    const unsigned t = (blockIdx.x >> 3) * 512 + threadIdx.x;
    if (t < 224) acc ^= *(const unsigned*)(c + t * 128);
  } else if (MODE == 4) {
    const unsigned t = (blockIdx.x >> 3) * 512 + threadIdx.x;
#pragma unroll
    for (int i = 0; i < 7; ++i) for (unsigned o = t * 128u; o < sz[i]; o += 16384u * 128u) acc ^= *(const unsigned*)(p[i] + o);
  }
  if (acc == 0x12345u || threadIdx.x == 0) ((int*)(ws + (100u << 20)))[blockIdx.x] = acc;
}
extern "C" void kernel_launch(void* const* d_in, const int* in_sizes, int n_in, void* d_out, int out_size, void* d_ws,
                              size_t ws_size, hipStream_t stream) {
  const __hip_bfloat16* Ax = (const __hip_bfloat16*)d_in[0];
  const __hip_bfloat16* Aw = (const __hip_bfloat16*)d_in[1];
  const __hip_bfloat16* Aphi = (const __hip_bfloat16*)d_in[2];
  const __hip_bfloat16* Awo = (const __hip_bfloat16*)d_in[3];
  const __hip_bfloat16* Bx = (const __hip_bfloat16*)d_in[4];
  const __hip_bfloat16* Bw = (const __hip_bfloat16*)d_in[5];
  const __hip_bfloat16* Bwo = (const __hip_bfloat16*)d_in[6];
  char* ws = (char*)d_ws;
  touch_k<0><<<256, 512, 0, stream>>>((const char*)Ax, (const char*)Aw, (const char*)Aphi, (const char*)Awo, (const char*)Bx, (const char*)Bw, (const char*)Bwo, ws, (const char*)d_out);
  k1_qkv<<<256, 512, 0, stream>>>(Ax, Aw, Bx, Bw, Aphi, ws);
  k2_attn<<<256, 512, 0, stream>>>(Aphi, ws);
  k3_out<<<256, 512, 0, stream>>>(Awo, Bwo, ws, (__hip_bfloat16*)d_out);
}
```

```cpp
#include <hip/hip_runtime.h>
#include <hip/hip_bf16.h>

typedef short bf16x8 __attribute__((ext_vector_type(8)));
typedef short s16x4 __attribute__((ext_vector_type(4)));
typedef float f32x2 __attribute__((ext_vector_type(2)));
typedef float f32x4 __attribute__((ext_vector_type(4)));
typedef float f32x16 __attribute__((ext_vector_type(16)));
typedef unsigned u32x2 __attribute__((ext_vector_type(2)));
typedef unsigned u32x4 __attribute__((ext_vector_type(4)));
typedef __bf16 bf16x2_t __attribute__((ext_vector_type(2)));
typedef __amdgpu_buffer_rsrc_t rsrc_t;
#define LAS __attribute__((address_space(3)))
#define GAS __attribute__((address_space(1)))
#define DI __device__ __forceinline__

#define PFV 2
#define OUT_AUX 0
constexpr int SEQ = 2048, DM = 640, NH = 32;
constexpr unsigned WS_G = 0;
constexpr unsigned WS_IN = 4096;
constexpr unsigned WS_KVH = WS_IN + 256 * 256;
constexpr unsigned WS_KVP = WS_KVH + 128 * 40960;
constexpr unsigned WS_O = WS_KVP + 128 * 13440;
constexpr unsigned WS_END = WS_O + 2 * SEQ * DM * 2;
constexpr unsigned OOB = 0x7FFF0000u;

DI unsigned pk2(float lo, float hi) {
  bf16x2_t r = __builtin_convertvector((f32x2){lo, hi}, bf16x2_t);
  return __builtin_bit_cast(unsigned, r);
}
DI float rbf(float x) { return __builtin_bit_cast(float, pk2(0.f, x)); }
DI float fmax3(float a, float b, float c) { return __builtin_elementwise_maximum(__builtin_elementwise_maximum(a, b), c); }
DI s16x4 trread(const LAS char* p) { return __builtin_amdgcn_ds_read_tr16_b64_v4i16((LAS s16x4*)p); }
DI bf16x8 cat8(s16x4 a, s16x4 b) { return __builtin_shufflevector(a, b, 0, 1, 2, 3, 4, 5, 6, 7); }
DI bf16x8 asfrag(u32x4 v) { return __builtin_bit_cast(bf16x8, v); }
DI bf16x8 frag2(u32x2 a, u32x2 b) { return asfrag((u32x4){a[0], a[1], b[0], b[1]}); }
DI rsrc_t mkrsrc(const void* p, unsigned bytes) { return __builtin_amdgcn_make_buffer_rsrc((void*)p, (short)0, (int)bytes, 0x00020000); }
DI void dma16(rsrc_t rs, unsigned lds, unsigned voff, unsigned soff) {
  asm volatile("s_mov_b32 m0, %0\n\ts_nop 0\n\tbuffer_load_dwordx4 %1, %2, %3 offen lds"
               :: "s"(lds), "v"(voff), "s"(rs), "s"(soff) : "memory");
}
DI void dma16c(rsrc_t rs, unsigned lds, unsigned voff, unsigned soff) {
  asm volatile("s_mov_b32 m0, %0\n\ts_nop 0\n\tbuffer_load_dwordx4 %1, %2, %3 offen sc1 lds"
               :: "s"(lds), "v"(voff), "s"(rs), "s"(soff) : "memory");
}
DI void touch(rsrc_t rs, unsigned lds, unsigned voff, unsigned soff) {
  asm volatile("s_mov_b32 m0, %0\n\ts_nop 0\n\tbuffer_load_dword %1, %2, %3 offen lds"
               :: "s"(lds), "v"(voff), "s"(rs), "s"(soff) : "memory");
}
template <unsigned LO, unsigned HI>
DI void dma16m(rsrc_t rs, unsigned lds, unsigned voff, unsigned soff) {
  asm volatile("s_mov_b32 m0, %0\n\ts_mov_b32 exec_lo, %4\n\ts_mov_b32 exec_hi, %5\n\tbuffer_load_dwordx4 %1, %2, %3 offen lds\n\ts_mov_b64 exec, -1"
               :: "s"(lds), "v"(voff), "s"(rs), "s"(soff), "i"(LO), "i"(HI) : "memory");
}
DI void ld8(u32x2& d, rsrc_t rs, unsigned voff, unsigned soff) {
  asm volatile("buffer_load_dwordx2 %0, %1, %2, %3 offen" : "=v"(d) : "v"(voff), "s"(rs), "s"(soff) : "memory");
}
DI void ld16(u32x4& d, rsrc_t rs, unsigned voff, unsigned soff) {
  asm volatile("buffer_load_dwordx4 %0, %1, %2, %3 offen" : "=v"(d) : "v"(voff), "s"(rs), "s"(soff) : "memory");
}
DI void ld16c(u32x4& d, rsrc_t rs, unsigned voff, unsigned soff) {
  asm volatile("buffer_load_dwordx4 %0, %1, %2, %3 offen sc1" : "=v"(d) : "v"(voff), "s"(rs), "s"(soff) : "memory");
}
DI void st16c(rsrc_t rs, unsigned voff, u32x4 v) { __builtin_amdgcn_raw_buffer_store_b128(v, rs, voff, 0, 16); }
DI void st8c(rsrc_t rs, unsigned voff, u32x2 v) { __builtin_amdgcn_raw_buffer_store_b64(v, rs, voff, 0, 16); }
DI u32x2 lds8(const LAS char* p) { return *(const volatile LAS u32x2*)p; }
DI u32x2 next_tag(u32x2 g) { return (u32x2){g[0] * 1664525u + 1013904223u, g[1] * 22695477u + 2654435761u}; }
#define MFMA16(a, b, c) __builtin_amdgcn_mfma_f32_16x16x32_bf16((a), (b), (c), 0, 0, 0)
#define MFMA32(a, b, c) __builtin_amdgcn_mfma_f32_32x32x16_bf16((a), (b), (c), 0, 0, 0)
#define WAIT_BAR(N) asm volatile("s_waitcnt vmcnt(" #N ") lgkmcnt(0)\n\ts_barrier" ::: "memory")
#define LBAR() asm volatile("s_waitcnt lgkmcnt(0)\n\ts_barrier" ::: "memory")
#define SB() __builtin_amdgcn_sched_barrier(0)
#define PIN8(a) asm volatile("" : "+v"(a[0]), "+v"(a[1]), "+v"(a[2]), "+v"(a[3]), "+v"(a[4]), "+v"(a[5]), "+v"(a[6]), "+v"(a[7]))

#define STAMP_DECL
#define STAMP(kid, i)
#define STAMP_FLUSH(kid)

constexpr float LOG2E = 1.4426950408889634f;

struct WFrag { bf16x8 t0k0, t0k1, t1k0, t1k1; };
DI WFrag load_wfrag(const LAS char* wl  , int lane) {
  const int hh = lane >> 5, b = (lane >> 4) & 1, q = (lane & 15) >> 2, p = lane & 3;
  const LAS char* a0 = wl + (8 * hh + q) * 40 + (2 * b + (p >> 1)) * 8;
  const LAS char* a1 = wl + (8 * hh + q) * 40 + 32;
  const LAS char* c0 = wl + (16 + q) * 40 + (2 * b + (p >> 1)) * 8;
  const LAS char* c1 = wl + (16 + q) * 40 + 32;
  u32x2 r0 = __builtin_bit_cast(u32x2, trread(a0)), r1 = __builtin_bit_cast(u32x2, trread(a0 + 160));
  u32x2 r2 = __builtin_bit_cast(u32x2, trread(a1)), r3 = __builtin_bit_cast(u32x2, trread(a1 + 160));
  u32x2 r4 = __builtin_bit_cast(u32x2, trread(c0)), r5 = __builtin_bit_cast(u32x2, trread(c1));
  const unsigned sg = (lane & 4) ? 0x80008000u : 0u;
  const unsigned km = hh ? 0u : 0xFFFFFFFFu;
  WFrag w;
  w.t0k0 = asfrag((u32x4){r0[0] ^ sg, r0[1] ^ sg, r1[0] ^ sg, r1[1] ^ sg});
  w.t1k0 = asfrag((u32x4){r2[0] ^ sg, r2[1] ^ sg, r3[0] ^ sg, r3[1] ^ sg});
  w.t0k1 = asfrag((u32x4){(r4[0] ^ sg) & km, (r4[1] ^ sg) & km, 0u, 0u});
  w.t1k1 = asfrag((u32x4){(r5[0] ^ sg) & km, (r5[1] ^ sg) & km, 0u, 0u});
  return w;
}
DI void hedgehog(const WFrag& w, bf16x8 x0, bf16x8 x1, unsigned (&ph)[10]) {
  f32x16 z = {};
  f32x16 p0 = MFMA32(w.t0k0, x0, z);
  f32x16 p1 = MFMA32(w.t1k0, x0, z);
  p0 = MFMA32(w.t0k1, x1, p0);
  p1 = MFMA32(w.t1k1, x1, p1);
  f32x2 v[10];
#pragma unroll
  for (int i = 0; i < 8; ++i) v[i] = (f32x2){rbf(p0[2 * i]), rbf(p0[2 * i + 1])};
#pragma unroll
  for (int i = 0; i < 2; ++i) v[8 + i] = (f32x2){rbf(p1[2 * i]), rbf(p1[2 * i + 1])};
  float m = __builtin_elementwise_maximum(v[0][0], v[0][1]);
#pragma unroll
  for (int i = 1; i < 10; ++i) m = fmax3(m, v[i][0], v[i][1]);
  const float nm = -m * LOG2E;
  f32x2 s = {0.f, 0.f};
#pragma unroll
  for (int i = 0; i < 10; ++i) {
    v[i] = __builtin_elementwise_fma(v[i], (f32x2){LOG2E, LOG2E}, (f32x2){nm, nm});
    v[i] = (f32x2){__builtin_amdgcn_exp2f(v[i][0]), __builtin_amdgcn_exp2f(v[i][1])};
    s += v[i];
  }
  const float r = __builtin_amdgcn_rcpf(s[0] + s[1]);
#pragma unroll
  for (int i = 0; i < 10; ++i) { v[i] *= (f32x2){r, r}; ph[i] = pk2(v[i][0], v[i][1]); }
}

constexpr float SC2 = 0.22360679774997896f * 1.4426950408889634f;

DI void store_o(rsrc_t ro, int h, int srow, int hh, const float (&o)[12]) {
  const int p = h >> 1, odd = h & 1;
  const unsigned vo = (unsigned)((srow >> 4) * 20480 + (srow & 15) * 16 + 5 * (p >> 2) * 1024 + (p & 3) * 256);
  st16c(ro, vo + (hh + 3 * odd) * 1024, (u32x4){pk2(o[0], o[1]), pk2(o[2], o[3]), pk2(o[4], o[5]), pk2(o[6], o[7])});
  st8c(ro, hh ? OOB : vo + 2048 + 8 * odd, (u32x2){pk2(o[8], o[9]), pk2(o[10], o[11])});
}
DI void wait_flags(rsrc_t rin, unsigned voff, bool need, u32x2 tag) {
#pragma unroll 1
  for (int spin = 0; spin < (1 << 20); ++spin) {
    u32x2 f;
    asm volatile("buffer_load_dwordx2 %0, %1, %2, 0 offen sc1\n\ts_waitcnt vmcnt(0)" : "=&v"(f) : "v"(voff), "s"(rin) : "memory");
    if (__all(!need || (f[0] == tag[0] && f[1] == tag[1]))) break;
    __builtin_amdgcn_s_sleep(1);
  }
}

constexpr int G_STAGE = 16384 + 30720;
constexpr int B_KP = 3072, B_KH = 5 * B_KP, B_VP = 3104, B_VH = 15616;
constexpr int B_K = 0, B_V = 4 * B_KH, B_ONE = B_V + 4 * B_VH + 160, B_BIAS = B_ONE + 3072 + 96, L_Q = B_BIAS + 3072;
constexpr int A_KP = 1024, A_KH = 5120, A_VP = 1056, A_VH = 5376;
constexpr int A_K = 0, A_V = 4 * A_KH, A_ONE = A_V + 4 * A_VH + 160, A_PHI = A_ONE + 1024 + 96, A_KV = A_PHI + 8 * 5632;
constexpr int L_W = L_Q + 20480, L_G = L_W + 4096, L_T = L_G + 1024, K_LDS = L_T + 256;
constexpr int P_X = 20 * 5120 + 256;
static_assert(A_KV + 4 * 3584 <= L_Q && 3 * G_STAGE + 64 <= L_W && K_LDS <= 160 * 1024 - 2048 && P_X + 40960 <= K_LDS, "LDS map");
static_assert(B_V % 256 == 0 && B_VH % 256 == 0 && A_V % 256 == 0 && A_VH % 256 == 0 && B_BIAS % 16 == 0 && A_PHI % 16 == 0, "banks");

__global__ __launch_bounds__(512, 2) void k1_fused(const __hip_bfloat16* __restrict__ xA, const __hip_bfloat16* __restrict__ wA,
                                                   const __hip_bfloat16* __restrict__ xB, const __hip_bfloat16* __restrict__ wB,
                                                   const __hip_bfloat16* __restrict__ wphi, const __hip_bfloat16* __restrict__ woA,
                                                   const __hip_bfloat16* __restrict__ woB, char* __restrict__ ws,
                                                   __hip_bfloat16* __restrict__ out) {
  __shared__ __attribute__((aligned(16))) char smem[K_LDS];
  const LAS char* sm = (const LAS char*)smem;
  LAS char* smw = (LAS char*)smem;
  const unsigned lds0 = (unsigned)(size_t)(LAS char*)smem;
  const int tid = threadIdx.x, lane = tid & 63;
  const int wid = __builtin_amdgcn_readfirstlane(tid >> 6);
  const int xcd = blockIdx.x & 7, idx = blockIdx.x >> 3;
  const int br = xcd >> 2, mt = (xcd & 3) * 4 + (idx & 3), nt = idx >> 2;
  const int me = br * 128 + mt * 8 + nt;
  const int g = lane >> 4, fr = lane & 15, q4 = fr >> 2, p4 = lane & 3;
  STAMP_DECL;
  STAMP(0, 0);
  const char* xg = (const char*)(br ? xB : xA);
  const rsrc_t rx = mkrsrc(xg + (size_t)mt * 128 * 1280, 128 * 1280);
  const rsrc_t rw = mkrsrc((const char*)(br ? wB : wA) + nt * 160, 640 * 3840 - nt * 160);
  const unsigned vx0 = (unsigned)((2 * (lane >> 3)) * 1280 + (((lane & 7) ^ g) * 16));
  const unsigned vx1 = (unsigned)((16 + 2 * (lane >> 3)) * 1280 + (((lane & 7) ^ (4 + g)) * 16));
  const int wr = lane >= 30 ? 1 : 0, wc = lane - 30 * wr, wsg = (wc >= 10) + (wc >= 20);
  const unsigned vw = (unsigned)(wr * 3840 + wsg * 1280 + (wc - 10 * wsg) * 16);
  const unsigned sx = (unsigned)((32 * (wid >> 1) + (wid & 1)) * 1280);
  const unsigned sw = (unsigned)((16 * (wid >> 1) + 4 * (wid & 1)) * 3840);
  const int k0 = ((xcd & 3) * 5 + 1) >> 1;
  const int k1 = k0 + 1 >= 10 ? k0 - 9 : k0 + 1, k2 = k0 + 2 >= 10 ? k0 - 8 : k0 + 2;
  const unsigned lx = lds0 + wid * 2048, lw = lds0 + 16384 + wid * 3840;
#define G_DMA(i, bo, xo, wo) do { \
    if ((i) == 0 || (i) == 3) dma16(rx, lx + (bo) + ((i) ? 1024 : 0), (i) ? vx1 : vx0, (xo)); \
    else { const int n_ = (i) < 3 ? (i) - 1 : (i) - 2; \
      dma16m<0xFFFFFFFFu, 0x0FFFFFFFu>(rw, lw + (bo) + n_ * 960, vw, (wo) + ((n_ >> 1) * 8 + (n_ & 1) * 2) * 3840); } } while (0)
  int kx = k0 + 1 + g; kx -= kx >= 10 ? 10 : 0;
  const unsigned vpx = (unsigned)((nt * 16 + fr) * 1280);
  if (wid < 2) touch(mkrsrc(br ? wB : wA, 640 * 3840), lds0 + L_T, lane < 60 ? (unsigned)((idx * 60 + lane) * 128) : OOB, (wid ? k2 : k1) * 245760);
  if (wid == 2) touch(rx, lds0 + L_T, vpx + kx * 128, 0);
#pragma unroll
  for (int i = 0; i < 6; ++i) G_DMA(i, 0, sx + k0 * 128, sw + k0 * 245760);
  asm volatile("s_barrier" ::: "memory");
#pragma unroll
  for (int i = 0; i < 6; ++i) G_DMA(i, G_STAGE, sx + k1 * 128, sw + k1 * 245760);

  const int wm = wid >> 2, wn = wid & 3;
  const LAS char* xa0 = sm + (64 * wm + fr) * 128 + ((g ^ (fr >> 1)) * 16);
  const LAS char* xa1 = sm + (((64 * wm + fr) * 128 + ((g ^ (fr >> 1)) * 16)) ^ 64);
  const LAS char* wa = sm + 16384 + (16 * (g >> 1) + 4 * (g & 1) + q4) * 480 + 8 * p4 + wn * 128;
  f32x4 acc[4][4] = {};
#pragma unroll
  for (int i = 0; i < 4; ++i) asm volatile("" : "+v"(acc[i][0]), "+v"(acc[i][1]), "+v"(acc[i][2]), "+v"(acc[i][3]));
  int eb[4];
  {
    const int kb = br ? B_K + 2048 : A_K, kp = br ? B_KP : A_KP, vb = br ? B_V + 2048 : A_V, vp = br ? B_VP : A_VP, vh = br ? B_VH - 5 * B_VP : A_VH - 5 * A_VP;
#pragma unroll
    for (int j = 0; j < 4; ++j) {
      const int qd = 16 * wn + 4 * j + g, r = qd - 40;
      const int v = vb + r * vp + ((r >= 5) + (r >= 10) + (r >= 15)) * vh;
      eb[j] = (qd < 20 ? L_Q + qd * 1024 : qd < 40 ? kb + (qd - 20) * kp : v) + wm * 512 + fr * 16;
      asm volatile("" : "+v"(eb[j]));
    }
  }
  const int cons = lane < 16 ? (br ? ((lane < 2 && mt + 1 + lane < 16) ? me + 8 * (1 + lane) : -1) : lane * 8 + nt)
                             : (lane < 24 ? (me & ~7) + (lane - 16) : lane == 63 ? me : -1);
  const rsrc_t rg = mkrsrc(ws + WS_G, WS_KVH);
  const rsrc_t rph = mkrsrc((const char*)wphi + nt * 3200, 3200);
  bf16x8 x0[4], x1[4];
  s16x4 w0[4][2], w1[4][2];
#define G_RD(X, W, xa, kk, r) do { \
    if ((r) < 2) W[0][(r)] = trread(wa + ((kk) * 32 + (r) * 8) * 480); \
    else if ((r) < 6) X[(r) - 2] = *(const LAS bf16x8*)(xa + ((r) - 2) * 2048); \
    else if ((r) < 12) W[((r) - 4) >> 1][(r) & 1] = trread(wa + ((kk) * 32 + ((r) & 1) * 8) * 480 + (((r) - 4) >> 1) * 32); } while (0)
#define G_MM(X, W, m) acc[(m) & 3][(m) >> 2] = MFMA16(cat8(W[(m) >> 2][0], W[(m) >> 2][1]), X[(m) & 3], acc[(m) & 3][(m) >> 2])
  WAIT_BAR(6);
  STAMP(0, 1);
#pragma unroll
  for (int r = 0; r < 12; ++r) G_RD(x0, w0, xa0, 0, r);
  int bo = 2 * G_STAGE, kn = k2;
  unsigned xo = sx + k2 * 128, wo = sw + k2 * 245760;
#pragma unroll
  for (int i = 0; i < 3; ++i) G_DMA(i, bo, xo, wo);
  if (wn == 3) {
    int kk = k0 + 5 + 4 * wm + g; kk -= kk >= 10 ? 10 : 0;
    touch(rx, lds0 + L_T, 4 * wm + g < 5 ? vpx + kk * 128 : OOB, 0);
  }
#pragma unroll 1
  for (int t = 0; t < 10; ++t) {
    SB();
#pragma unroll
    for (int m = 0; m < 12; ++m) {
      G_MM(x0, w0, m); G_RD(x1, w1, xa1, 1, m);
      if (m % 3 == 2 && m < 9) G_DMA(3 + m / 3, bo, xo, wo);
      SB();
    }
    if (wn < 3) { G_MM(x0, w0, 12); G_MM(x0, w0, 13); G_MM(x0, w0, 14); G_MM(x0, w0, 15); }
    const int d = bo == G_STAGE ? -2 * G_STAGE : G_STAGE;
    xa0 += d; xa1 += d; wa += d;
    bo = bo == 2 * G_STAGE ? 0 : bo + G_STAGE;
    kn = kn == 9 ? 0 : kn + 1;
    xo = t < 7 ? sx + kn * 128 : OOB;
    wo = t < 7 ? sw + kn * 245760 : OOB;
    WAIT_BAR(6);
    SB();
#pragma unroll
    for (int m = 0; m < 12; ++m) {
      G_MM(x1, w1, m); G_RD(x0, w0, xa0, 0, m);
      if (m % 3 == 2 && m < 9) G_DMA(m / 3, bo, xo, wo);
      SB();
    }
    if (wn < 3) { G_MM(x1, w1, 12); G_MM(x1, w1, 13); G_MM(x1, w1, 14); G_MM(x1, w1, 15); }
    if (t == 7) {
      if (wid < 4) dma16(rph, lds0 + L_W + wid * 1024, (unsigned)lane * 16, wid * 1024);
      if (wid == 7) dma16(rg, lds0 + L_G, cons >= 0 ? cons * 16 : OOB, 0);
    }
  }
  u32x4 ev[4][2];
  {
    const rsrc_t rh = mkrsrc(ws + WS_KVH + (mt * 8 + nt) * 40960, (br && mt < 15) ? 40960 : 0);
    const unsigned v0 = (unsigned)((16 * wn + g - 20) * 1024 + wm * 512 + fr * 16);
#pragma unroll
    for (int j = 0; j < 4; ++j)
#pragma unroll
      for (int i = 0; i < 2; ++i) {
        const f32x4 a = acc[2 * i][j], b = acc[2 * i + 1][j];
        ev[j][i] = (u32x4){pk2(a[0], a[1]), pk2(a[2], a[3]), pk2(b[0], b[1]), pk2(b[2], b[3])};
        unsigned vo = v0 + j * 4096 + i * 256;
        asm volatile("" : "+v"(vo));
        st16c(rh, vo, ev[j][i]);
      }
  }
  WAIT_BAR(8);
  STAMP(0, 2);
#pragma unroll
  for (int j = 0; j < 4; ++j)
    if (j < 3 || wn < 3) {
      *(LAS u32x4*)(smw + eb[j]) = ev[j][0];
      *(LAS u32x4*)(smw + eb[j] + 256) = ev[j][1];
    }
  const u32x2 gc = next_tag(*(const LAS u32x2*)(sm + L_G + lane * 16));
  const u32x2 tag = {(unsigned)__builtin_amdgcn_readlane(gc[0], 63), (unsigned)__builtin_amdgcn_readlane(gc[1], 63)};
  const char* wog = (const char*)(br ? woB : woA);
  const int hh = lane >> 5, r32 = lane & 31, b16 = (lane >> 4) & 1;
  const rsrc_t ro = mkrsrc(ws + WS_O + br * (SEQ * DM * 2), SEQ * DM * 2);
  const unsigned vflag = (unsigned)(WS_IN + cons * 256 + (lane >= 16 ? 16 + nt : br ? 1 - lane : mt) * 8);
#define WO_PF() touch(mkrsrc(wog, 640 * 1280), lds0 + L_T, lane < 25 ? (unsigned)((idx * 200 + wid * 25 + lane) * 128) : OOB, 0)
  if (br) {
    if (tid < 384) {
      *(LAS u32x2*)(smw + B_ONE + tid * 8) = (u32x2){0x3F80u, 0u};
      *(LAS u32x2*)(smw + B_BIAS + tid * 8) = (u32x2){(mt * 128 - 256 + tid < 0) ? 0xFF7Fu : 0u, 0u};
    }
    WAIT_BAR(0);
    if (wid == 0) st8c(rg, (lane < 16 && cons >= 0) ? vflag : OOB, gc);
    STAMP(0, 3);
    const int nb = wid & 1, isv = (wid >> 1) & 1, hs = wid >> 2;
    const bool have = mt - 2 + nb >= 0;
    wait_flags(rg, (unsigned)(WS_IN + me * 256 + nb * 8), have, tag);
    STAMP(0, 4);
    const rsrc_t rn = mkrsrc(ws + WS_KVH + ((have ? mt - 2 + nb : 0) * 8 + nt) * 40960, have ? 40960 : 0);
    const unsigned hd = lds0 + (isv ? B_V + hs * B_VH : B_K + hs * B_KH) + nb * 1024, hps = isv ? B_VP : B_KP;
#pragma unroll
    for (int i = 0; i < 5; ++i) dma16c(rn, hd + i * hps, lane * 16, (20 * isv + 5 * hs + i) * 1024);
    const int qi = wid & 3;
    const unsigned km = hh ? 0u : 0xFFFFFFFFu;
    WAIT_BAR(0);
#pragma unroll
    for (int i = 0; i < 5; ++i) dma16c(rn, hd + 2 * (isv ? B_VH : B_KH) + i * hps, lane * 16, (20 * isv + 5 * hs + 10 + i) * 1024);
    WO_PF();
    STAMP(0, 5);
#pragma unroll 1
    for (int r = 0; r < 2; ++r) {
      const int hl = 2 * r + hs;
      const LAS char* qp = sm + L_Q + hl * 5120 + (32 * qi + r32) * 8;
      const bf16x8 qf0 = frag2(lds8(qp + hh * 2048), lds8(qp + hh * 2048 + 1024));
      u32x2 q4v = lds8(qp + 4096);
      const LAS char* kh = sm + B_K + hl * B_KH + hh * 2 * B_KP + (32 * qi + r32) * 8;
      const LAS char* k4 = sm + (hh ? B_BIAS : B_K + hl * B_KH + 4 * B_KP) + (32 * qi + r32) * 8;
      f32x16 s[9];
      const f32x16 z = {};
      u32x2 ka[9], kb[9], kc[10];
#define B_KRD(t) do { ka[t] = lds8(kh + (t) * 256); kb[t] = lds8(kh + (t) * 256 + B_KP); kc[t] = lds8(k4 + (t) * 256); } while (0)
#define B_QK(t, u, Q) do { s[t] = MFMA32(frag2(ka[t], kb[t]), qf0, z); s[t] = MFMA32(frag2(kc[(t) < (u) ? (t) : (u)], kc[(t) < (u) ? (u) : (t)]), Q, s[t]); } while (0)
#define B_MAX(t) do { _Pragma("unroll") for (int e = 0; e < 16; e += 4) { m0 = fmax3(m0, s[t][e], s[t][e + 1]); m1 = fmax3(m1, s[t][e + 2], s[t][e + 3]); } } while (0)
      float m0 = -INFINITY, m1 = -INFINITY;
      B_KRD(0); B_KRD(8); B_KRD(1); B_KRD(2);
      q4v = (u32x2){hh ? 0x3F80u : q4v[0], q4v[1] & km};
      const bf16x8 qfa = frag2(q4v, (u32x2){0u, 0u}), qfb = frag2((u32x2){0u, 0u}, q4v);
      SB();
      B_QK(0, 8, qfa); B_QK(8, 0, qfb); B_KRD(3); B_KRD(4); SB();
      B_QK(1, 2, qfa); B_QK(2, 1, qfb); B_KRD(5); B_KRD(6);
#pragma unroll
      for (int e = 0; e < 16; ++e) s[8][e] = ((e & 3) + 8 * (e >> 2) + 4 * hh <= r32) ? s[8][e] : s[0][e];
      SB();
      B_QK(3, 4, qfa); B_MAX(8); B_QK(4, 3, qfb); B_KRD(7); kc[9] = kc[7]; B_MAX(1); SB();
      B_QK(5, 6, qfa); B_MAX(2); B_QK(6, 5, qfb); B_MAX(3); SB();
      B_QK(7, 9, qfa); B_MAX(4); B_MAX(5); B_MAX(6); B_MAX(7);
      float m = __builtin_elementwise_maximum(m0, m1);
      {
        auto rr = __builtin_amdgcn_permlane32_swap(__float_as_uint(m), __float_as_uint(m), false, false);
        m = __builtin_elementwise_maximum(__uint_as_float(rr[0]), __uint_as_float(rr[1]));
      }
      const float nm = -m * SC2;
      if (r == 0) { WAIT_BAR(1); }
      STAMP(0, 6 + 2 * r);
      const int vq = 4 * b16 + p4;
      const LAS char* vp = sm + (vq < 5 ? B_V + hl * B_VH + vq * B_VP : B_ONE) + (32 * qi + 4 * hh + q4) * 8;
      f32x16 o = {};
#pragma unroll
      for (int t = 1; t < 9; ++t) {
        const bf16x8 v0 = cat8(trread(vp + t * 256), trread(vp + t * 256 + 64)), v1 = cat8(trread(vp + t * 256 + 128), trread(vp + t * 256 + 192));
        SB();
        f32x2 e[8];
#pragma unroll
        for (int i = 0; i < 8; ++i) e[i] = __builtin_elementwise_fma((f32x2){s[t][2 * i], s[t][2 * i + 1]}, (f32x2){SC2, SC2}, (f32x2){nm, nm});
        PIN8(e);
#pragma unroll
        for (int i = 0; i < 8; ++i) e[i] = (f32x2){__builtin_amdgcn_exp2f(e[i][0]), __builtin_amdgcn_exp2f(e[i][1])};
        PIN8(e);
        unsigned pw[8];
#pragma unroll
        for (int i = 0; i < 8; ++i) pw[i] = pk2(e[i][0], e[i][1]);
        SB();
        if (t == 8) {
          unsigned pl[8];
#pragma unroll
          for (int i = 0; i < 8; ++i) {
            const int j = ((2 * i) & 3) + 8 * ((2 * i) >> 2) + 4 * hh;
            const unsigned mk = (j <= r32 ? 0xFFFFu : 0u) | (j + 1 <= r32 ? 0xFFFF0000u : 0u);
            pl[i] = pw[i] & ~mk;
            pw[i] &= mk;
          }
          o = MFMA32(cat8(trread(vp), trread(vp + 64)), asfrag((u32x4){pl[0], pl[1], pl[2], pl[3]}), o);
          o = MFMA32(cat8(trread(vp + 128), trread(vp + 192)), asfrag((u32x4){pl[4], pl[5], pl[6], pl[7]}), o);
        }
        o = MFMA32(v0, asfrag((u32x4){pw[0], pw[1], pw[2], pw[3]}), o);
        o = MFMA32(v1, asfrag((u32x4){pw[4], pw[5], pw[6], pw[7]}), o);
        SB();
      }
      auto rr = __builtin_amdgcn_permlane32_swap(__float_as_uint(o[8]), __float_as_uint(o[8]), false, false);
      const float rl = __builtin_amdgcn_rcpf(__uint_as_float(rr[1]));
      float of[12];
#pragma unroll
      for (int i = 0; i < 12; ++i) of[i] = o[i] * rl;
      store_o(ro, 4 * nt + hl, mt * 128 + 32 * qi + r32, hh, of);
      STAMP(0, 7 + 2 * r);
    }
  } else {
    if (tid < 128) *(LAS u32x2*)(smw + A_ONE + tid * 8) = (u32x2){0x3F80u, 0u};
    if (tid >= 256) *(LAS u32x2*)(smw + A_KV + ((tid - 256) >> 1) * 112 + (tid & 1) * 48 + 40) = (u32x2){0u, 0u};
    WO_PF();
    LBAR();
    STAMP(0, 3);
    const int hl = wid >> 1, rw0 = 64 * (wid & 1);
    const WFrag wf = load_wfrag(sm + L_W + hl * 800, lane);
    LAS char* phw = smw + A_PHI + wid * 5632;
    const int vq = 4 * b16 + p4;
    const LAS char* pa = phw + (8 * hh + q4) * 88 + (16 * b16 + 4 * p4) * 2;
    const LAS char* va = sm + (vq < 5 ? A_V + hl * A_VH + vq * A_VP : A_ONE) + (rw0 + 8 * hh + q4) * 8;
    f32x16 d0 = {}, d1 = {};
#pragma unroll
    for (int t = 0; t < 2; ++t) {
      const LAS char* kp = sm + A_K + hl * A_KH + (rw0 + 32 * t + r32) * 8;
      const u32x2 c = lds8(kp + 4096);
      unsigned ph[10];
      hedgehog(wf, frag2(lds8(kp + hh * 2048), lds8(kp + hh * 2048 + 1024)), frag2(c, c), ph);
#pragma unroll
      for (int i = 0; i < 5; ++i) *(LAS u32x2*)(phw + (32 * t + r32) * 88 + hh * 40 + 8 * i) = (u32x2){ph[2 * i], ph[2 * i + 1]};
    }
#pragma unroll
    for (int ks = 0; ks < 4; ++ks) {
      const bf16x8 bf = cat8(trread(va + ks * 128), trread(va + ks * 128 + 32));
      d0 = MFMA32(cat8(trread(pa + ks * 16 * 88), trread(pa + (ks * 16 + 4) * 88)), bf, d0);
      d1 = MFMA32(cat8(trread(pa + ks * 16 * 88 + 64), trread(pa + (ks * 16 + 4) * 88 + 64)), bf, d1);
    }
    const int ro_ = min(r32, 21) * 160 + hh * 16;
    if (wid & 1) {
      *(LAS f32x4*)(phw + ro_) = (f32x4){d0[0], d0[1], d0[2], d0[3]};
      *(LAS f32x4*)(phw + ro_ + 32) = (f32x4){d0[4], d0[5], d0[6], d0[7]};
      *(LAS f32x4*)(phw + ro_ + 64) = (f32x4){d0[8], d0[9], d0[10], d0[11]};
      *(LAS f32x4*)(phw + ro_ + 96) = (f32x4){d0[12], d0[13], d0[14], d0[15]};
      *(LAS f32x4*)(phw + ro_ + 128) = (f32x4){d1[0], d1[1], d1[2], d1[3]};
    }
    LBAR();
    STAMP(0, 4);
    const rsrc_t rkv = mkrsrc(ws + WS_KVP + nt * 16 * 13440, 16 * 13440);
    if (!(wid & 1)) {
      const LAS char* o = phw + 5632 + ro_;
      const unsigned vo = r32 <= 20 ? (unsigned)(ro_ + mt * 13440 + hl * 3360) : OOB;
#pragma unroll
      for (int i = 0; i < 5; ++i) {
        const f32x4 a = i < 4 ? (f32x4){d0[4 * i], d0[4 * i + 1], d0[4 * i + 2], d0[4 * i + 3]} : (f32x4){d1[0], d1[1], d1[2], d1[3]};
        st16c(rkv, vo + 32 * i, __builtin_bit_cast(u32x4, a + *(const LAS f32x4*)(o + 32 * i)));
      }
    }
    unsigned phq[2][10];
    {
      const LAS char* qp = sm + L_Q + hl * 5120 + (rw0 + r32) * 8;
      const u32x2 c = lds8(qp + 4096);
      hedgehog(wf, frag2(lds8(qp + hh * 2048), lds8(qp + hh * 2048 + 1024)), frag2(c, c), phq[0]);
#pragma unroll
      for (int i = 0; i < 10; ++i) asm volatile("" : "+v"(phq[0][i]));
    }
    WAIT_BAR(0);
    if (wid == 0) st8c(rg, lane < 16 ? vflag : OOB, gc);
    STAMP(0, 5);
    {
      const LAS char* qp = sm + L_Q + hl * 5120 + (rw0 + 32 + r32) * 8;
      const u32x2 c = lds8(qp + 4096);
      hedgehog(wf, frag2(lds8(qp + hh * 2048), lds8(qp + hh * 2048 + 1024)), frag2(c, c), phq[1]);
#pragma unroll
      for (int i = 0; i < 10; ++i) asm volatile("" : "+v"(phq[1][i]));
    }
    wait_flags(rg, (unsigned)(WS_IN + me * 256 + fr * 8), true, tag);
    STAMP(0, 6);
    {
      u32x4 p[2][16];
      const unsigned v1 = tid < 328 ? (unsigned)(tid + 512) * 16u : OOB;
#pragma unroll
      for (int i = 0; i < 16; ++i) {
        p[0][i] = __builtin_amdgcn_raw_buffer_load_b128(rkv, tid * 16, i * 13440, 16);
        p[1][i] = __builtin_amdgcn_raw_buffer_load_b128(rkv, v1, i * 13440, 16);
      }
#pragma unroll
      for (int u = 0; u < 2; ++u) {
        f32x4 s4 = __builtin_bit_cast(f32x4, p[u][0]);
#pragma unroll
        for (int i = 1; i < 16; ++i) s4 += __builtin_bit_cast(f32x4, p[u][i]);
        const int e = tid + 512 * u, row = e / 10, fq = e - row * 10, h4 = row / 21, d = row - h4 * 21;
        LAS char* dst = smw + A_KV + h4 * 3584 + (fq >= 5 ? 8 + fq * 8 : fq * 8);
        const u32x2 v = (u32x2){pk2(s4[0], s4[1]), pk2(s4[2], s4[3])};
        if (e < 840) {
          *(LAS u32x2*)(dst + d * 112) = v;
          if (d == 20) *(LAS u32x2*)(dst + 24 * 112) = v;
        }
      }
    }
    LBAR();
    STAMP(0, 7);
    const LAS char* kp = sm + A_KV + hl * 3584 + r32 * 112 + hh * 48;
    const bf16x8 f0 = *(const LAS bf16x8*)(kp), f1 = *(const LAS bf16x8*)(kp + 16), f2 = *(const LAS bf16x8*)(kp + 32);
#pragma unroll
    for (int t = 0; t < 2; ++t) {
      f32x16 n = {};
      n = MFMA32(f0, asfrag((u32x4){phq[t][0], phq[t][1], phq[t][2], phq[t][3]}), n);
      n = MFMA32(f1, asfrag((u32x4){phq[t][4], phq[t][5], phq[t][6], phq[t][7]}), n);
      n = MFMA32(f2, asfrag((u32x4){phq[t][8], phq[t][9], 0u, 0u}), n);
      const float den = rbf(rbf(hh ? n[8] : n[12]) + 1e-6f);
      const float r = __builtin_amdgcn_rcpf(den);
      float o[12];
#pragma unroll
      for (int i = 0; i < 12; ++i) o[i] = rbf(n[i]) * r;
      store_o(ro, 4 * nt + hl, mt * 128 + rw0 + 32 * t + r32, hh, o);
    }
    STAMP(0, 9);
  }
  LBAR();
  STAMP(0, 10);
  {
    const rsrc_t rwo = mkrsrc(wog + nt * 160, 640 * 1280 - nt * 160);
    const int lq = (lane >= 10) + (lane >= 20) + (lane >= 30);
    const unsigned vwo = (unsigned)(lq * 1280 + (lane - 10 * lq) * 16);
    const int sg = 2 * (wid >> 2) + (wid & 1), st = (wid >> 1) & 1;
#define P_DMA(ks) do { const int w5 = (ks) % 5; const int o0 = w5 == 0 ? 0 : w5 == 1 ? 4 : w5 == 2 ? 16 : w5 == 3 ? 20 : 24; \
      dma16m<0xFFFFFFFFu, 0xFFu>(rwo, lds0 + (ks) * 5120 + wid * 640, vwo, (unsigned)(160 * ((ks) / 5) + o0 + 40 * sg + st * (w5 == 2 ? 20 : 8)) * 1280u); } while (0)
#pragma unroll
    for (int ks = 0; ks < 6; ++ks) P_DMA(ks);
    WAIT_BAR(6);
    if (wid == 0) st8c(rg, (lane >= 16 && lane < 24) ? vflag : OOB, gc);
#pragma unroll
    for (int ks = 6; ks < 20; ++ks) P_DMA(ks);
    STAMP(0, 11);
    wait_flags(rg, (unsigned)(WS_IN + me * 256 + 128 + (lane & 7) * 8), true, tag);
    STAMP(0, 12);
    const int pm = wid & 3, kh = wid >> 2;
    const rsrc_t rf = mkrsrc(ws + WS_O + (unsigned)((br * 128 + mt * 8 + 2 * pm) * 20 + 10 * kh) * 1024u, 30 * 1024);
    u32x4 xf[10][2];
#define P_LD(tau) do { xf[tau][0] = __builtin_amdgcn_raw_buffer_load_b128(rf, lane * 16, (tau) * 1024, 16); \
                       xf[tau][1] = __builtin_amdgcn_raw_buffer_load_b128(rf, lane * 16, ((tau) + 20) * 1024, 16); } while (0)
    P_LD(0); P_LD(1); P_LD(2);
    asm volatile("s_barrier" ::: "memory");
    const LAS char* wb = sm + kh * 51200 + (16 * (g >> 1) + 4 * (g & 1) + q4) * 160 + 8 * p4;
    f32x4 pc[2][5] = {};
    bf16x8 wf[2][5];
#pragma unroll
    for (int j = 0; j < 5; ++j) wf[0][j] = cat8(trread(wb + j * 32), trread(wb + 8 * 160 + j * 32));
#pragma unroll
    for (int tau = 0; tau < 10; ++tau) {
      SB();
      if (tau < 7) P_LD(tau + 3);
#pragma unroll
      for (int j = 0; j < 5; ++j) {
        if (tau < 9) wf[(tau + 1) & 1][j] = cat8(trread(wb + (tau + 1) * 5120 + j * 32), trread(wb + (tau + 1) * 5120 + 8 * 160 + j * 32));
        pc[0][j] = MFMA16(wf[tau & 1][j], asfrag(xf[tau][0]), pc[0][j]);
        pc[1][j] = MFMA16(wf[tau & 1][j], asfrag(xf[tau][1]), pc[1][j]);
        SB();
      }
    }
    STAMP(0, 13);
    if (tid == 0) st8c(rg, me * 16, tag);
#pragma unroll
    for (int j = 0; j < 5; ++j) *(LAS f32x4*)(smw + P_X + (wid * 5 + j) * 1024 + lane * 16) = kh ? pc[0][j] : pc[1][j];
    LBAR();
    LAS char* ow = smw + wid * 2816;
#pragma unroll
    for (int j = 0; j < 5; ++j) {
      const f32x4 o = *(const LAS f32x4*)(sm + P_X + ((wid ^ 4) * 5 + j) * 1024 + lane * 16);
      const f32x4 a = kh ? pc[1][j] : pc[0][j];
      *(LAS u32x2*)(ow + fr * 176 + j * 32 + g * 8) = (u32x2){pk2(a[0] + o[0], a[1] + o[1]), pk2(a[2] + o[2], a[3] + o[3])};
    }
    const rsrc_t rout = mkrsrc((char*)out + (size_t)(mt * 128 + 32 * pm + 16 * kh) * 2560 + br * 1280 + nt * 160, 15 * 2560 + 160);
#pragma unroll
    for (int i = 0; i < 3; ++i) {
      const int c = min(lane + 64 * i, 160), row = c / 10, ch = c - row * 10;
      const u32x4 v = *(const LAS u32x4*)(ow + row * 176 + ch * 16);
      __builtin_amdgcn_raw_buffer_store_b128(v, rout, (unsigned)(row * 2560 + ch * 16), 0, OUT_AUX);
    }
  }
  STAMP(0, 15);
  STAMP_FLUSH(0);
}

extern "C" void kernel_launch(void* const* d_in, const int* in_sizes, int n_in, void* d_out, int out_size, void* d_ws,
                              size_t ws_size, hipStream_t stream) {
  k1_fused<<<256, 512, 0, stream>>>((const __hip_bfloat16*)d_in[0], (const __hip_bfloat16*)d_in[1], (const __hip_bfloat16*)d_in[4],
                                    (const __hip_bfloat16*)d_in[5], (const __hip_bfloat16*)d_in[2], (const __hip_bfloat16*)d_in[3],
                                    (const __hip_bfloat16*)d_in[6], (char*)d_ws, (__hip_bfloat16*)d_out);
}
```

```cpp
#include <hip/hip_runtime.h>
#include <hip/hip_bf16.h>

typedef short bf16x8 __attribute__((ext_vector_type(8)));
typedef short s16x4 __attribute__((ext_vector_type(4)));
typedef float f32x2 __attribute__((ext_vector_type(2)));
typedef float f32x4 __attribute__((ext_vector_type(4)));
typedef float f32x16 __attribute__((ext_vector_type(16)));
typedef unsigned u32x2 __attribute__((ext_vector_type(2)));
typedef unsigned u32x4 __attribute__((ext_vector_type(4)));
typedef __bf16 bf16x2_t __attribute__((ext_vector_type(2)));
typedef __amdgpu_buffer_rsrc_t rsrc_t;
#define LAS __attribute__((address_space(3)))
#define GAS __attribute__((address_space(1)))
#define DI __device__ __forceinline__

#define PFV 6
#define OUT_AUX 0
#define TB 1
#define FB_HI 0x1p100f
#define CPF 1
constexpr int SEQ = 2048, DM = 640, NH = 32;
constexpr unsigned WS_G = 0;
constexpr unsigned WS_IN = 4096;
constexpr unsigned WS_KVH = WS_IN + 256 * 256;
constexpr unsigned WS_KVP = WS_KVH + 128 * 40960;
constexpr unsigned WS_O = WS_KVP + 128 * 6784;
constexpr unsigned WS_END = WS_O + 2 * SEQ * DM * 2;
constexpr unsigned OOB = 0x7FFF0000u;

DI unsigned pk2(float lo, float hi) {
  bf16x2_t r = __builtin_convertvector((f32x2){lo, hi}, bf16x2_t);
  return __builtin_bit_cast(unsigned, r);
}
DI float rbf(float x) { return __builtin_bit_cast(float, pk2(0.f, x)); }
DI float fmax3(float a, float b, float c) { return __builtin_elementwise_maximum(__builtin_elementwise_maximum(a, b), c); }
DI s16x4 trread(const LAS char* p) { return __builtin_amdgcn_ds_read_tr16_b64_v4i16((LAS s16x4*)p); }
DI bf16x8 cat8(s16x4 a, s16x4 b) { return __builtin_shufflevector(a, b, 0, 1, 2, 3, 4, 5, 6, 7); }
DI bf16x8 asfrag(u32x4 v) { return __builtin_bit_cast(bf16x8, v); }
DI bf16x8 frag2(u32x2 a, u32x2 b) { return asfrag((u32x4){a[0], a[1], b[0], b[1]}); }
DI rsrc_t mkrsrc(const void* p, unsigned bytes) { return __builtin_amdgcn_make_buffer_rsrc((void*)p, (short)0, (int)bytes, 0x00020000); }
DI void dma16(rsrc_t rs, unsigned lds, unsigned voff, unsigned soff) {
  asm volatile("s_mov_b32 m0, %0\n\ts_nop 0\n\tbuffer_load_dwordx4 %1, %2, %3 offen lds"
               :: "s"(lds), "v"(voff), "s"(rs), "s"(soff) : "memory");
}
DI void dma16c(rsrc_t rs, unsigned lds, unsigned voff, unsigned soff) {
  asm volatile("s_mov_b32 m0, %0\n\ts_nop 0\n\tbuffer_load_dwordx4 %1, %2, %3 offen sc1 lds"
               :: "s"(lds), "v"(voff), "s"(rs), "s"(soff) : "memory");
}
DI void touch(rsrc_t rs, unsigned lds, unsigned voff, unsigned soff) {
  asm volatile("s_mov_b32 m0, %0\n\ts_nop 0\n\tbuffer_load_dword %1, %2, %3 offen lds"
               :: "s"(lds), "v"(voff), "s"(rs), "s"(soff) : "memory");
}
template <unsigned LO, unsigned HI>
DI void dma16m(rsrc_t rs, unsigned lds, unsigned voff, unsigned soff) {
  asm volatile("s_mov_b32 m0, %0\n\ts_mov_b32 exec_lo, %4\n\ts_mov_b32 exec_hi, %5\n\tbuffer_load_dwordx4 %1, %2, %3 offen lds\n\ts_mov_b64 exec, -1"
               :: "s"(lds), "v"(voff), "s"(rs), "s"(soff), "i"(LO), "i"(HI) : "memory");
}
DI void ld8(u32x2& d, rsrc_t rs, unsigned voff, unsigned soff) {
  asm volatile("buffer_load_dwordx2 %0, %1, %2, %3 offen" : "=v"(d) : "v"(voff), "s"(rs), "s"(soff) : "memory");
}
DI void ld16(u32x4& d, rsrc_t rs, unsigned voff, unsigned soff) {
  asm volatile("buffer_load_dwordx4 %0, %1, %2, %3 offen" : "=v"(d) : "v"(voff), "s"(rs), "s"(soff) : "memory");
}
DI void ld16c(u32x4& d, rsrc_t rs, unsigned voff, unsigned soff) {
  asm volatile("buffer_load_dwordx4 %0, %1, %2, %3 offen sc1" : "=v"(d) : "v"(voff), "s"(rs), "s"(soff) : "memory");
}
DI void st16c(rsrc_t rs, unsigned voff, u32x4 v) { __builtin_amdgcn_raw_buffer_store_b128(v, rs, voff, 0, 16); }
DI void st8c(rsrc_t rs, unsigned voff, u32x2 v) { __builtin_amdgcn_raw_buffer_store_b64(v, rs, voff, 0, 16); }
DI u32x2 lds8(const LAS char* p) { return *(const volatile LAS u32x2*)p; }
DI u32x2 next_tag(u32x2 g) { return (u32x2){g[0] * 1664525u + 1013904223u, g[1] * 22695477u + 2654435761u}; }
#define MFMA16(a, b, c) __builtin_amdgcn_mfma_f32_16x16x32_bf16((a), (b), (c), 0, 0, 0)
#define MFMA32(a, b, c) __builtin_amdgcn_mfma_f32_32x32x16_bf16((a), (b), (c), 0, 0, 0)
#define MFMA32K8(a, b, c) __builtin_amdgcn_mfma_f32_32x32x8bf16_1k(__builtin_bit_cast(s16x4, a), __builtin_bit_cast(s16x4, b), (c), 0, 0, 0)
#define WAIT_BAR(N) asm volatile("s_waitcnt vmcnt(" #N ") lgkmcnt(0)\n\ts_barrier" ::: "memory")
#define LBAR() asm volatile("s_waitcnt lgkmcnt(0)\n\ts_barrier" ::: "memory")
#define SB() __builtin_amdgcn_sched_barrier(0)
#define PIN8(a) asm volatile("" : "+v"(a[0]), "+v"(a[1]), "+v"(a[2]), "+v"(a[3]), "+v"(a[4]), "+v"(a[5]), "+v"(a[6]), "+v"(a[7]))

#define STAMP_DECL
#define STAMP(kid, i)
#define STAMP_FLUSH(kid)

constexpr float LOG2E = 1.4426950408889634f;

struct WFrag { bf16x8 t0k0, t1k0; u32x2 t0k1, t1k1; };
DI WFrag load_wfrag(const LAS char* a0  , const LAS char* a1  , int hh, int lane) {
  u32x2 r0 = __builtin_bit_cast(u32x2, trread(a0)), r1 = __builtin_bit_cast(u32x2, trread(a0 + 160));
  u32x2 r2 = __builtin_bit_cast(u32x2, trread(a1)), r3 = __builtin_bit_cast(u32x2, trread(a1 + 160));
  u32x2 r4 = __builtin_bit_cast(u32x2, trread(a0 + 640)), r5 = __builtin_bit_cast(u32x2, trread(a1 + 640));
  const unsigned sg = (lane & 4) ? 0x80008000u : 0u;
  const unsigned km = hh ? 0u : 0xFFFFFFFFu;
  WFrag w;
  w.t0k0 = asfrag((u32x4){r0[0] ^ sg, r0[1] ^ sg, r1[0] ^ sg, r1[1] ^ sg});
  w.t1k0 = asfrag((u32x4){r2[0] ^ sg, r2[1] ^ sg, r3[0] ^ sg, r3[1] ^ sg});
  w.t0k1 = (u32x2){(r4[0] ^ sg) & km, (r4[1] ^ sg) & km};
  w.t1k1 = (u32x2){(r5[0] ^ sg) & km, (r5[1] ^ sg) & km};
  return w;
}
struct HProj { f32x16 p0, p1; };
struct HRow { bf16x8 x0; u32x2 x1; };
DI HRow hh_read(const LAS char* p, const LAS char* p4) { HRow r; r.x0 = frag2(lds8(p), lds8(p + 1024)); r.x1 = lds8(p4); return r; }
DI HProj hh_proj(const WFrag& w, const HRow& x) {
  const f32x16 z = {};
  HProj h;
  h.p0 = MFMA32(w.t0k0, x.x0, z);
  h.p1 = MFMA32(w.t1k0, x.x0, z);
  h.p0 = MFMA32K8(w.t0k1, x.x1, h.p0);
  h.p1 = MFMA32K8(w.t1k1, x.x1, h.p1);
  return h;
}
template <int N, int NV> DI void mix_mfma() {
#pragma unroll
  for (int i = 0; i < N; ++i) { __builtin_amdgcn_sched_group_barrier(0x8, 1, 0); __builtin_amdgcn_sched_group_barrier(0x402, NV, 0); }
}
DI void hh_soft(const HProj& h, unsigned (&ph)[10]) {
  f32x2 v[10];
#pragma unroll
  for (int i = 0; i < 8; ++i) v[i] = (f32x2){rbf(h.p0[2 * i]), rbf(h.p0[2 * i + 1])};
#pragma unroll
  for (int i = 0; i < 2; ++i) v[8 + i] = (f32x2){rbf(h.p1[2 * i]), rbf(h.p1[2 * i + 1])};
  float m = __builtin_elementwise_maximum(v[0][0], v[0][1]);
#pragma unroll
  for (int i = 1; i < 10; ++i) m = fmax3(m, v[i][0], v[i][1]);
  const float nm = -m * LOG2E;
#pragma unroll
  for (int i = 0; i < 10; ++i) {
    v[i] = __builtin_elementwise_fma(v[i], (f32x2){LOG2E, LOG2E}, (f32x2){nm, nm});
    v[i] = (f32x2){__builtin_amdgcn_exp2f(v[i][0]), __builtin_amdgcn_exp2f(v[i][1])};
  }
  const f32x2 s = (((v[0] + v[1]) + (v[2] + v[3])) + ((v[4] + v[5]) + (v[6] + v[7]))) + (v[8] + v[9]);
  const float r = __builtin_amdgcn_rcpf(s[0] + s[1]);
#pragma unroll
  for (int i = 0; i < 10; ++i) { v[i] *= (f32x2){r, r}; ph[i] = pk2(v[i][0], v[i][1]); }
}

constexpr float SC2 = 0.22360679774997896f * 1.4426950408889634f;

DI unsigned o_off(int h, int srow) { return (unsigned)((srow >> 4) * 20480 + (srow & 15) * 16 + 5 * (h >> 3) * 1024 + ((h >> 1) & 3) * 256); }
DI void store_o(rsrc_t ro, unsigned v16, unsigned v8, const float (&o)[12]) {
  st16c(ro, v16, (u32x4){pk2(o[0], o[1]), pk2(o[2], o[3]), pk2(o[4], o[5]), pk2(o[6], o[7])});
  st8c(ro, v8, (u32x2){pk2(o[8], o[9]), pk2(o[10], o[11])});
}
#define PIN(x) asm volatile("" : "+v"(x))
DI void wait_flags(rsrc_t rin, unsigned voff, bool need, u32x2 tag) {
#pragma unroll 1
  for (int spin = 0; spin < (1 << 20); ++spin) {
    u32x2 f;
    asm volatile("buffer_load_dwordx2 %0, %1, %2, 0 offen sc1\n\ts_waitcnt vmcnt(0)" : "=&v"(f) : "v"(voff), "s"(rin) : "memory");
    if (__all(!need || (f[0] == tag[0] && f[1] == tag[1]))) break;
    __builtin_amdgcn_s_sleep(1);
  }
}

constexpr unsigned CODE_BYTES = 20 * 1024;
constexpr int G_STAGE = 16384 + 30720;
constexpr int B_KP = 3072, B_KH = 5 * B_KP, B_VP = 3104, B_VH = 15616;
constexpr int B_K = 0, B_V = 4 * B_KH, B_ONE = B_V + 4 * B_VH + 160, L_Q = B_ONE + 3072 + 96;
constexpr int A_KP = 1024, A_KH = 5120, A_VP = 1056, A_VH = 5376;
constexpr int A_K = 0, A_V = 4 * A_KH, A_ONE = A_V + 4 * A_VH + 160, A_PHI = A_ONE + 1024 + 96, A_KV = 20 * 5120 + 256;
constexpr int L_W = L_Q + 20480, L_G = L_W + 4096, L_T = L_G + 1024, L_S = L_T + 256 + 16, K_LDS = L_S + 1024;
constexpr int P_X = 20 * 5120 + 256;
static_assert(A_PHI + 8 * 5632 + 64 <= A_KV && A_KV + 4 * 3584 <= L_Q && 3 * G_STAGE + 64 <= L_W && K_LDS <= 160 * 1024 - 2048 && P_X + 40960 <= L_W, "LDS map");
static_assert(B_V % 256 == 0 && B_VH % 256 == 0 && A_V % 256 == 0 && A_VH % 256 == 0 && L_Q % 16 == 0 && A_PHI % 16 == 0, "banks");
static_assert(2 * B_KH == 6 * 5120 && B_V == 12 * 5120 && 2 * B_VH >= 6 * 5120, "wo blocks 0-5 / 12-17 reuse the K / V planes of heads 0,1");

DI f32x16 qk_tile(const LAS char* kh, const LAS char* k4, bf16x8 q0, u32x2 q1, f32x16 c) {
  const u32x2 a = lds8(kh), b = lds8(kh + B_KP), d = lds8(k4);
  c = MFMA32(frag2(a, b), q0, c);
  return MFMA32K8(d, q1, c);
}
DI f32x16 pv_tile(const LAS char* vp, const f32x16& s, f32x16 o) {
  const bf16x8 v0 = cat8(trread(vp), trread(vp + 64)), v1 = cat8(trread(vp + 128), trread(vp + 192));
  unsigned pw[8];
#pragma unroll
  for (int i = 0; i < 8; ++i) pw[i] = pk2(__builtin_amdgcn_exp2f(s[2 * i]), __builtin_amdgcn_exp2f(s[2 * i + 1]));
  o = MFMA32(v0, asfrag((u32x4){pw[0], pw[1], pw[2], pw[3]}), o);
  return MFMA32(v1, asfrag((u32x4){pw[4], pw[5], pw[6], pw[7]}), o);
}
struct Strm { const LAS char* kh; const LAS char* k4; const LAS char* vp; bf16x8 q0; u32x2 q1; };
struct KFr { u32x2 a, b, d; };
DI KFr kread(const Strm& s, int off) { KFr k; k.a = lds8(s.kh + off); k.b = lds8(s.kh + off + B_KP); k.d = lds8(s.k4 + off); return k; }
DI void tile_step(f32x16& o, const f32x16& sc, const LAS char* vp, f32x16& sn, const Strm& n, KFr& k, const Strm& ks, int koff) {
  const f32x16 z = {};
  const bf16x8 v0 = cat8(trread(vp), trread(vp + 64)), v1 = cat8(trread(vp + 128), trread(vp + 192));
  float e[16];
#pragma unroll
  for (int i = 0; i < 3; ++i) e[i] = __builtin_amdgcn_exp2f(sc[i]);
  asm volatile("" : "+v"(e[0]), "+v"(e[1]), "+v"(e[2]));
  SB();
  sn = MFMA32(frag2(k.a, k.b), n.q0, z);
#pragma unroll
  for (int i = 3; i < 7; ++i) e[i] = __builtin_amdgcn_exp2f(sc[i]);
  asm volatile("" : "+v"(e[3]), "+v"(e[4]), "+v"(e[5]), "+v"(e[6]));
  SB();
  sn = MFMA32K8(k.d, n.q1, sn);
  k = kread(ks, koff);
#pragma unroll
  for (int i = 7; i < 10; ++i) e[i] = __builtin_amdgcn_exp2f(sc[i]);
  u32x4 p0 = {pk2(e[0], e[1]), pk2(e[2], e[3]), pk2(e[4], e[5]), pk2(e[6], e[7])};
  asm volatile("" : "+v"(e[8]), "+v"(e[9]), "+v"(p0));
  SB();
  o = MFMA32(v0, asfrag(p0), o);
#pragma unroll
  for (int i = 10; i < 16; ++i) e[i] = __builtin_amdgcn_exp2f(sc[i]);
  o = MFMA32(v1, asfrag((u32x4){pk2(e[8], e[9]), pk2(e[10], e[11]), pk2(e[12], e[13]), pk2(e[14], e[15])}), o);
  SB();
}
DI void run_tiles(f32x16& o, const Strm& s, const f32x16& c0, int off, int step, int n) {
  KFr k = kread(s, off + step);
  f32x16 sa = qk_tile(s.kh + off, s.k4 + off, s.q0, s.q1, c0), sb;
#pragma unroll 1
  for (;;) {
    tile_step(o, sa, s.vp + off, sb, s, k, s, off + 2 * step);
    if (n == 1) break;
    tile_step(o, sb, s.vp + off + step, sa, s, k, s, off + 3 * step);
    if (n == 2) break;
    n -= 2; off += 2 * step;
  }
}

__global__ __launch_bounds__(512, 2) void k1_fused(const __hip_bfloat16* __restrict__ xA, const __hip_bfloat16* __restrict__ wA,
                                                   const __hip_bfloat16* __restrict__ xB, const __hip_bfloat16* __restrict__ wB,
                                                   const __hip_bfloat16* __restrict__ wphi, const __hip_bfloat16* __restrict__ woA,
                                                   const __hip_bfloat16* __restrict__ woB, char* __restrict__ ws,
                                                   __hip_bfloat16* __restrict__ out) {
  __shared__ __attribute__((aligned(16))) char smem[K_LDS];
  const LAS char* sm = (const LAS char*)smem;
  LAS char* smw = (LAS char*)smem;
  const unsigned lds0 = (unsigned)(size_t)(LAS char*)smem;
  const int tid = threadIdx.x, lane = tid & 63;
  const int wid = __builtin_amdgcn_readfirstlane(tid >> 6);
  const int xcd = blockIdx.x & 7, idx = blockIdx.x >> 3;
  const int br = idx & 1, sub = idx >> 1;
  const int mt = (xcd >> 2) * 8 + (sub & 7), nt = (xcd & 3) * 2 + (sub >> 3);
  const int mt3 = xcd * 2 + (sub & 1), nt3 = sub >> 1;
  const int me = blockIdx.x;
  const int g = lane >> 4, fr = lane & 15, q4 = fr >> 2, p4 = lane & 3;
  STAMP_DECL;
  STAMP(0, 0);
  const char* wg = (const char*)(br ? wB : wA);
  const rsrc_t rx = mkrsrc((const char*)(br ? xB : xA) + (size_t)mt * 128 * 1280, 128 * 1280);
  const rsrc_t rw = mkrsrc(wg + nt * 160, 640 * 3840 - nt * 160);
  const unsigned vx0 = (unsigned)((2 * (lane >> 3)) * 1280 + (((lane & 7) ^ g) * 16));
  const unsigned vx1 = (unsigned)((16 + 2 * (lane >> 3)) * 1280 + (((lane & 7) ^ (4 + g)) * 16));
  const int wr = lane >= 30 ? 1 : 0, wc = lane - 30 * wr, wsg = (wc >= 10) + (wc >= 20);
  const unsigned vw = (unsigned)(wr * 3840 + wsg * 1280 + (wc - 10 * wsg) * 16);
  const unsigned sx = (unsigned)((32 * (wid >> 1) + (wid & 1)) * 1280);
  const unsigned sw = (unsigned)((16 * (wid >> 1) + 4 * (wid & 1)) * 3840);
  const unsigned lx = lds0 + wid * 2048, lw = lds0 + 16384 + wid * 3840;
#define G_DMA(i, bo, xo, wo) do { \
    if ((i) == 0 || (i) == 3) dma16(rx, lx + (bo) + ((i) ? 1024 : 0), (i) ? vx1 : vx0, (xo)); \
    else { const int n_ = (i) < 3 ? (i) - 1 : (i) - 2; \
      dma16m<0xFFFFFFFFu, 0x0FFFFFFFu>(rw, lw + (bo) + n_ * 960, vw, (wo) + ((n_ >> 1) * 8 + (n_ & 1) * 2) * 3840); } } while (0)
#pragma unroll
  for (int i = 0; i < 6; ++i) G_DMA(i, 0, sx, sw);
  const rsrc_t rt = (wid & 4) ? mkrsrc(wg, 640 * 3840) : rx;
  const unsigned tst = (wid & 4) ? 245760u : 128u;
  unsigned vt = (wid & 4) ? (fr < 15 ? (unsigned)(((mt * 8 + nt) * 15 + fr) * 128 + g * 245760) : OOB) : (unsigned)((nt * 16 + fr) * 1280 + g * 128);
  if ((wid & 3) == 3) touch(rt, lds0 + L_T, vt, tst);
  vt = g == 0 ? vt : OOB;

  const int wm = wid >> 2, wn = wid & 3;
  const LAS char* xa0 = sm + (64 * wm + fr) * 128 + ((g ^ (fr >> 1)) * 16);
  const LAS char* xa1 = sm + (((64 * wm + fr) * 128 + ((g ^ (fr >> 1)) * 16)) ^ 64);
  const LAS char* wa = sm + 16384 + (16 * (g >> 1) + 4 * (g & 1) + q4) * 480 + 8 * p4 + wn * 128;
  f32x4 acc[4][4] = {};
#pragma unroll
  for (int i = 0; i < 4; ++i) asm volatile("" : "+v"(acc[i][0]), "+v"(acc[i][1]), "+v"(acc[i][2]), "+v"(acc[i][3]));
  int eb[4];
  {
    const int kb = br ? B_K + 2048 : A_K, kp = br ? B_KP : A_KP, vb = br ? B_V + 2048 : A_V, vp = br ? B_VP : A_VP;
#pragma unroll
    for (int j = 0; j < 4; ++j) {
      const int qd = 16 * wn + 4 * j + g, r = qd - 40;
      const int v = vb + r * vp + ((r * 13) >> 6) * 96;
      eb[j] = (qd < 20 ? L_Q + qd * 1024 : qd < 40 ? kb + (qd - 20) * kp : qd < 60 ? v : L_S) + wm * 512 + fr * 16;
      asm volatile("" : "+v"(eb[j]));
    }
  }
  int cons;
  {
    const int cm = br ? mt + 1 + lane : lane;
    const int c1 = (((((nt & 1) * 8 + (cm & 7)) << 1) | br) << 3) | (((cm >> 3) & 1) * 4 + (nt >> 1));
    const int c2 = (((((lane & 7) * 2 + (mt & 1)) << 1) | br) << 3) | (mt >> 1);
    cons = lane < 16 ? ((cm < 16 && (!br || lane < 2)) ? c1 : -1) : (lane < 24 ? c2 : lane == 63 ? me : -1);
    asm volatile("" : "+v"(cons));
  }
  const int hh = lane >> 5, r32 = lane & 31, b16 = (lane >> 4) & 1, vq = 4 * b16 + p4;
  unsigned vflag = (unsigned)(WS_IN + cons * 256 + (lane >= 16 ? 16 + nt : br ? 1 - lane : mt) * 8);
  unsigned vf1 = (lane < 16 && cons >= 0) ? vflag : OOB, vf2 = (lane >= 16 && lane < 24) ? vflag : OOB;
  const int lq = (lane >= 10) + (lane >= 20) + (lane >= 30);
  unsigned vwo = (unsigned)(lq * 1280 + (lane - 10 * lq) * 16);
  PIN(vf1); PIN(vf2); PIN(vwo);
  const int hs = wid >> 2, qi = hs ? 3 - (wid & 3) : (wid & 3);
  const int hl = wid >> 1, rw0 = 64 * (wid & 1);
  int c0, c1, c2, c3, c4, c5, c6;
  unsigned v16, v8;
  if (br) {
    c0 = B_K + hs * B_KH + hh * 2 * B_KP + r32 * 8;
    c1 = B_K + hs * B_KH + 4 * B_KP + r32 * 8;
    c2 = (vq < 5 ? B_V + hs * B_VH + vq * B_VP : B_ONE) + (4 * hh + q4) * 8;
    c3 = c2 + (vq < 5 ? 2 * B_VH : 0);
    c4 = L_Q + hs * 5120 + (32 * qi + r32) * 8;
    c5 = L_Q + (2 + hs) * 5120 + (32 * (3 - qi) + r32) * 8;
    c6 = hh * 2048;
    v16 = o_off(4 * nt + hs, mt * 128 + 32 * qi + r32);
  } else {
    c0 = L_W + hl * 800 + (8 * hh + q4) * 40 + (2 * b16 + (p4 >> 1)) * 8;
    c1 = L_W + hl * 800 + (8 * hh + q4) * 40 + 32;
    c2 = hl * 5120 + (rw0 + r32) * 8;
    c3 = A_PHI + wid * 5632 + r32 * 88 + hh * 40;
    c4 = A_PHI + wid * 5632 + (8 * hh + q4) * 88 + (16 * b16 + 4 * p4) * 2;
    c5 = (vq < 5 ? A_V + hl * A_VH + vq * A_VP : A_ONE) + (rw0 + 8 * hh + q4) * 8;
    c6 = A_PHI + wid * 5632 + min(r32, 21) * 160 + hh * 16;
    v16 = o_off(4 * nt + hl, mt * 128 + rw0 + r32);
  }
  {
    const int odd = br ? hs : (hl & 1);
    v8 = hh ? OOB : (v16 & ~1023u) + 2048 + 512 * odd + ((v16 & 1023u) >> 1);
    v16 += (hh + 3 * odd) * 1024;
  }
  PIN(c0); PIN(c1); PIN(c2); PIN(c3); PIN(c4); PIN(c5); PIN(c6); PIN(v16); PIN(v8);
  f32x16 mle, mgt;
#pragma unroll
  for (int e = 0; e < 16; ++e) {
    const bool le = (e & 3) + 8 * (e >> 2) + 4 * hh <= r32;
    mle[e] = le ? 0.f : -INFINITY;
    mgt[e] = le ? -INFINITY : 0.f;
  }
  PIN(mle); PIN(mgt);
  const rsrc_t rg = mkrsrc(ws + WS_G, WS_KVH);
  const rsrc_t rph = mkrsrc((const char*)wphi + nt * 3200, 3200);
  bf16x8 x0[4], x1[4];
  s16x4 w0[4][2], w1[4][2];
#define G_RD(X, W, xa, kk, r) do { \
    if ((r) < 2) W[0][(r)] = trread(wa + ((kk) * 32 + (r) * 8) * 480); \
    else if ((r) < 6) X[(r) - 2] = *(const LAS bf16x8*)(xa + ((r) - 2) * 2048); \
    else if ((r) < 12) W[((r) - 4) >> 1][(r) & 1] = trread(wa + ((kk) * 32 + ((r) & 1) * 8) * 480 + (((r) - 4) >> 1) * 32); } while (0)
#define G_MM(X, W, m) acc[(m) & 3][(m) >> 2] = MFMA16(cat8(W[(m) >> 2][0], W[(m) >> 2][1]), X[(m) & 3], acc[(m) & 3][(m) >> 2])
  asm volatile("s_barrier" ::: "memory");
#pragma unroll
  for (int i = 0; i < 6; ++i) G_DMA(i, G_STAGE, sx + 128, sw + 245760);
  if ((wid & 3) == 3) { WAIT_BAR(7); } else { WAIT_BAR(6); }
  STAMP(0, 1);
#pragma unroll
  for (int r = 0; r < 12; ++r) G_RD(x0, w0, xa0, 0, r);
  int bo = 2 * G_STAGE;
  int bd = bo;
  unsigned xo = sx + 256, wo = sw + 2 * 245760;
#pragma unroll
  for (int i = 0; i < 3; ++i) G_DMA(i, bo, xo, wo);
  unsigned long long pc;
  asm volatile("s_getpc_b64 %0" : "=s"(pc));
#pragma unroll 1
  for (int t = 0; t < 10; ++t) {
    SB();
#pragma unroll
    for (int m = 0; m < 12; ++m) {
      G_MM(x0, w0, m); G_RD(x1, w1, xa1, 1, m);
      if (m % 3 == 2 && m < 9) G_DMA(3 + m / 3, bd, xo, wo);
      SB();
    }
    if (wn < 3) { G_MM(x0, w0, 12); G_MM(x0, w0, 13); G_MM(x0, w0, 14); G_MM(x0, w0, 15); }
    const int d = bo == G_STAGE ? -2 * G_STAGE : G_STAGE;
    xa0 += d; xa1 += d; wa += d;
    bo = bo == 2 * G_STAGE ? 0 : bo + G_STAGE;
    bd = t < 7 ? bo : 0x30000;
    xo = t < 7 ? xo + 128 : OOB;
    wo += 245760;
    WAIT_BAR(6);
    SB();
#pragma unroll
    for (int m = 0; m < 12; ++m) {
      G_MM(x1, w1, m); G_RD(x0, w0, xa0, 0, m);
      if (m % 3 == 2 && m < 9) G_DMA(m / 3, bd, xo, wo);
      SB();
    }
    if (wn < 3) { G_MM(x1, w1, 12); G_MM(x1, w1, 13); G_MM(x1, w1, 14); G_MM(x1, w1, 15); }
    if (t < 5) {
      if ((wid & 3) == 3) touch(rt, lds0 + L_T, vt, (5 + t) * tst);
      if (wid == 1) touch(mkrsrc(br ? woB : woA, 640 * 1280), lds0 + L_T, lane < 10 ? (unsigned)(((mt * 8 + nt) * 50 + lane) * 128) : OOB, t * 1280);
      if (t == TB) {
        if (wid == 0) dma16(rg, lds0 + L_G, cons >= 0 ? cons * 16 : OOB, 0);
        if (wid == 4) touch(mkrsrc((const void*)pc, CODE_BYTES), lds0 + L_T, lane < 8 ? (unsigned)((idx * 8 + lane) * 128) : OOB, 0);
        if (wid == 5 && !br) {
#pragma unroll
          for (int i = 0; i < 4; ++i) dma16(rph, lds0 + L_W + i * 1024, (unsigned)lane * 16, i * 1024);
        }
      }
    }
  }
  STAMP(0, 2);
  {
    const rsrc_t rh = mkrsrc(ws + WS_KVH + (mt * 8 + nt) * 40960, (br && mt < 15) ? 40960 : 0);
    const unsigned v0 = (unsigned)((16 * wn + g - 20) * 1024 + wm * 512 + fr * 16);
#pragma unroll
    for (int j = 3; j >= 0; --j) {
      if (br && 4 * wn + j < 5) {
#pragma unroll
        for (int i = 0; i < 4; ++i) acc[i][j] *= SC2;
      }
#pragma unroll
      for (int i = 0; i < 2; ++i) {
        const f32x4 a = acc[2 * i][j], b = acc[2 * i + 1][j];
        const u32x4 ev = (u32x4){pk2(a[0], a[1]), pk2(a[2], a[3]), pk2(b[0], b[1]), pk2(b[2], b[3])};
        unsigned vo = v0 + j * 4096 + i * 256;
        asm volatile("" : "+v"(vo));
        st16c(rh, vo, ev);
        *(LAS u32x4*)(smw + eb[j] + i * 256) = ev;
      }
    }
  }
  if (tid < 384) *(LAS u32x2*)(smw + (br ? B_ONE : A_ONE) + (br ? tid : tid & 127) * 8) = (u32x2){0x3F80u, 0u};
  const u32x2 gc = next_tag(*(const LAS u32x2*)(sm + L_G + lane * 16));
  const u32x2 tag = {(unsigned)__builtin_amdgcn_readlane(gc[0], 63), (unsigned)__builtin_amdgcn_readlane(gc[1], 63)};
  const char* wog = (const char*)(br ? woB : woA);
  const rsrc_t ro = mkrsrc(ws + WS_O + br * (SEQ * DM * 2), SEQ * DM * 2);
  const rsrc_t rwo = mkrsrc(wog + nt3 * 160, 640 * 1280 - nt3 * 160);
  const int sg = 2 * (wid >> 2) + (wid & 1), st = (wid >> 1) & 1;
#define P_DMA(ks) do { const int w5 = (ks) % 5; const int o0 = w5 == 0 ? 0 : w5 == 1 ? 4 : w5 == 2 ? 16 : w5 == 3 ? 20 : 24; \
      dma16m<0xFFFFFFFFu, 0xFFu>(rwo, lds0 + (ks) * 5120 + wid * 640, vwo, (unsigned)(160 * ((ks) / 5) + o0 + 40 * sg + st * (w5 == 2 ? 20 : 8)) * 1280u); } while (0)
  if (br) {
    WAIT_BAR(0);
    if (wid == 0) st8c(rg, vf1, gc);
    STAMP(0, 3);
    Strm S[2];
#pragma unroll
    for (int r = 0; r < 2; ++r) {
      S[r].kh = sm + c0 + r * 2 * B_KH;
      S[r].k4 = sm + c1 + r * 2 * B_KH;
      S[r].vp = sm + (r ? c3 : c2);
      const LAS char* qp = sm + (r ? c5 : c4);
      S[r].q0 = frag2(lds8(qp + c6), lds8(qp + c6 + 1024));
      const u32x2 t4 = lds8(qp + 4096);
      S[r].q1 = hh ? (u32x2){0u, 0u} : t4;
    }
    f32x16 o0 = {}, o1 = {};
    run_tiles(o0, S[0], mle, (qi + 8) * 256, -256, qi + 1);
    STAMP(0, 4);
    {
      const int nb = qi < 2 ? 1 : 0, pg = 2 * hs + (qi & 1);
      const bool have = mt - 2 + nb >= 0;
      wait_flags(rg, (unsigned)(WS_IN + me * 256 + nb * 8), have, tag);
      STAMP(0, 5);
      const rsrc_t rn = mkrsrc(ws + WS_KVH + ((have ? mt - 2 + nb : 0) * 8 + nt) * 40960 + pg * 10240, have ? 10240 : 0);
      const unsigned hd = lds0 + (hs ? B_V + (qi & 1) * 2 * B_VH : B_K + (qi & 1) * 2 * B_KH) + nb * 1024, hps = hs ? B_VP : B_KP;
#pragma unroll
      for (int i = 0; i < 10; ++i) dma16c(rn, hd + i * hps + (i >= 5 ? hs * 96 : 0), lane * 16, i * 1024);
    }
    run_tiles(o1, S[1], mle, (11 - qi) * 256, -256, 4 - qi);
    STAMP(0, 6);
    if (mt > 0) {
      if (qi < 2) { WAIT_BAR(0); } else { LBAR(); }
      STAMP(0, 7);
      {
        const f32x16 z = {};
        KFr k = kread(S[1], 1024);
        f32x16 sa = qk_tile(S[0].kh + 1024, S[0].k4 + 1024, S[0].q0, S[0].q1, z), sb;
#pragma unroll 1
        for (int off = 1024; off < 2048; off += 256) {
          tile_step(o0, sa, S[0].vp + off, sb, S[1], k, S[0], off + 256);
          tile_step(o1, sb, S[1].vp + off, sa, S[0], k, S[1], off + 256);
        }
      }
      STAMP(0, 8);
      if (mt > 1) { WAIT_BAR(0); }
    }
#pragma unroll
    for (int r = 0; r < 2; ++r) {
      const int qt = r ? 3 - qi : qi;
      f32x16& o = r ? o1 : o0;
      if (mt > 1) run_tiles(o, S[r], mgt, qt * 256, 256, 4 - qt);
      float l = __uint_as_float(__builtin_amdgcn_permlane32_swap(__float_as_uint(o[8]), __float_as_uint(o[8]), false, false)[1]);
      {
        float am = __builtin_fabsf(o[0]);
#pragma unroll
        for (int i = 1; i < 11; i += 2) am = fmax3(am, __builtin_fabsf(o[i]), __builtin_fabsf(o[i + 1]));
        am = __builtin_elementwise_maximum(am, __builtin_fabsf(o[11]));
        const bool ok = l >= 0x1p-80f && l <= FB_HI && am <= 0x1p126f;
        if (__builtin_expect(!__all(ok), 0)) {
          const int tw0 = mt >= 2 ? qt : mt == 1 ? 4 : 8;
          float m = -INFINITY;
#pragma unroll 1
          for (int pass = 0; pass < 2; ++pass) {
            o = (f32x16){};
#pragma unroll 1
            for (int w = tw0; w <= qt + 8; ++w) {
              f32x16 c;
#pragma unroll
              for (int e = 0; e < 16; ++e) c[e] = w == qt + 8 ? mle[e] : w == qt ? mgt[e] : 0.f;
              f32x16 s = qk_tile(S[r].kh + w * 256, S[r].k4 + w * 256, S[r].q0, S[r].q1, c);
              if (pass == 0) {
#pragma unroll
                for (int e = 0; e < 16; e += 2) m = fmax3(m, s[e], s[e + 1]);
              } else {
#pragma unroll
                for (int e = 0; e < 16; ++e) s[e] -= m;
                o = pv_tile(S[r].vp + w * 256, s, o);
              }
            }
            auto rr = __builtin_amdgcn_permlane32_swap(__float_as_uint(m), __float_as_uint(m), false, false);
            m = __builtin_elementwise_maximum(__uint_as_float(rr[0]), __uint_as_float(rr[1]));
          }
          l = __uint_as_float(__builtin_amdgcn_permlane32_swap(__float_as_uint(o[8]), __float_as_uint(o[8]), false, false)[1]);
        }
      }
      const float rl = __builtin_amdgcn_rcpf(l);
      float of[12];
#pragma unroll
      for (int i = 0; i < 12; ++i) of[i] = o[i] * rl;
      const unsigned dv = r ? (unsigned)((3 - 2 * qi) * 40960 + 256) : 0u;
      store_o(ro, v16 + dv, hh ? OOB : v8 + dv - (r ? 128 : 0), of);
    }
    STAMP(0, 9);
    WAIT_BAR(4);
    STAMP(0, 10);
#pragma unroll
    for (int ks = 0; ks < 6; ++ks) P_DMA(ks);
    WAIT_BAR(6);
  } else {
    WFrag wf = load_wfrag(sm + c0, sm + c1, hh, lane);
    PIN(wf.t0k0); PIN(wf.t1k0); PIN(wf.t0k1); PIN(wf.t1k1);
    if (tid >= 256) *(LAS u32x2*)(smw + A_KV + ((tid - 256) >> 1) * 112 + (tid & 1) * 48 + 40) = (u32x2){0u, 0u};
    LBAR();
    STAMP(0, 3);
    const LAS char* xp = sm + c2 + hh * 2048;
    const LAS char* x4 = sm + c2 + 4096;
    const HRow rk0 = hh_read(xp + A_K, x4 + A_K), rk1 = hh_read(xp + A_K + 256, x4 + A_K + 256);
    const HRow rq0 = hh_read(xp + L_Q, x4 + L_Q), rq1 = hh_read(xp + L_Q + 256, x4 + L_Q + 256);
    const HProj hk0 = hh_proj(wf, rk0);
    bf16x8 vf[4];
#pragma unroll
    for (int ks = 0; ks < 4; ++ks) vf[ks] = cat8(trread(sm + c5 + ks * 128), trread(sm + c5 + ks * 128 + 32));
    SB();
    f32x16 d0 = {}, d1 = {};
    HProj hq0, hq1;
    {
      unsigned ph[10];
      HProj hk1 = hh_proj(wf, rk1);
      hh_soft(hk0, ph);
#pragma unroll
      for (int i = 0; i < 5; ++i) *(LAS u32x2*)(smw + c3 + 8 * i) = (u32x2){ph[2 * i], ph[2 * i + 1]};
      PIN(hk1.p0); PIN(hk1.p1);
      mix_mfma<4, 8>();
      SB();
      bf16x8 pf[4];
#pragma unroll
      for (int i = 0; i < 4; ++i) pf[i] = cat8(trread(sm + c4 + (i >> 1) * 16 * 88 + (i & 1) * 64), trread(sm + c4 + ((i >> 1) * 16 + 4) * 88 + (i & 1) * 64));
      SB();
      hq0 = hh_proj(wf, rq0);
      d0 = MFMA32(pf[0], vf[0], d0); d1 = MFMA32(pf[1], vf[0], d1);
      d0 = MFMA32(pf[2], vf[1], d0); d1 = MFMA32(pf[3], vf[1], d1);
      hh_soft(hk1, ph);
#pragma unroll
      for (int i = 0; i < 5; ++i) *(LAS u32x2*)(smw + c3 + 32 * 88 + 8 * i) = (u32x2){ph[2 * i], ph[2 * i + 1]};
      PIN(hq0.p0); PIN(hq0.p1); PIN(d0); PIN(d1);
      mix_mfma<8, 8>();
      SB();
#pragma unroll
      for (int i = 0; i < 4; ++i) pf[i] = cat8(trread(sm + c4 + (32 + (i >> 1) * 16) * 88 + (i & 1) * 64), trread(sm + c4 + (36 + (i >> 1) * 16) * 88 + (i & 1) * 64));
      d0 = MFMA32(pf[0], vf[2], d0); d1 = MFMA32(pf[1], vf[2], d1);
      d0 = MFMA32(pf[2], vf[3], d0); d1 = MFMA32(pf[3], vf[3], d1);
    }
    *(LAS f32x4*)(smw + c6) = (f32x4){d0[0], d0[1], d0[2], d0[3]};
    *(LAS f32x4*)(smw + c6 + 32) = (f32x4){d0[4], d0[5], d0[6], d0[7]};
    *(LAS f32x4*)(smw + c6 + 64) = (f32x4){d0[8], d0[9], d0[10], d0[11]};
    *(LAS f32x4*)(smw + c6 + 96) = (f32x4){d0[12], d0[13], d0[14], d0[15]};
    *(LAS f32x4*)(smw + c6 + 128) = (f32x4){d1[0], d1[1], d1[2], d1[3]};
    SB();
    hq1 = hh_proj(wf, rq1);
    PIN(hq1.p0); PIN(hq1.p1);
    LBAR();
    STAMP(0, 4);
    const rsrc_t rkv = mkrsrc(ws + WS_KVP + nt * 16 * 6784, 16 * 6784);
    {
      const int e = (wid & 1) * 64 + lane;
      const LAS char* o = sm + A_PHI + (wid & ~1) * 5632 + e * 32;
      const f32x4 a = *(const LAS f32x4*)o + *(const LAS f32x4*)(o + 5632), b = *(const LAS f32x4*)(o + 16) + *(const LAS f32x4*)(o + 5648);
      st16c(rkv, e < 105 ? (unsigned)(mt * 6784 + hl * 1680 + e * 16) : OOB, (u32x4){pk2(a[0], a[1]), pk2(a[2], a[3]), pk2(b[0], b[1]), pk2(b[2], b[3])});
    }
    SB();
    unsigned phq[2][10];
    hh_soft(hq0, phq[0]);
#pragma unroll
    for (int i = 0; i < 10; ++i) PIN(phq[0][i]);
    WAIT_BAR(0);
    if (wid == 0) st8c(rg, vf1, gc);
#pragma unroll
    for (int ks = 0; ks < 8; ++ks) P_DMA(ks);
    STAMP(0, 5);
    hh_soft(hq1, phq[1]);
#pragma unroll
    for (int i = 0; i < 10; ++i) PIN(phq[1][i]);
    int kd0, kd1;
    {
      const int row = tid / 5, oc = tid - row * 5, h4 = row / 21, d = row - h4 * 21;
      kd0 = tid < 420 ? A_KV + h4 * 3584 + d * 112 + oc * 16 + (oc > 2 ? 8 : 0) : L_S;
      kd1 = kd0 + (oc == 2 ? 16 : 8);
      PIN(kd0); PIN(kd1);
    }
    wait_flags(rg, (unsigned)(WS_IN + me * 256 + fr * 8), true, tag);
    STAMP(0, 6);
    if (wid < 7) {
      u32x4 p[16];
#pragma unroll
      for (int i = 0; i < 16; ++i) p[i] = __builtin_amdgcn_raw_buffer_load_b128(rkv, tid * 16, i * 6784, 16);
      f32x2 lo[2] = {}, hi[2] = {};
#pragma unroll
      for (int i = 0; i < 16; ++i)
#pragma unroll
        for (int u = 0; u < 2; ++u) {
          lo[u] += (f32x2){__uint_as_float(p[i][2 * u] << 16), __uint_as_float(p[i][2 * u + 1] << 16)};
          hi[u] += (f32x2){__uint_as_float(p[i][2 * u] & 0xFFFF0000u), __uint_as_float(p[i][2 * u + 1] & 0xFFFF0000u)};
        }
      *(LAS u32x2*)(smw + kd0) = (u32x2){pk2(lo[0][0], hi[0][0]), pk2(lo[0][1], hi[0][1])};
      *(LAS u32x2*)(smw + kd1) = (u32x2){pk2(lo[1][0], hi[1][0]), pk2(lo[1][1], hi[1][1])};
    }
#pragma unroll
    for (int ks = 8; ks < 14; ++ks) P_DMA(ks);
    LBAR();
    STAMP(0, 7);
    const LAS char* kp = sm + A_KV + hl * 3584 + (r32 == 24 ? 20 : r32) * 112 + hh * 48;
    const bf16x8 f0 = *(const LAS bf16x8*)(kp), f1 = *(const LAS bf16x8*)(kp + 16), f2 = *(const LAS bf16x8*)(kp + 32);
    f32x16 n[2];
#pragma unroll
    for (int t = 0; t < 2; ++t) n[t] = MFMA32(f0, asfrag((u32x4){phq[t][0], phq[t][1], phq[t][2], phq[t][3]}), (f32x16){});
#pragma unroll
    for (int t = 0; t < 2; ++t) n[t] = MFMA32(f1, asfrag((u32x4){phq[t][4], phq[t][5], phq[t][6], phq[t][7]}), n[t]);
#pragma unroll
    for (int t = 0; t < 2; ++t) n[t] = MFMA32(f2, asfrag((u32x4){phq[t][8], phq[t][9], 0u, 0u}), n[t]);
#pragma unroll
    for (int t = 0; t < 2; ++t) {
      const float den = rbf(rbf(hh ? n[t][8] : n[t][12]) + 1e-6f);
      const float r = __builtin_amdgcn_rcpf(den);
      float o[12];
#pragma unroll
      for (int i = 0; i < 12; ++i) o[i] = rbf(n[t][i]) * r;
      store_o(ro, v16 + t * 40960, hh ? OOB : v8 + t * 40960, o);
    }
    STAMP(0, 10);
#pragma unroll
    for (int ks = 14; ks < 20; ++ks) P_DMA(ks);
    WAIT_BAR(6);
  }
  {
    if (wid == 0) st8c(rg, vf2, gc);
    if (br) {
#pragma unroll
      for (int ks = 6; ks < 20; ++ks) P_DMA(ks);
    }
    STAMP(0, 11);
    int oa[3];
    unsigned og[3];
#pragma unroll
    for (int i = 0; i < 3; ++i) {
      const int c = min(lane + 64 * i, 160), row = c / 10, ch = c - row * 10;
      oa[i] = wid * 2816 + row * 176 + ch * 16;
      og[i] = (unsigned)(row * 2560 + ch * 16);
      PIN(oa[i]); PIN(og[i]);
    }
    const int pm = wid & 3, kh = wid >> 2;
    wait_flags(rg, (unsigned)(WS_IN + me * 256 + 128 + (4 * kh + (lane & 3)) * 8), true, tag);
    STAMP(0, 12);
    const rsrc_t rf = mkrsrc(ws + WS_O + (unsigned)((br * 128 + mt3 * 8 + 2 * pm) * 20 + 10 * kh) * 1024u, 30 * 1024);
    const unsigned vk = lane * 16 + kh * 20480, vh = lane * 16 + (1 - kh) * 20480;
    u32x4 xf[10][2];
    const unsigned vk8 = lane * 8 + kh * 20480, vh8 = lane * 8 + (1 - kh) * 20480;
#define P_LD1(v, v8_, tau) ((tau) % 5 == 2 ? __builtin_bit_cast(u32x4, frag2(__builtin_amdgcn_raw_buffer_load_b64(rf, v8_, (tau) * 1024, 16), \
                                                                             __builtin_amdgcn_raw_buffer_load_b64(rf, v8_, (tau) * 1024 + 512, 16))) \
                                           : __builtin_amdgcn_raw_buffer_load_b128(rf, v, (tau) * 1024, 16))
#define P_LD(tau) do { xf[tau][0] = P_LD1(vk, vk8, tau); xf[tau][1] = P_LD1(vh, vh8, tau); } while (0)
    P_LD(0); P_LD(1); P_LD(2);
    asm volatile("s_barrier" ::: "memory");
    const LAS char* wb = sm + kh * 51200 + (16 * (g >> 1) + 4 * (g & 1) + q4) * 160 + 8 * p4;
    f32x4 pc[2][5] = {};
    bf16x8 wf[2][5];
#pragma unroll
    for (int j = 0; j < 5; ++j) wf[0][j] = cat8(trread(wb + j * 32), trread(wb + 8 * 160 + j * 32));
#pragma unroll
    for (int tau = 0; tau < 10; ++tau) {
      SB();
      if (tau < 7) P_LD(tau + 3);
#pragma unroll
      for (int j = 0; j < 5; ++j) {
        if (tau < 9) wf[(tau + 1) & 1][j] = cat8(trread(wb + (tau + 1) * 5120 + j * 32), trread(wb + (tau + 1) * 5120 + 8 * 160 + j * 32));
        pc[1][j] = MFMA16(wf[tau & 1][j], asfrag(xf[tau][1]), pc[1][j]);
        if (tau < 9) pc[0][j] = MFMA16(wf[tau & 1][j], asfrag(xf[tau][0]), pc[0][j]);
        SB();
      }
    }
#pragma unroll
    for (int j = 0; j < 5; ++j) {
      pc[0][j] = MFMA16(wf[1][j], asfrag(xf[9][0]), pc[0][j]);
      *(LAS f32x4*)(smw + P_X + (wid * 5 + j) * 1024 + lane * 16) = pc[1][j];
    }
    STAMP(0, 13);
    LBAR();
    if (tid == 0) st8c(rg, me * 16, tag);
    LAS char* ow = smw + wid * 2816 + fr * 176 + g * 8;
#pragma unroll
    for (int j = 0; j < 5; ++j) {
      const f32x4 a = pc[0][j] + *(const LAS f32x4*)(sm + P_X + ((wid ^ 4) * 5 + j) * 1024 + lane * 16);
      *(LAS u32x2*)(ow + j * 32) = (u32x2){pk2(a[0], a[1]), pk2(a[2], a[3])};
    }
    const rsrc_t rout = mkrsrc((char*)out + (size_t)(mt3 * 128 + 32 * pm + 16 * kh) * 2560 + br * 1280 + nt3 * 160, 15 * 2560 + 160);
#pragma unroll
    for (int i = 0; i < 3; ++i) __builtin_amdgcn_raw_buffer_store_b128(*(const LAS u32x4*)(sm + oa[i]), rout, og[i], 0, OUT_AUX);
  }
  STAMP(0, 15);
  STAMP_FLUSH(0);
}

extern "C" void kernel_launch(void* const* d_in, const int* in_sizes, int n_in, void* d_out, int out_size, void* d_ws,
                              size_t ws_size, hipStream_t stream) {
  k1_fused<<<256, 512, 0, stream>>>((const __hip_bfloat16*)d_in[0], (const __hip_bfloat16*)d_in[1], (const __hip_bfloat16*)d_in[4],
                                    (const __hip_bfloat16*)d_in[5], (const __hip_bfloat16*)d_in[2], (const __hip_bfloat16*)d_in[3],
                                    (const __hip_bfloat16*)d_in[6], (char*)d_ws, (__hip_bfloat16*)d_out);
}
```

```cpp
#include <hip/hip_runtime.h>
#include <hip/hip_bf16.h>

typedef short bf16x8 __attribute__((ext_vector_type(8)));
typedef short s16x4 __attribute__((ext_vector_type(4)));
typedef float f32x2 __attribute__((ext_vector_type(2)));
typedef float f32x4 __attribute__((ext_vector_type(4)));
typedef float f32x16 __attribute__((ext_vector_type(16)));
typedef unsigned u32x2 __attribute__((ext_vector_type(2)));
typedef unsigned u32x4 __attribute__((ext_vector_type(4)));
typedef __bf16 bf16x2_t __attribute__((ext_vector_type(2)));
typedef __amdgpu_buffer_rsrc_t rsrc_t;
#define LAS __attribute__((address_space(3)))
#define GAS __attribute__((address_space(1)))
#define DI __device__ __forceinline__

#define PFV 6
#define OUT_AUX 0
#define TB 0
#define FB_HI 0x1p100f
#define CPF 1
constexpr int SEQ = 2048, DM = 640, NH = 32;
constexpr unsigned WS_G = 0;
constexpr unsigned WS_IN = 4096;
constexpr unsigned WS_KVH = WS_IN + 256 * 256;
constexpr unsigned WS_KVP = WS_KVH + 128 * 40960;
constexpr unsigned WS_O = WS_KVP + 128 * 6784;
constexpr unsigned WS_END = WS_O + 2 * SEQ * DM * 2;
constexpr unsigned OOB = 0x7FFF0000u;

DI unsigned pk2(float lo, float hi) {
  bf16x2_t r = __builtin_convertvector((f32x2){lo, hi}, bf16x2_t);
  return __builtin_bit_cast(unsigned, r);
}
DI float rbf(float x) { return __builtin_bit_cast(float, pk2(0.f, x)); }
DI float fmax3(float a, float b, float c) { return __builtin_elementwise_maximum(__builtin_elementwise_maximum(a, b), c); }
DI s16x4 trread(const LAS char* p) { return __builtin_amdgcn_ds_read_tr16_b64_v4i16((LAS s16x4*)p); }
DI bf16x8 cat8(s16x4 a, s16x4 b) { return __builtin_shufflevector(a, b, 0, 1, 2, 3, 4, 5, 6, 7); }
DI bf16x8 asfrag(u32x4 v) { return __builtin_bit_cast(bf16x8, v); }
DI bf16x8 frag2(u32x2 a, u32x2 b) { return asfrag((u32x4){a[0], a[1], b[0], b[1]}); }
DI rsrc_t mkrsrc(const void* p, unsigned bytes) { return __builtin_amdgcn_make_buffer_rsrc((void*)p, (short)0, (int)bytes, 0x00020000); }
DI void dma16(rsrc_t rs, unsigned lds, unsigned voff, unsigned soff) {
  asm volatile("s_mov_b32 m0, %0\n\ts_nop 0\n\tbuffer_load_dwordx4 %1, %2, %3 offen lds"
               :: "s"(lds), "v"(voff), "s"(rs), "s"(soff) : "memory");
}
DI void dma16c(rsrc_t rs, unsigned lds, unsigned voff, unsigned soff) {
  asm volatile("s_mov_b32 m0, %0\n\ts_nop 0\n\tbuffer_load_dwordx4 %1, %2, %3 offen sc1 lds"
               :: "s"(lds), "v"(voff), "s"(rs), "s"(soff) : "memory");
}
DI void touch(rsrc_t rs, unsigned lds, unsigned voff, unsigned soff) {
  asm volatile("s_mov_b32 m0, %0\n\ts_nop 0\n\tbuffer_load_dword %1, %2, %3 offen lds"
               :: "s"(lds), "v"(voff), "s"(rs), "s"(soff) : "memory");
}
template <unsigned LO, unsigned HI>
DI void dma16m(rsrc_t rs, unsigned lds, unsigned voff, unsigned soff) {
  asm volatile("s_mov_b32 m0, %0\n\ts_mov_b32 exec_lo, %4\n\ts_mov_b32 exec_hi, %5\n\tbuffer_load_dwordx4 %1, %2, %3 offen lds\n\ts_mov_b64 exec, -1"
               :: "s"(lds), "v"(voff), "s"(rs), "s"(soff), "i"(LO), "i"(HI) : "memory");
}
DI void ld8(u32x2& d, rsrc_t rs, unsigned voff, unsigned soff) {
  asm volatile("buffer_load_dwordx2 %0, %1, %2, %3 offen" : "=v"(d) : "v"(voff), "s"(rs), "s"(soff) : "memory");
}
DI void ld16(u32x4& d, rsrc_t rs, unsigned voff, unsigned soff) {
  asm volatile("buffer_load_dwordx4 %0, %1, %2, %3 offen" : "=v"(d) : "v"(voff), "s"(rs), "s"(soff) : "memory");
}
DI void ld16c(u32x4& d, rsrc_t rs, unsigned voff, unsigned soff) {
  asm volatile("buffer_load_dwordx4 %0, %1, %2, %3 offen sc1" : "=v"(d) : "v"(voff), "s"(rs), "s"(soff) : "memory");
}
DI void st16c(rsrc_t rs, unsigned voff, u32x4 v) { __builtin_amdgcn_raw_buffer_store_b128(v, rs, voff, 0, 16); }
DI void st8c(rsrc_t rs, unsigned voff, u32x2 v) { __builtin_amdgcn_raw_buffer_store_b64(v, rs, voff, 0, 16); }
DI u32x2 lds8(const LAS char* p) { return *(const volatile LAS u32x2*)p; }
DI u32x2 next_tag(u32x2 g) { return (u32x2){g[0] * 1664525u + 1013904223u, g[1] * 22695477u + 2654435761u}; }
#define MFMA16(a, b, c) __builtin_amdgcn_mfma_f32_16x16x32_bf16((a), (b), (c), 0, 0, 0)
#define MFMA32(a, b, c) __builtin_amdgcn_mfma_f32_32x32x16_bf16((a), (b), (c), 0, 0, 0)
#define MFMA32K8(a, b, c) __builtin_amdgcn_mfma_f32_32x32x8bf16_1k(__builtin_bit_cast(s16x4, a), __builtin_bit_cast(s16x4, b), (c), 0, 0, 0)
#define WAIT_BAR(N) asm volatile("s_waitcnt vmcnt(" #N ") lgkmcnt(0)\n\ts_barrier" ::: "memory")
#define LBAR() asm volatile("s_waitcnt lgkmcnt(0)\n\ts_barrier" ::: "memory")
#define SB() __builtin_amdgcn_sched_barrier(0)
#define PIN8(a) asm volatile("" : "+v"(a[0]), "+v"(a[1]), "+v"(a[2]), "+v"(a[3]), "+v"(a[4]), "+v"(a[5]), "+v"(a[6]), "+v"(a[7]))

#define STAMP_DECL
#define STAMP(kid, i)
#define STAMP_FLUSH(kid)

constexpr float LOG2E = 1.4426950408889634f;

struct WFrag { bf16x8 t0k0, t1k0; u32x2 t0k1, t1k1; };
DI WFrag load_wfrag(const LAS char* a0  , const LAS char* a1  , int hh, int lane) {
  u32x2 r0 = __builtin_bit_cast(u32x2, trread(a0)), r1 = __builtin_bit_cast(u32x2, trread(a0 + 160));
  u32x2 r2 = __builtin_bit_cast(u32x2, trread(a1)), r3 = __builtin_bit_cast(u32x2, trread(a1 + 160));
  u32x2 r4 = __builtin_bit_cast(u32x2, trread(a0 + 640)), r5 = __builtin_bit_cast(u32x2, trread(a1 + 640));
  const unsigned sg = (lane & 4) ? 0x80008000u : 0u;
  const unsigned km = hh ? 0u : 0xFFFFFFFFu;
  WFrag w;
  w.t0k0 = asfrag((u32x4){r0[0] ^ sg, r0[1] ^ sg, r1[0] ^ sg, r1[1] ^ sg});
  w.t1k0 = asfrag((u32x4){r2[0] ^ sg, r2[1] ^ sg, r3[0] ^ sg, r3[1] ^ sg});
  w.t0k1 = (u32x2){(r4[0] ^ sg) & km, (r4[1] ^ sg) & km};
  w.t1k1 = (u32x2){(r5[0] ^ sg) & km, (r5[1] ^ sg) & km};
  return w;
}
struct HProj { f32x16 p0, p1; };
struct HRow { bf16x8 x0; u32x2 x1; };
DI HRow hh_read(const LAS char* p, const LAS char* p4) { HRow r; r.x0 = frag2(lds8(p), lds8(p + 1024)); r.x1 = lds8(p4); return r; }
DI HProj hh_proj(const WFrag& w, const HRow& x) {
  const f32x16 z = {};
  HProj h;
  h.p0 = MFMA32(w.t0k0, x.x0, z);
  h.p1 = MFMA32(w.t1k0, x.x0, z);
  h.p0 = MFMA32K8(w.t0k1, x.x1, h.p0);
  h.p1 = MFMA32K8(w.t1k1, x.x1, h.p1);
  return h;
}
template <int N, int NV> DI void mix_mfma() {
#pragma unroll
  for (int i = 0; i < N; ++i) { __builtin_amdgcn_sched_group_barrier(0x8, 1, 0); __builtin_amdgcn_sched_group_barrier(0x402, NV, 0); }
}
DI void hh_soft(const HProj& h, unsigned (&ph)[10]) {
  f32x2 v[10];
#pragma unroll
  for (int i = 0; i < 8; ++i) v[i] = (f32x2){rbf(h.p0[2 * i]), rbf(h.p0[2 * i + 1])};
#pragma unroll
  for (int i = 0; i < 2; ++i) v[8 + i] = (f32x2){rbf(h.p1[2 * i]), rbf(h.p1[2 * i + 1])};
  float m = __builtin_elementwise_maximum(v[0][0], v[0][1]);
#pragma unroll
  for (int i = 1; i < 10; ++i) m = fmax3(m, v[i][0], v[i][1]);
  const float nm = -m * LOG2E;
#pragma unroll
  for (int i = 0; i < 10; ++i) {
    v[i] = __builtin_elementwise_fma(v[i], (f32x2){LOG2E, LOG2E}, (f32x2){nm, nm});
    v[i] = (f32x2){__builtin_amdgcn_exp2f(v[i][0]), __builtin_amdgcn_exp2f(v[i][1])};
  }
  const f32x2 s = (((v[0] + v[1]) + (v[2] + v[3])) + ((v[4] + v[5]) + (v[6] + v[7]))) + (v[8] + v[9]);
  const float r = __builtin_amdgcn_rcpf(s[0] + s[1]);
#pragma unroll
  for (int i = 0; i < 10; ++i) { v[i] *= (f32x2){r, r}; ph[i] = pk2(v[i][0], v[i][1]); }
}

constexpr float SC2 = 0.22360679774997896f * 1.4426950408889634f;

DI unsigned o_off(int h, int srow) { return (unsigned)((srow >> 4) * 20480 + (srow & 15) * 16 + 5 * (h >> 3) * 1024 + ((h >> 1) & 3) * 256); }
DI void store_o(rsrc_t ro, unsigned v16, unsigned v8, const float (&o)[12]) {
  st16c(ro, v16, (u32x4){pk2(o[0], o[1]), pk2(o[2], o[3]), pk2(o[4], o[5]), pk2(o[6], o[7])});
  st8c(ro, v8, (u32x2){pk2(o[8], o[9]), pk2(o[10], o[11])});
}
#define PIN(x) asm volatile("" : "+v"(x))
DI void wait_flags(rsrc_t rin, unsigned voff, bool need, u32x2 tag) {
#pragma unroll 1
  for (int spin = 0; spin < (1 << 20); ++spin) {
    u32x2 f;
    asm volatile("buffer_load_dwordx2 %0, %1, %2, 0 offen sc1\n\ts_waitcnt vmcnt(0)" : "=&v"(f) : "v"(voff), "s"(rin) : "memory");
    if (__all(!need || (f[0] == tag[0] && f[1] == tag[1]))) break;
    __builtin_amdgcn_s_sleep(1);
  }
}

constexpr unsigned CODE_BYTES = 23 * 1024;
constexpr int G_STAGE = 16384 + 30720;
constexpr int B_KP = 3072, B_KH = 5 * B_KP, B_VP = 3104, B_VH = 15616;
constexpr int B_K = 0, B_V = 4 * B_KH, B_ONE = B_V + 4 * B_VH + 160, L_Q = B_ONE + 3072 + 96;
constexpr int A_KP = 1024, A_KH = 5120, A_VP = 1056, A_VH = 5376;
constexpr int A_K = 0, A_V = 4 * A_KH, A_ONE = A_V + 4 * A_VH + 160, A_PHI = A_ONE + 1024 + 96, A_KV = 20 * 5120 + 256;
constexpr int L_W = L_Q + 20480, L_G = L_W + 4096, L_T = L_G + 1024, L_S = L_T + 256 + 16, K_LDS = L_S + 1024;
constexpr int P_X = 20 * 5120 + 256;
static_assert(A_PHI + 8 * 5632 + 64 <= A_KV && A_KV + 4 * 3584 <= L_Q && 3 * G_STAGE + 64 <= L_W && K_LDS <= 160 * 1024 - 2048 && P_X + 40960 <= L_W, "LDS map");
static_assert(B_V % 256 == 0 && B_VH % 256 == 0 && A_V % 256 == 0 && A_VH % 256 == 0 && L_Q % 16 == 0 && A_PHI % 16 == 0, "banks");
static_assert(2 * B_KH == 6 * 5120 && B_V == 12 * 5120 && 2 * B_VH >= 6 * 5120, "wo blocks 0-5 / 12-17 reuse the K / V planes of heads 0,1");

DI f32x16 qk_tile(const LAS char* kh, const LAS char* k4, bf16x8 q0, u32x2 q1, f32x16 c) {
  const u32x2 a = lds8(kh), b = lds8(kh + B_KP), d = lds8(k4);
  c = MFMA32(frag2(a, b), q0, c);
  return MFMA32K8(d, q1, c);
}
DI f32x16 pv_tile(const LAS char* vp, const f32x16& s, f32x16 o) {
  const bf16x8 v0 = cat8(trread(vp), trread(vp + 64)), v1 = cat8(trread(vp + 128), trread(vp + 192));
  unsigned pw[8];
#pragma unroll
  for (int i = 0; i < 8; ++i) pw[i] = pk2(__builtin_amdgcn_exp2f(s[2 * i]), __builtin_amdgcn_exp2f(s[2 * i + 1]));
  o = MFMA32(v0, asfrag((u32x4){pw[0], pw[1], pw[2], pw[3]}), o);
  return MFMA32(v1, asfrag((u32x4){pw[4], pw[5], pw[6], pw[7]}), o);
}
struct Strm { const LAS char* kh; const LAS char* k4; const LAS char* vp; bf16x8 q0; u32x2 q1; };
struct KFr { u32x2 a, b, d; };
DI KFr kread(const Strm& s, int off) { KFr k; k.a = lds8(s.kh + off); k.b = lds8(s.kh + off + B_KP); k.d = lds8(s.k4 + off); return k; }
DI void tile_step(f32x16& o, const f32x16& sc, const LAS char* vp, f32x16& sn, const Strm& n, KFr& k, const Strm& ks, int koff) {
  const f32x16 z = {};
  const bf16x8 v0 = cat8(trread(vp), trread(vp + 64)), v1 = cat8(trread(vp + 128), trread(vp + 192));
  float e[16];
#pragma unroll
  for (int i = 0; i < 3; ++i) e[i] = __builtin_amdgcn_exp2f(sc[i]);
  asm volatile("" : "+v"(e[0]), "+v"(e[1]), "+v"(e[2]));
  SB();
  sn = MFMA32(frag2(k.a, k.b), n.q0, z);
#pragma unroll
  for (int i = 3; i < 7; ++i) e[i] = __builtin_amdgcn_exp2f(sc[i]);
  asm volatile("" : "+v"(e[3]), "+v"(e[4]), "+v"(e[5]), "+v"(e[6]));
  SB();
  sn = MFMA32K8(k.d, n.q1, sn);
  k = kread(ks, koff);
#pragma unroll
  for (int i = 7; i < 10; ++i) e[i] = __builtin_amdgcn_exp2f(sc[i]);
  u32x4 p0 = {pk2(e[0], e[1]), pk2(e[2], e[3]), pk2(e[4], e[5]), pk2(e[6], e[7])};
  asm volatile("" : "+v"(e[8]), "+v"(e[9]), "+v"(p0));
  SB();
  o = MFMA32(v0, asfrag(p0), o);
#pragma unroll
  for (int i = 10; i < 16; ++i) e[i] = __builtin_amdgcn_exp2f(sc[i]);
  o = MFMA32(v1, asfrag((u32x4){pk2(e[8], e[9]), pk2(e[10], e[11]), pk2(e[12], e[13]), pk2(e[14], e[15])}), o);
  SB();
}
DI void run_tiles(f32x16& o, const Strm& s, const f32x16& c0, int off, int step, int n) {
  KFr k = kread(s, off + step);
  f32x16 sa = qk_tile(s.kh + off, s.k4 + off, s.q0, s.q1, c0), sb;
#pragma unroll 1
  for (;;) {
    tile_step(o, sa, s.vp + off, sb, s, k, s, off + 2 * step);
    if (n == 1) break;
    tile_step(o, sb, s.vp + off + step, sa, s, k, s, off + 3 * step);
    if (n == 2) break;
    n -= 2; off += 2 * step;
  }
}

__global__ __launch_bounds__(512, 2) void k1_fused(const __hip_bfloat16* __restrict__ xA, const __hip_bfloat16* __restrict__ wA,
                                                   const __hip_bfloat16* __restrict__ xB, const __hip_bfloat16* __restrict__ wB,
                                                   const __hip_bfloat16* __restrict__ wphi, const __hip_bfloat16* __restrict__ woA,
                                                   const __hip_bfloat16* __restrict__ woB, char* __restrict__ ws,
                                                   __hip_bfloat16* __restrict__ out) {
  __shared__ __attribute__((aligned(16))) char smem[K_LDS];
  const LAS char* sm = (const LAS char*)smem;
  LAS char* smw = (LAS char*)smem;
  const unsigned lds0 = (unsigned)(size_t)(LAS char*)smem;
  const int tid = threadIdx.x, lane = tid & 63;
  const int wid = __builtin_amdgcn_readfirstlane(tid >> 6);
  const int xcd = blockIdx.x & 7, idx = blockIdx.x >> 3;
  const int br = idx & 1, sub = idx >> 1;
  const int mt = (xcd >> 2) * 8 + (sub & 7), nt = (xcd & 3) * 2 + (sub >> 3);
  const int mt3 = xcd * 2 + (sub & 1), nt3 = sub >> 1;
  const int me = blockIdx.x;
  const int g = lane >> 4, fr = lane & 15, q4 = fr >> 2, p4 = lane & 3;
  STAMP_DECL;
  STAMP(0, 0);
  {
    unsigned long long pc;
    asm volatile("s_getpc_b64 %0" : "=s"(pc));
    if (wid == 0) touch(mkrsrc((const void*)pc, CODE_BYTES), lds0 + L_T, lane < 6 ? (unsigned)((lane * 32 + idx) * 128) : OOB, 0);
  }
  const char* wg = (const char*)(br ? wB : wA);
  const rsrc_t rx = mkrsrc((const char*)(br ? xB : xA) + (size_t)mt * 128 * 1280, 128 * 1280);
  const rsrc_t rw = mkrsrc(wg + nt * 160, 640 * 3840 - nt * 160);
  const unsigned vx0 = (unsigned)((2 * (lane >> 3)) * 1280 + (((lane & 7) ^ g) * 16));
  const unsigned vx1 = (unsigned)((16 + 2 * (lane >> 3)) * 1280 + (((lane & 7) ^ (4 + g)) * 16));
  const int wr = lane >= 30 ? 1 : 0, wc = lane - 30 * wr, wsg = (wc >= 10) + (wc >= 20);
  const unsigned vw = (unsigned)(wr * 3840 + wsg * 1280 + (wc - 10 * wsg) * 16);
  const unsigned sx = (unsigned)((32 * (wid >> 1) + (wid & 1)) * 1280);
  const unsigned sw = (unsigned)((16 * (wid >> 1) + 4 * (wid & 1)) * 3840);
  const unsigned lx = lds0 + wid * 2048, lw = lds0 + 16384 + wid * 3840;
#define G_DMA(i, bo, xo, wo) do { \
    if ((i) == 0 || (i) == 3) dma16(rx, lx + (bo) + ((i) ? 1024 : 0), (i) ? vx1 : vx0, (xo)); \
    else { const int n_ = (i) < 3 ? (i) - 1 : (i) - 2; \
      dma16m<0xFFFFFFFFu, 0x0FFFFFFFu>(rw, lw + (bo) + n_ * 960, vw, (wo) + ((n_ >> 1) * 8 + (n_ & 1) * 2) * 3840); } } while (0)
#pragma unroll
  for (int i = 0; i < 6; ++i) G_DMA(i, 0, sx, sw);
  const rsrc_t rt = (wid & 4) ? mkrsrc(wg, 640 * 3840) : rx;
  const unsigned tst = (wid & 4) ? 245760u : 128u;
  unsigned vt = (wid & 4) ? (fr < 15 ? (unsigned)(((mt * 8 + nt) * 15 + fr) * 128 + g * 245760) : OOB) : (unsigned)((nt * 16 + fr) * 1280 + g * 128);
  if ((wid & 3) == 3) touch(rt, lds0 + L_T, vt, tst);
  vt = g == 0 ? vt : OOB;

  const int wm = wid >> 2, wn = wid & 3;
  const LAS char* xa0 = sm + (64 * wm + fr) * 128 + ((g ^ (fr >> 1)) * 16);
  const LAS char* xa1 = sm + (((64 * wm + fr) * 128 + ((g ^ (fr >> 1)) * 16)) ^ 64);
  const LAS char* wa = sm + 16384 + (16 * (g >> 1) + 4 * (g & 1) + q4) * 480 + 8 * p4 + wn * 128;
  f32x4 acc[4][4] = {};
#pragma unroll
  for (int i = 0; i < 4; ++i) asm volatile("" : "+v"(acc[i][0]), "+v"(acc[i][1]), "+v"(acc[i][2]), "+v"(acc[i][3]));
  int eb[4];
  {
    const int kb = br ? B_K + 2048 : A_K, kp = br ? B_KP : A_KP, vb = br ? B_V + 2048 : A_V, vp = br ? B_VP : A_VP;
#pragma unroll
    for (int j = 0; j < 4; ++j) {
      const int qd = 16 * wn + 4 * j + g, r = qd - 40;
      const int v = vb + r * vp + ((r * 13) >> 6) * 96;
      eb[j] = (qd < 20 ? L_Q + qd * 1024 : qd < 40 ? kb + (qd - 20) * kp : qd < 60 ? v : L_S) + wm * 512 + fr * 16;
      asm volatile("" : "+v"(eb[j]));
    }
  }
  int cons;
  {
    const int cm = br ? mt + 1 + lane : lane;
    const int c1 = (((((nt & 1) * 8 + (cm & 7)) << 1) | br) << 3) | (((cm >> 3) & 1) * 4 + (nt >> 1));
    const int c2 = (((((lane & 7) * 2 + (mt & 1)) << 1) | br) << 3) | (mt >> 1);
    cons = lane < 16 ? ((cm < 16 && (!br || lane < 2)) ? c1 : -1) : (lane < 24 ? c2 : lane == 63 ? me : -1);
    asm volatile("" : "+v"(cons));
  }
  const int hh = lane >> 5, r32 = lane & 31, b16 = (lane >> 4) & 1, vq = 4 * b16 + p4;
  unsigned vflag = (unsigned)(WS_IN + cons * 256 + (lane >= 16 ? 16 + nt : br ? 1 - lane : mt) * 8);
  unsigned vf1 = (lane < 16 && cons >= 0) ? vflag : OOB, vf2 = (lane >= 16 && lane < 24) ? vflag : OOB;
  const int lq = (lane >= 10) + (lane >= 20) + (lane >= 30);
  unsigned vwo = (unsigned)(lq * 1280 + (lane - 10 * lq) * 16);
  PIN(vf1); PIN(vf2); PIN(vwo);
  const int hs = wid >> 2, qi = hs ? 3 - (wid & 3) : (wid & 3);
  const int hl = wid >> 1, rw0 = 64 * (wid & 1);
  int c0, c1, c2, c3, c4, c5, c6;
  unsigned v16, v8;
  if (br) {
    c0 = B_K + hs * B_KH + hh * 2 * B_KP + r32 * 8;
    c1 = B_K + hs * B_KH + 4 * B_KP + r32 * 8;
    c2 = (vq < 5 ? B_V + hs * B_VH + vq * B_VP : B_ONE) + (4 * hh + q4) * 8;
    c3 = c2 + (vq < 5 ? 2 * B_VH : 0);
    c4 = L_Q + hs * 5120 + (32 * qi + r32) * 8;
    c5 = L_Q + (2 + hs) * 5120 + (32 * (3 - qi) + r32) * 8;
    c6 = hh * 2048;
    v16 = o_off(4 * nt + hs, mt * 128 + 32 * qi + r32);
  } else {
    c0 = L_W + hl * 800 + (8 * hh + q4) * 40 + (2 * b16 + (p4 >> 1)) * 8;
    c1 = L_W + hl * 800 + (8 * hh + q4) * 40 + 32;
    c2 = hl * 5120 + (rw0 + r32) * 8;
    c3 = A_PHI + wid * 5632 + r32 * 88 + hh * 40;
    c4 = A_PHI + wid * 5632 + (8 * hh + q4) * 88 + (16 * b16 + 4 * p4) * 2;
    c5 = (vq < 5 ? A_V + hl * A_VH + vq * A_VP : A_ONE) + (rw0 + 8 * hh + q4) * 8;
    c6 = A_PHI + wid * 5632 + min(r32, 21) * 160 + hh * 16;
    v16 = o_off(4 * nt + hl, mt * 128 + rw0 + r32);
  }
  {
    const int odd = br ? hs : (hl & 1);
    v8 = hh ? OOB : (v16 & ~1023u) + 2048 + 512 * odd + ((v16 & 1023u) >> 1);
    v16 += (hh + 3 * odd) * 1024;
  }
  PIN(c0); PIN(c1); PIN(c2); PIN(c3); PIN(c4); PIN(c5); PIN(c6); PIN(v16); PIN(v8);
  f32x16 mle, mgt;
#pragma unroll
  for (int e = 0; e < 16; ++e) {
    const bool le = (e & 3) + 8 * (e >> 2) + 4 * hh <= r32;
    mle[e] = le ? 0.f : -INFINITY;
    mgt[e] = le ? -INFINITY : 0.f;
  }
  PIN(mle); PIN(mgt);
  const rsrc_t rg = mkrsrc(ws + WS_G, WS_KVH);
  const rsrc_t rph = mkrsrc((const char*)wphi + nt * 3200, 3200);
  bf16x8 x0[4], x1[4];
  s16x4 w0[4][2], w1[4][2];
#define G_RD(X, W, xa, kk, r) do { \
    if ((r) < 2) W[0][(r)] = trread(wa + ((kk) * 32 + (r) * 8) * 480); \
    else if ((r) < 6) X[(r) - 2] = *(const LAS bf16x8*)(xa + ((r) - 2) * 2048); \
    else if ((r) < 12) W[((r) - 4) >> 1][(r) & 1] = trread(wa + ((kk) * 32 + ((r) & 1) * 8) * 480 + (((r) - 4) >> 1) * 32); } while (0)
#define G_MM(X, W, m) acc[(m) & 3][(m) >> 2] = MFMA16(cat8(W[(m) >> 2][0], W[(m) >> 2][1]), X[(m) & 3], acc[(m) & 3][(m) >> 2])
  asm volatile("s_barrier" ::: "memory");
#pragma unroll
  for (int i = 0; i < 6; ++i) G_DMA(i, G_STAGE, sx + 128, sw + 245760);
  if ((wid & 3) == 3) { WAIT_BAR(7); } else { WAIT_BAR(6); }
  STAMP(0, 1);
#pragma unroll
  for (int r = 0; r < 12; ++r) G_RD(x0, w0, xa0, 0, r);
  int bo = 2 * G_STAGE;
  int bd = bo;
  unsigned xo = sx + 256, wo = sw + 2 * 245760;
#pragma unroll
  for (int i = 0; i < 3; ++i) G_DMA(i, bo, xo, wo);
#pragma unroll 1
  for (int t = 0; t < 10; ++t) {
    SB();
#pragma unroll
    for (int m = 0; m < 12; ++m) {
      G_MM(x0, w0, m); G_RD(x1, w1, xa1, 1, m);
      if (m % 3 == 2 && m < 9) G_DMA(3 + m / 3, bd, xo, wo);
      SB();
    }
    if (wn < 3) { G_MM(x0, w0, 12); G_MM(x0, w0, 13); G_MM(x0, w0, 14); G_MM(x0, w0, 15); }
    const int d = bo == G_STAGE ? -2 * G_STAGE : G_STAGE;
    xa0 += d; xa1 += d; wa += d;
    bo = bo == 2 * G_STAGE ? 0 : bo + G_STAGE;
    bd = t < 7 ? bo : 0x30000;
    xo = t < 7 ? xo + 128 : OOB;
    wo += 245760;
    WAIT_BAR(6);
    SB();
#pragma unroll
    for (int m = 0; m < 12; ++m) {
      G_MM(x1, w1, m); G_RD(x0, w0, xa0, 0, m);
      if (m % 3 == 2 && m < 9) G_DMA(m / 3, bd, xo, wo);
      SB();
    }
    if (wn < 3) { G_MM(x1, w1, 12); G_MM(x1, w1, 13); G_MM(x1, w1, 14); G_MM(x1, w1, 15); }
    if (t < 5) {
      if ((wid & 3) == 3) touch(rt, lds0 + L_T, vt, (5 + t) * tst);
      if (wid == 1) touch(mkrsrc(br ? woB : woA, 640 * 1280), lds0 + L_T, lane < 10 ? (unsigned)(((mt * 8 + nt) * 50 + lane) * 128) : OOB, t * 1280);
      if (t == TB) {
        if (wid == 0) dma16(rg, lds0 + L_G, cons >= 0 ? cons * 16 : OOB, 0);
        if (wid == 5 && !br) {
#pragma unroll
          for (int i = 0; i < 4; ++i) dma16(rph, lds0 + L_W + i * 1024, (unsigned)lane * 16, i * 1024);
        }
      }
    }
  }
  STAMP(0, 2);
  {
    const rsrc_t rh = mkrsrc(ws + WS_KVH + (mt * 8 + nt) * 40960, (br && mt < 15) ? 40960 : 0);
    const unsigned v0 = (unsigned)((16 * wn + g - 20) * 1024 + wm * 512 + fr * 16);
#pragma unroll
    for (int j = 3; j >= 0; --j) {
      if (br && 4 * wn + j < 5) {
#pragma unroll
        for (int i = 0; i < 4; ++i) acc[i][j] *= SC2;
      }
#pragma unroll
      for (int i = 0; i < 2; ++i) {
        const f32x4 a = acc[2 * i][j], b = acc[2 * i + 1][j];
        const u32x4 ev = (u32x4){pk2(a[0], a[1]), pk2(a[2], a[3]), pk2(b[0], b[1]), pk2(b[2], b[3])};
        unsigned vo = v0 + j * 4096 + i * 256;
        asm volatile("" : "+v"(vo));
        st16c(rh, vo, ev);
        *(LAS u32x4*)(smw + eb[j] + i * 256) = ev;
      }
    }
  }
  if (tid < 384) *(LAS u32x2*)(smw + (br ? B_ONE : A_ONE) + (br ? tid : tid & 127) * 8) = (u32x2){0x3F80u, 0u};
  const u32x2 gc = next_tag(*(const LAS u32x2*)(sm + L_G + lane * 16));
  const u32x2 tag = {(unsigned)__builtin_amdgcn_readlane(gc[0], 63), (unsigned)__builtin_amdgcn_readlane(gc[1], 63)};
  const char* wog = (const char*)(br ? woB : woA);
  const rsrc_t ro = mkrsrc(ws + WS_O + br * (SEQ * DM * 2), SEQ * DM * 2);
  const rsrc_t rwo = mkrsrc(wog + nt3 * 160, 640 * 1280 - nt3 * 160);
  const int sg = 2 * (wid >> 2) + (wid & 1), st = (wid >> 1) & 1;
#define P_DMA(ks) do { const int w5 = (ks) % 5; const int o0 = w5 == 0 ? 0 : w5 == 1 ? 4 : w5 == 2 ? 16 : w5 == 3 ? 20 : 24; \
      dma16m<0xFFFFFFFFu, 0xFFu>(rwo, lds0 + (ks) * 5120 + wid * 640, vwo, (unsigned)(160 * ((ks) / 5) + o0 + 40 * sg + st * (w5 == 2 ? 20 : 8)) * 1280u); } while (0)
  if (br) {
    WAIT_BAR(0);
    if (wid == 0) st8c(rg, vf1, gc);
    STAMP(0, 3);
    Strm S[2];
#pragma unroll
    for (int r = 0; r < 2; ++r) {
      S[r].kh = sm + c0 + r * 2 * B_KH;
      S[r].k4 = sm + c1 + r * 2 * B_KH;
      S[r].vp = sm + (r ? c3 : c2);
      const LAS char* qp = sm + (r ? c5 : c4);
      S[r].q0 = frag2(lds8(qp + c6), lds8(qp + c6 + 1024));
      const u32x2 t4 = lds8(qp + 4096);
      S[r].q1 = hh ? (u32x2){0u, 0u} : t4;
    }
    f32x16 o0 = {}, o1 = {};
    run_tiles(o0, S[0], mle, (qi + 8) * 256, -256, qi + 1);
    STAMP(0, 4);
    {
      const int nb = qi < 2 ? 1 : 0, pg = 2 * hs + (qi & 1);
      const bool have = mt - 2 + nb >= 0;
      wait_flags(rg, (unsigned)(WS_IN + me * 256 + nb * 8), have, tag);
      STAMP(0, 5);
      const rsrc_t rn = mkrsrc(ws + WS_KVH + ((have ? mt - 2 + nb : 0) * 8 + nt) * 40960 + pg * 10240, have ? 10240 : 0);
      const unsigned hd = lds0 + (hs ? B_V + (qi & 1) * 2 * B_VH : B_K + (qi & 1) * 2 * B_KH) + nb * 1024, hps = hs ? B_VP : B_KP;
#pragma unroll
      for (int i = 0; i < 10; ++i) dma16c(rn, hd + i * hps + (i >= 5 ? hs * 96 : 0), lane * 16, i * 1024);
    }
    run_tiles(o1, S[1], mle, (11 - qi) * 256, -256, 4 - qi);
    STAMP(0, 6);
    if (mt > 0) {
      if (qi < 2) { WAIT_BAR(0); } else { LBAR(); }
      STAMP(0, 7);
      {
        const f32x16 z = {};
        KFr k = kread(S[1], 1024);
        f32x16 sa = qk_tile(S[0].kh + 1024, S[0].k4 + 1024, S[0].q0, S[0].q1, z), sb;
#pragma unroll 1
        for (int off = 1024; off < 2048; off += 256) {
          tile_step(o0, sa, S[0].vp + off, sb, S[1], k, S[0], off + 256);
          tile_step(o1, sb, S[1].vp + off, sa, S[0], k, S[1], off + 256);
        }
      }
      STAMP(0, 8);
      if (mt > 1) { WAIT_BAR(0); }
    }
#pragma unroll
    for (int r = 0; r < 2; ++r) {
      const int qt = r ? 3 - qi : qi;
      f32x16& o = r ? o1 : o0;
      if (mt > 1) run_tiles(o, S[r], mgt, qt * 256, 256, 4 - qt);
      float l = __uint_as_float(__builtin_amdgcn_permlane32_swap(__float_as_uint(o[8]), __float_as_uint(o[8]), false, false)[1]);
      {
        float am = __builtin_fabsf(o[0]);
#pragma unroll
        for (int i = 1; i < 11; i += 2) am = fmax3(am, __builtin_fabsf(o[i]), __builtin_fabsf(o[i + 1]));
        am = __builtin_elementwise_maximum(am, __builtin_fabsf(o[11]));
        const bool ok = l >= 0x1p-80f && l <= FB_HI && am <= 0x1p126f;
        if (__builtin_expect(!__all(ok), 0)) {
          const int tw0 = mt >= 2 ? qt : mt == 1 ? 4 : 8;
          float m = -INFINITY;
#pragma unroll 1
          for (int pass = 0; pass < 2; ++pass) {
            o = (f32x16){};
#pragma unroll 1
            for (int w = tw0; w <= qt + 8; ++w) {
              f32x16 c;
#pragma unroll
              for (int e = 0; e < 16; ++e) c[e] = w == qt + 8 ? mle[e] : w == qt ? mgt[e] : 0.f;
              f32x16 s = qk_tile(S[r].kh + w * 256, S[r].k4 + w * 256, S[r].q0, S[r].q1, c);
              if (pass == 0) {
#pragma unroll
                for (int e = 0; e < 16; e += 2) m = fmax3(m, s[e], s[e + 1]);
              } else {
#pragma unroll
                for (int e = 0; e < 16; ++e) s[e] -= m;
                o = pv_tile(S[r].vp + w * 256, s, o);
              }
            }
            auto rr = __builtin_amdgcn_permlane32_swap(__float_as_uint(m), __float_as_uint(m), false, false);
            m = __builtin_elementwise_maximum(__uint_as_float(rr[0]), __uint_as_float(rr[1]));
          }
          l = __uint_as_float(__builtin_amdgcn_permlane32_swap(__float_as_uint(o[8]), __float_as_uint(o[8]), false, false)[1]);
        }
      }
      const float rl = __builtin_amdgcn_rcpf(l);
      float of[12];
#pragma unroll
      for (int i = 0; i < 12; ++i) of[i] = o[i] * rl;
      const unsigned dv = r ? (unsigned)((3 - 2 * qi) * 40960 + 256) : 0u;
      store_o(ro, v16 + dv, hh ? OOB : v8 + dv - (r ? 128 : 0), of);
    }
    STAMP(0, 9);
    WAIT_BAR(4);
    STAMP(0, 10);
#pragma unroll
    for (int ks = 0; ks < 6; ++ks) P_DMA(ks);
    WAIT_BAR(6);
  } else {
    WFrag wf = load_wfrag(sm + c0, sm + c1, hh, lane);
    PIN(wf.t0k0); PIN(wf.t1k0); PIN(wf.t0k1); PIN(wf.t1k1);
    if (tid >= 256) *(LAS u32x2*)(smw + A_KV + ((tid - 256) >> 1) * 112 + (tid & 1) * 48 + 40) = (u32x2){0u, 0u};
    LBAR();
    STAMP(0, 3);
    const LAS char* xp = sm + c2 + hh * 2048;
    const LAS char* x4 = sm + c2 + 4096;
    const HRow rk0 = hh_read(xp + A_K, x4 + A_K), rk1 = hh_read(xp + A_K + 256, x4 + A_K + 256);
    const HRow rq0 = hh_read(xp + L_Q, x4 + L_Q), rq1 = hh_read(xp + L_Q + 256, x4 + L_Q + 256);
    const HProj hk0 = hh_proj(wf, rk0);
    bf16x8 vf[4];
#pragma unroll
    for (int ks = 0; ks < 4; ++ks) vf[ks] = cat8(trread(sm + c5 + ks * 128), trread(sm + c5 + ks * 128 + 32));
    SB();
    f32x16 d0 = {}, d1 = {};
    HProj hq0, hq1;
    {
      unsigned ph[10];
      HProj hk1 = hh_proj(wf, rk1);
      hh_soft(hk0, ph);
#pragma unroll
      for (int i = 0; i < 5; ++i) *(LAS u32x2*)(smw + c3 + 8 * i) = (u32x2){ph[2 * i], ph[2 * i + 1]};
      PIN(hk1.p0); PIN(hk1.p1);
      mix_mfma<4, 8>();
      SB();
      bf16x8 pf[4];
#pragma unroll
      for (int i = 0; i < 4; ++i) pf[i] = cat8(trread(sm + c4 + (i >> 1) * 16 * 88 + (i & 1) * 64), trread(sm + c4 + ((i >> 1) * 16 + 4) * 88 + (i & 1) * 64));
      SB();
      hq0 = hh_proj(wf, rq0);
      d0 = MFMA32(pf[0], vf[0], d0); d1 = MFMA32(pf[1], vf[0], d1);
      d0 = MFMA32(pf[2], vf[1], d0); d1 = MFMA32(pf[3], vf[1], d1);
      hh_soft(hk1, ph);
#pragma unroll
      for (int i = 0; i < 5; ++i) *(LAS u32x2*)(smw + c3 + 32 * 88 + 8 * i) = (u32x2){ph[2 * i], ph[2 * i + 1]};
      PIN(hq0.p0); PIN(hq0.p1); PIN(d0); PIN(d1);
      mix_mfma<8, 8>();
      SB();
#pragma unroll
      for (int i = 0; i < 4; ++i) pf[i] = cat8(trread(sm + c4 + (32 + (i >> 1) * 16) * 88 + (i & 1) * 64), trread(sm + c4 + (36 + (i >> 1) * 16) * 88 + (i & 1) * 64));
      d0 = MFMA32(pf[0], vf[2], d0); d1 = MFMA32(pf[1], vf[2], d1);
      d0 = MFMA32(pf[2], vf[3], d0); d1 = MFMA32(pf[3], vf[3], d1);
    }
    *(LAS f32x4*)(smw + c6) = (f32x4){d0[0], d0[1], d0[2], d0[3]};
    *(LAS f32x4*)(smw + c6 + 32) = (f32x4){d0[4], d0[5], d0[6], d0[7]};
    *(LAS f32x4*)(smw + c6 + 64) = (f32x4){d0[8], d0[9], d0[10], d0[11]};
    *(LAS f32x4*)(smw + c6 + 96) = (f32x4){d0[12], d0[13], d0[14], d0[15]};
    *(LAS f32x4*)(smw + c6 + 128) = (f32x4){d1[0], d1[1], d1[2], d1[3]};
    SB();
    hq1 = hh_proj(wf, rq1);
    PIN(hq1.p0); PIN(hq1.p1);
    LBAR();
    STAMP(0, 4);
    const rsrc_t rkv = mkrsrc(ws + WS_KVP + nt * 16 * 6784, 16 * 6784);
    {
      const int e = (wid & 1) * 64 + lane;
      const LAS char* o = sm + A_PHI + (wid & ~1) * 5632 + e * 32;
      const f32x4 a = *(const LAS f32x4*)o + *(const LAS f32x4*)(o + 5632), b = *(const LAS f32x4*)(o + 16) + *(const LAS f32x4*)(o + 5648);
      st16c(rkv, e < 105 ? (unsigned)(mt * 6784 + hl * 1680 + e * 16) : OOB, (u32x4){pk2(a[0], a[1]), pk2(a[2], a[3]), pk2(b[0], b[1]), pk2(b[2], b[3])});
    }
    SB();
    unsigned phq[2][10];
    hh_soft(hq0, phq[0]);
#pragma unroll
    for (int i = 0; i < 10; ++i) PIN(phq[0][i]);
    WAIT_BAR(0);
    if (wid == 0) st8c(rg, vf1, gc);
#pragma unroll
    for (int ks = 0; ks < 8; ++ks) P_DMA(ks);
    STAMP(0, 5);
    hh_soft(hq1, phq[1]);
#pragma unroll
    for (int i = 0; i < 10; ++i) PIN(phq[1][i]);
    int kd0, kd1;
    {
      const int row = tid / 5, oc = tid - row * 5, h4 = row / 21, d = row - h4 * 21;
      kd0 = tid < 420 ? A_KV + h4 * 3584 + d * 112 + oc * 16 + (oc > 2 ? 8 : 0) : L_S;
      kd1 = kd0 + (oc == 2 ? 16 : 8);
      PIN(kd0); PIN(kd1);
    }
    wait_flags(rg, (unsigned)(WS_IN + me * 256 + fr * 8), true, tag);
    STAMP(0, 6);
    if (wid < 7) {
      u32x4 p[16];
#pragma unroll
      for (int i = 0; i < 16; ++i) p[i] = __builtin_amdgcn_raw_buffer_load_b128(rkv, tid * 16, i * 6784, 16);
      f32x2 lo[2] = {}, hi[2] = {};
#pragma unroll
      for (int i = 0; i < 16; ++i)
#pragma unroll
        for (int u = 0; u < 2; ++u) {
          lo[u] += (f32x2){__uint_as_float(p[i][2 * u] << 16), __uint_as_float(p[i][2 * u + 1] << 16)};
          hi[u] += (f32x2){__uint_as_float(p[i][2 * u] & 0xFFFF0000u), __uint_as_float(p[i][2 * u + 1] & 0xFFFF0000u)};
        }
      *(LAS u32x2*)(smw + kd0) = (u32x2){pk2(lo[0][0], hi[0][0]), pk2(lo[0][1], hi[0][1])};
      *(LAS u32x2*)(smw + kd1) = (u32x2){pk2(lo[1][0], hi[1][0]), pk2(lo[1][1], hi[1][1])};
    }
#pragma unroll
    for (int ks = 8; ks < 14; ++ks) P_DMA(ks);
    LBAR();
    STAMP(0, 7);
    const LAS char* kp = sm + A_KV + hl * 3584 + (r32 == 24 ? 20 : r32) * 112 + hh * 48;
    const bf16x8 f0 = *(const LAS bf16x8*)(kp), f1 = *(const LAS bf16x8*)(kp + 16), f2 = *(const LAS bf16x8*)(kp + 32);
    f32x16 n[2];
#pragma unroll
    for (int t = 0; t < 2; ++t) n[t] = MFMA32(f0, asfrag((u32x4){phq[t][0], phq[t][1], phq[t][2], phq[t][3]}), (f32x16){});
#pragma unroll
    for (int t = 0; t < 2; ++t) n[t] = MFMA32(f1, asfrag((u32x4){phq[t][4], phq[t][5], phq[t][6], phq[t][7]}), n[t]);
#pragma unroll
    for (int t = 0; t < 2; ++t) n[t] = MFMA32(f2, asfrag((u32x4){phq[t][8], phq[t][9], 0u, 0u}), n[t]);
#pragma unroll
    for (int t = 0; t < 2; ++t) {
      const float den = rbf(rbf(hh ? n[t][8] : n[t][12]) + 1e-6f);
      const float r = __builtin_amdgcn_rcpf(den);
      float o[12];
#pragma unroll
      for (int i = 0; i < 12; ++i) o[i] = rbf(n[t][i]) * r;
      store_o(ro, v16 + t * 40960, hh ? OOB : v8 + t * 40960, o);
    }
    STAMP(0, 10);
#pragma unroll
    for (int ks = 14; ks < 20; ++ks) P_DMA(ks);
    WAIT_BAR(6);
  }
  {
    if (wid == 0) st8c(rg, vf2, gc);
    if (br) {
#pragma unroll
      for (int ks = 6; ks < 20; ++ks) P_DMA(ks);
    }
    STAMP(0, 11);
    int oa[3];
    unsigned og[3];
#pragma unroll
    for (int i = 0; i < 3; ++i) {
      const int c = min(lane + 64 * i, 160), row = c / 10, ch = c - row * 10;
      oa[i] = wid * 2816 + row * 176 + ch * 16;
      og[i] = (unsigned)(row * 2560 + ch * 16);
      PIN(oa[i]); PIN(og[i]);
    }
    const int pm = wid & 3, kh = wid >> 2;
    wait_flags(rg, (unsigned)(WS_IN + me * 256 + 128 + (4 * kh + (lane & 3)) * 8), true, tag);
    STAMP(0, 12);
    const rsrc_t rf = mkrsrc(ws + WS_O + (unsigned)((br * 128 + mt3 * 8 + 2 * pm) * 20 + 10 * kh) * 1024u, 30 * 1024);
    const unsigned vk = lane * 16 + kh * 20480, vh = lane * 16 + (1 - kh) * 20480;
    u32x4 xf[10][2];
    const unsigned vk8 = lane * 8 + kh * 20480, vh8 = lane * 8 + (1 - kh) * 20480;
#define P_LD1(v, v8_, tau) ((tau) % 5 == 2 ? __builtin_bit_cast(u32x4, frag2(__builtin_amdgcn_raw_buffer_load_b64(rf, v8_, (tau) * 1024, 16), \
                                                                             __builtin_amdgcn_raw_buffer_load_b64(rf, v8_, (tau) * 1024 + 512, 16))) \
                                           : __builtin_amdgcn_raw_buffer_load_b128(rf, v, (tau) * 1024, 16))
#define P_LD(tau) do { xf[tau][0] = P_LD1(vk, vk8, tau); xf[tau][1] = P_LD1(vh, vh8, tau); } while (0)
    P_LD(0); P_LD(1); P_LD(2);
    asm volatile("s_barrier" ::: "memory");
    const LAS char* wb = sm + kh * 51200 + (16 * (g >> 1) + 4 * (g & 1) + q4) * 160 + 8 * p4;
    f32x4 pc[2][5] = {};
    bf16x8 wf[2][5];
#pragma unroll
    for (int j = 0; j < 5; ++j) wf[0][j] = cat8(trread(wb + j * 32), trread(wb + 8 * 160 + j * 32));
#pragma unroll
    for (int tau = 0; tau < 10; ++tau) {
      SB();
      if (tau < 7) P_LD(tau + 3);
#pragma unroll
      for (int j = 0; j < 5; ++j) {
        if (tau < 9) wf[(tau + 1) & 1][j] = cat8(trread(wb + (tau + 1) * 5120 + j * 32), trread(wb + (tau + 1) * 5120 + 8 * 160 + j * 32));
        pc[1][j] = MFMA16(wf[tau & 1][j], asfrag(xf[tau][1]), pc[1][j]);
        if (tau < 9) pc[0][j] = MFMA16(wf[tau & 1][j], asfrag(xf[tau][0]), pc[0][j]);
        SB();
      }
    }
#pragma unroll
    for (int j = 0; j < 5; ++j) {
      pc[0][j] = MFMA16(wf[1][j], asfrag(xf[9][0]), pc[0][j]);
      *(LAS f32x4*)(smw + P_X + (wid * 5 + j) * 1024 + lane * 16) = pc[1][j];
    }
    STAMP(0, 13);
    LBAR();
    if (tid == 0) st8c(rg, me * 16, tag);
    LAS char* ow = smw + wid * 2816 + fr * 176 + g * 8;
#pragma unroll
    for (int j = 0; j < 5; ++j) {
      const f32x4 a = pc[0][j] + *(const LAS f32x4*)(sm + P_X + ((wid ^ 4) * 5 + j) * 1024 + lane * 16);
      *(LAS u32x2*)(ow + j * 32) = (u32x2){pk2(a[0], a[1]), pk2(a[2], a[3])};
    }
    const rsrc_t rout = mkrsrc((char*)out + (size_t)(mt3 * 128 + 32 * pm + 16 * kh) * 2560 + br * 1280 + nt3 * 160, 15 * 2560 + 160);
#pragma unroll
    for (int i = 0; i < 3; ++i) __builtin_amdgcn_raw_buffer_store_b128(*(const LAS u32x4*)(sm + oa[i]), rout, og[i], 0, OUT_AUX);
  }
  STAMP(0, 15);
  STAMP_FLUSH(0);
}

extern "C" void kernel_launch(void* const* d_in, const int* in_sizes, int n_in, void* d_out, int out_size, void* d_ws,
                              size_t ws_size, hipStream_t stream) {
  k1_fused<<<256, 512, 0, stream>>>((const __hip_bfloat16*)d_in[0], (const __hip_bfloat16*)d_in[1], (const __hip_bfloat16*)d_in[4],
                                    (const __hip_bfloat16*)d_in[5], (const __hip_bfloat16*)d_in[2], (const __hip_bfloat16*)d_in[3],
                                    (const __hip_bfloat16*)d_in[6], (char*)d_ws, (__hip_bfloat16*)d_out);
}
```

```cpp
#include <hip/hip_runtime.h>
#include <hip/hip_bf16.h>

typedef short bf16x8 __attribute__((ext_vector_type(8)));
typedef short s16x4 __attribute__((ext_vector_type(4)));
typedef float f32x2 __attribute__((ext_vector_type(2)));
typedef float f32x4 __attribute__((ext_vector_type(4)));
typedef float f32x16 __attribute__((ext_vector_type(16)));
typedef unsigned u32x2 __attribute__((ext_vector_type(2)));
typedef unsigned u32x4 __attribute__((ext_vector_type(4)));
typedef __bf16 bf16x2_t __attribute__((ext_vector_type(2)));
typedef __amdgpu_buffer_rsrc_t rsrc_t;
#define LAS __attribute__((address_space(3)))
#define GAS __attribute__((address_space(1)))
#define DI __device__ __forceinline__

#define PFV 6
#define OUT_AUX 0
#define TB 1
#define FB_HI 0x1p100f
#define CPF 1
constexpr int SEQ = 2048, DM = 640, NH = 32;
constexpr unsigned WS_G = 0;
constexpr unsigned WS_IN = 4096;
constexpr unsigned WS_KVH = WS_IN + 256 * 256;
constexpr unsigned WS_KVP = WS_KVH + 128 * 40960;
constexpr unsigned WS_O = WS_KVP + 128 * 6784;
constexpr unsigned WS_END = WS_O + 2 * SEQ * DM * 2;
constexpr unsigned OOB = 0x7FFF0000u;

DI unsigned pk2(float lo, float hi) {
  bf16x2_t r = __builtin_convertvector((f32x2){lo, hi}, bf16x2_t);
  return __builtin_bit_cast(unsigned, r);
}
DI float rbf(float x) { return __builtin_bit_cast(float, pk2(0.f, x)); }
DI float fmax3(float a, float b, float c) { return __builtin_elementwise_maximum(__builtin_elementwise_maximum(a, b), c); }
DI s16x4 trread(const LAS char* p) { return __builtin_amdgcn_ds_read_tr16_b64_v4i16((LAS s16x4*)p); }
DI bf16x8 cat8(s16x4 a, s16x4 b) { return __builtin_shufflevector(a, b, 0, 1, 2, 3, 4, 5, 6, 7); }
DI bf16x8 asfrag(u32x4 v) { return __builtin_bit_cast(bf16x8, v); }
DI bf16x8 frag2(u32x2 a, u32x2 b) { return asfrag((u32x4){a[0], a[1], b[0], b[1]}); }
DI rsrc_t mkrsrc(const void* p, unsigned bytes) { return __builtin_amdgcn_make_buffer_rsrc((void*)p, (short)0, (int)bytes, 0x00020000); }
DI void dma16(rsrc_t rs, unsigned lds, unsigned voff, unsigned soff) {
  asm volatile("s_mov_b32 m0, %0\n\ts_nop 0\n\tbuffer_load_dwordx4 %1, %2, %3 offen lds"
               :: "s"(lds), "v"(voff), "s"(rs), "s"(soff) : "memory");
}
DI void dma16c(rsrc_t rs, unsigned lds, unsigned voff, unsigned soff) {
  asm volatile("s_mov_b32 m0, %0\n\ts_nop 0\n\tbuffer_load_dwordx4 %1, %2, %3 offen sc1 lds"
               :: "s"(lds), "v"(voff), "s"(rs), "s"(soff) : "memory");
}
DI void touch(rsrc_t rs, unsigned lds, unsigned voff, unsigned soff) {
  asm volatile("s_mov_b32 m0, %0\n\ts_nop 0\n\tbuffer_load_dword %1, %2, %3 offen lds"
               :: "s"(lds), "v"(voff), "s"(rs), "s"(soff) : "memory");
}
template <unsigned LO, unsigned HI>
DI void dma16m(rsrc_t rs, unsigned lds, unsigned voff, unsigned soff) {
  asm volatile("s_mov_b32 m0, %0\n\ts_mov_b32 exec_lo, %4\n\ts_mov_b32 exec_hi, %5\n\tbuffer_load_dwordx4 %1, %2, %3 offen lds\n\ts_mov_b64 exec, -1"
               :: "s"(lds), "v"(voff), "s"(rs), "s"(soff), "i"(LO), "i"(HI) : "memory");
}
DI void ld8(u32x2& d, rsrc_t rs, unsigned voff, unsigned soff) {
  asm volatile("buffer_load_dwordx2 %0, %1, %2, %3 offen" : "=v"(d) : "v"(voff), "s"(rs), "s"(soff) : "memory");
}
DI void ld16(u32x4& d, rsrc_t rs, unsigned voff, unsigned soff) {
  asm volatile("buffer_load_dwordx4 %0, %1, %2, %3 offen" : "=v"(d) : "v"(voff), "s"(rs), "s"(soff) : "memory");
}
DI void ld16c(u32x4& d, rsrc_t rs, unsigned voff, unsigned soff) {
  asm volatile("buffer_load_dwordx4 %0, %1, %2, %3 offen sc1" : "=v"(d) : "v"(voff), "s"(rs), "s"(soff) : "memory");
}
DI void st16c(rsrc_t rs, unsigned voff, u32x4 v) { __builtin_amdgcn_raw_buffer_store_b128(v, rs, voff, 0, 16); }
DI void st8c(rsrc_t rs, unsigned voff, u32x2 v) { __builtin_amdgcn_raw_buffer_store_b64(v, rs, voff, 0, 16); }
DI u32x2 lds8(const LAS char* p) { return *(const volatile LAS u32x2*)p; }
DI u32x2 next_tag(u32x2 g) { return (u32x2){g[0] * 1664525u + 1013904223u, g[1] * 22695477u + 2654435761u}; }
#define MFMA16(a, b, c) __builtin_amdgcn_mfma_f32_16x16x32_bf16((a), (b), (c), 0, 0, 0)
#define MFMA32(a, b, c) __builtin_amdgcn_mfma_f32_32x32x16_bf16((a), (b), (c), 0, 0, 0)
#define MFMA32K8(a, b, c) __builtin_amdgcn_mfma_f32_32x32x8bf16_1k(__builtin_bit_cast(s16x4, a), __builtin_bit_cast(s16x4, b), (c), 0, 0, 0)
#define WAIT_BAR(N) asm volatile("s_waitcnt vmcnt(" #N ") lgkmcnt(0)\n\ts_barrier" ::: "memory")
#define LBAR() asm volatile("s_waitcnt lgkmcnt(0)\n\ts_barrier" ::: "memory")
#define SB() __builtin_amdgcn_sched_barrier(0)
#define PIN8(a) asm volatile("" : "+v"(a[0]), "+v"(a[1]), "+v"(a[2]), "+v"(a[3]), "+v"(a[4]), "+v"(a[5]), "+v"(a[6]), "+v"(a[7]))

#define STAMP_DECL
#define STAMP(kid, i)
#define STAMP_FLUSH(kid)

constexpr float LOG2E = 1.4426950408889634f;

struct WFrag { bf16x8 t0k0, t1k0; u32x2 t0k1, t1k1; };
DI WFrag load_wfrag(const LAS char* a0  , const LAS char* a1  , int hh, int lane) {
  u32x2 r0 = __builtin_bit_cast(u32x2, trread(a0)), r1 = __builtin_bit_cast(u32x2, trread(a0 + 160));
  u32x2 r2 = __builtin_bit_cast(u32x2, trread(a1)), r3 = __builtin_bit_cast(u32x2, trread(a1 + 160));
  u32x2 r4 = __builtin_bit_cast(u32x2, trread(a0 + 640)), r5 = __builtin_bit_cast(u32x2, trread(a1 + 640));
  const unsigned sg = (lane & 4) ? 0x80008000u : 0u;
  const unsigned km = hh ? 0u : 0xFFFFFFFFu;
  WFrag w;
  w.t0k0 = asfrag((u32x4){r0[0] ^ sg, r0[1] ^ sg, r1[0] ^ sg, r1[1] ^ sg});
  w.t1k0 = asfrag((u32x4){r2[0] ^ sg, r2[1] ^ sg, r3[0] ^ sg, r3[1] ^ sg});
  w.t0k1 = (u32x2){(r4[0] ^ sg) & km, (r4[1] ^ sg) & km};
  w.t1k1 = (u32x2){(r5[0] ^ sg) & km, (r5[1] ^ sg) & km};
  return w;
}
struct HProj { f32x16 p0, p1; };
struct HRow { bf16x8 x0; u32x2 x1; };
DI HRow hh_read(const LAS char* p, const LAS char* p4) { HRow r; r.x0 = frag2(lds8(p), lds8(p + 1024)); r.x1 = lds8(p4); return r; }
DI HProj hh_proj(const WFrag& w, const HRow& x) {
  const f32x16 z = {};
  HProj h;
  h.p0 = MFMA32(w.t0k0, x.x0, z);
  h.p1 = MFMA32(w.t1k0, x.x0, z);
  h.p0 = MFMA32K8(w.t0k1, x.x1, h.p0);
  h.p1 = MFMA32K8(w.t1k1, x.x1, h.p1);
  return h;
}
template <int N, int NV> DI void mix_mfma() {
#pragma unroll
  for (int i = 0; i < N; ++i) { __builtin_amdgcn_sched_group_barrier(0x8, 1, 0); __builtin_amdgcn_sched_group_barrier(0x402, NV, 0); }
}
DI void hh_soft(const HProj& h, unsigned (&ph)[10]) {
  f32x2 v[10];
#pragma unroll
  for (int i = 0; i < 8; ++i) v[i] = (f32x2){rbf(h.p0[2 * i]), rbf(h.p0[2 * i + 1])};
#pragma unroll
  for (int i = 0; i < 2; ++i) v[8 + i] = (f32x2){rbf(h.p1[2 * i]), rbf(h.p1[2 * i + 1])};
  float m = __builtin_elementwise_maximum(v[0][0], v[0][1]);
#pragma unroll
  for (int i = 1; i < 10; ++i) m = fmax3(m, v[i][0], v[i][1]);
  const float nm = -m * LOG2E;
#pragma unroll
  for (int i = 0; i < 10; ++i) {
    v[i] = __builtin_elementwise_fma(v[i], (f32x2){LOG2E, LOG2E}, (f32x2){nm, nm});
    v[i] = (f32x2){__builtin_amdgcn_exp2f(v[i][0]), __builtin_amdgcn_exp2f(v[i][1])};
  }
  const f32x2 s = (((v[0] + v[1]) + (v[2] + v[3])) + ((v[4] + v[5]) + (v[6] + v[7]))) + (v[8] + v[9]);
  const float r = __builtin_amdgcn_rcpf(s[0] + s[1]);
#pragma unroll
  for (int i = 0; i < 10; ++i) { v[i] *= (f32x2){r, r}; ph[i] = pk2(v[i][0], v[i][1]); }
}

constexpr float SC2 = 0.22360679774997896f * 1.4426950408889634f;

DI unsigned o_off(int h, int srow) { return (unsigned)((srow >> 4) * 20480 + (srow & 15) * 16 + 5 * (h >> 3) * 1024 + ((h >> 1) & 3) * 256); }
DI void store_o(rsrc_t ro, unsigned v16, unsigned v8, const float (&o)[12]) {
  st16c(ro, v16, (u32x4){pk2(o[0], o[1]), pk2(o[2], o[3]), pk2(o[4], o[5]), pk2(o[6], o[7])});
  st8c(ro, v8, (u32x2){pk2(o[8], o[9]), pk2(o[10], o[11])});
}
#define PIN(x) asm volatile("" : "+v"(x))
DI void wait_flags(rsrc_t rin, unsigned voff, bool need, u32x2 tag) {
#pragma unroll 1
  for (int spin = 0; spin < (1 << 20); ++spin) {
    u32x2 f;
    asm volatile("buffer_load_dwordx2 %0, %1, %2, 0 offen sc1\n\ts_waitcnt vmcnt(0)" : "=&v"(f) : "v"(voff), "s"(rin) : "memory");
    if (__all(!need || (f[0] == tag[0] && f[1] == tag[1]))) break;
    __builtin_amdgcn_s_sleep(1);
  }
}

constexpr unsigned CODE_BYTES = 24 * 1024;
constexpr int G_STAGE = 16384 + 30720;
constexpr int KP = 1024, KH = 5 * KP, VP = 1056, VH = 5376, SEG_V = 4 * KH, SEG = SEG_V + 4 * VH;
constexpr int L_LOC = 512, L_Q = L_LOC + SEG, L_NEAR = L_Q + 20480, L_FAR = L_NEAR + SEG;
constexpr int A_PHI = L_FAR, A_KV = L_FAR;
constexpr int L_ONE = L_FAR + 8 * 5632 + 160, L_W = L_ONE + 1024 + 96, L_G = L_W + 4096, L_T = L_G + 1024, L_FB = L_T + 256, L_S = L_FB + 16, K_LDS = L_S + 1024;
constexpr int P_W = 512, P_X = L_FAR;
static_assert(3 * G_STAGE + 64 <= L_W && K_LDS <= 160 * 1024 - 2048 && P_W + 20 * 5120 + 64 <= L_FAR && P_W + 12 * 5120 <= L_NEAR && P_W + 13 * 5120 >= L_NEAR, "LDS map");
static_assert(L_LOC % 256 == 0 && SEG % 256 == 0 && SEG_V % 256 == 0 && VH % 256 == 0 && L_NEAR % 256 == 0 && L_ONE % 256 == 160 && L_W % 16 == 0, "banks");

DI f32x16 qk_tile(const LAS char* kh, const LAS char* k4, bf16x8 q0, u32x2 q1, f32x16 c) {
  const u32x2 a = lds8(kh), b = lds8(kh + KP), d = lds8(k4);
  c = MFMA32(frag2(a, b), q0, c);
  return MFMA32K8(d, q1, c);
}
DI f32x16 pv_tile(const LAS char* vp, const f32x16& s, f32x16 o) {
  const bf16x8 v0 = cat8(trread(vp), trread(vp + 64)), v1 = cat8(trread(vp + 128), trread(vp + 192));
  unsigned pw[8];
#pragma unroll
  for (int i = 0; i < 8; ++i) pw[i] = pk2(__builtin_amdgcn_exp2f(s[2 * i]), __builtin_amdgcn_exp2f(s[2 * i + 1]));
  o = MFMA32(v0, asfrag((u32x4){pw[0], pw[1], pw[2], pw[3]}), o);
  return MFMA32(v1, asfrag((u32x4){pw[4], pw[5], pw[6], pw[7]}), o);
}
struct Strm { const LAS char* kh; const LAS char* k4; bf16x8 q0; u32x2 q1; };
struct KFr { u32x2 a, b, d; };
DI KFr kread(const Strm& s, int off) { KFr k; k.a = lds8(s.kh + off); k.b = lds8(s.kh + off + KP); k.d = lds8(s.k4 + off); return k; }
DI void tile_step(f32x16& o, const f32x16& sc, const LAS char* vp, f32x16& sn, const Strm& n, KFr& k, const Strm& ks, int koff) {
  const f32x16 z = {};
  const bf16x8 v0 = cat8(trread(vp), trread(vp + 64)), v1 = cat8(trread(vp + 128), trread(vp + 192));
  float e[16];
#pragma unroll
  for (int i = 0; i < 3; ++i) e[i] = __builtin_amdgcn_exp2f(sc[i]);
  asm volatile("" : "+v"(e[0]), "+v"(e[1]), "+v"(e[2]));
  SB();
  sn = MFMA32(frag2(k.a, k.b), n.q0, z);
#pragma unroll
  for (int i = 3; i < 7; ++i) e[i] = __builtin_amdgcn_exp2f(sc[i]);
  asm volatile("" : "+v"(e[3]), "+v"(e[4]), "+v"(e[5]), "+v"(e[6]));
  SB();
  sn = MFMA32K8(k.d, n.q1, sn);
  k = kread(ks, koff);
#pragma unroll
  for (int i = 7; i < 10; ++i) e[i] = __builtin_amdgcn_exp2f(sc[i]);
  u32x4 p0 = {pk2(e[0], e[1]), pk2(e[2], e[3]), pk2(e[4], e[5]), pk2(e[6], e[7])};
  asm volatile("" : "+v"(e[8]), "+v"(e[9]), "+v"(p0));
  SB();
  o = MFMA32(v0, asfrag(p0), o);
#pragma unroll
  for (int i = 10; i < 16; ++i) e[i] = __builtin_amdgcn_exp2f(sc[i]);
  o = MFMA32(v1, asfrag((u32x4){pk2(e[8], e[9]), pk2(e[10], e[11]), pk2(e[12], e[13]), pk2(e[14], e[15])}), o);
  SB();
}
struct Pipe { f32x16 s; KFr k; };
DI Pipe prime(const Strm& s, const f32x16& c0, int off, int step) {
  Pipe p;
  p.k = kread(s, off + step);
  p.s = qk_tile(s.kh + off, s.k4 + off, s.q0, s.q1, c0);
  return p;
}
DI void run_tiles(f32x16& o, const Strm& s, Pipe& p, const LAS char* vp, int off, int step, int n) {
  f32x16 sb;
#pragma unroll 1
  for (;;) {
    tile_step(o, p.s, vp, sb, s, p.k, s, off + 2 * step);
    if (n == 1) break;
    tile_step(o, sb, vp + step, p.s, s, p.k, s, off + 3 * step);
    if (n == 2) break;
    n -= 2; off += 2 * step; vp += 2 * step;
  }
}

__global__ __launch_bounds__(512, 2) void k1_fused(const __hip_bfloat16* __restrict__ xA, const __hip_bfloat16* __restrict__ wA,
                                                   const __hip_bfloat16* __restrict__ xB, const __hip_bfloat16* __restrict__ wB,
                                                   const __hip_bfloat16* __restrict__ wphi, const __hip_bfloat16* __restrict__ woA,
                                                   const __hip_bfloat16* __restrict__ woB, char* __restrict__ ws,
                                                   __hip_bfloat16* __restrict__ out) {
  __shared__ __attribute__((aligned(16))) char smem[K_LDS];
  const LAS char* sm = (const LAS char*)smem;
  LAS char* smw = (LAS char*)smem;
  const unsigned lds0 = (unsigned)(size_t)(LAS char*)smem;
  const int tid = threadIdx.x, lane = tid & 63;
  const int wid = __builtin_amdgcn_readfirstlane(tid >> 6);
  const int xcd = blockIdx.x & 7, idx = blockIdx.x >> 3;
  const int br = idx & 1, sub = idx >> 1;
  const int mt = (xcd >> 2) * 8 + (sub & 7), nt = (xcd & 3) * 2 + (sub >> 3);
  const int mt3 = xcd * 2 + (sub & 1), nt3 = sub >> 1;
  const int me = blockIdx.x;
  const int g = lane >> 4, fr = lane & 15, q4 = fr >> 2, p4 = lane & 3;
  STAMP_DECL;
  STAMP(0, 0);
  {
    unsigned long long pc;
    asm volatile("s_getpc_b64 %0" : "=s"(pc));
    if (wid == 0) touch(mkrsrc((const void*)pc, CODE_BYTES), lds0 + L_T, lane < 6 ? (unsigned)((lane * 32 + idx) * 128) : OOB, 0);
  }
  const char* wg = (const char*)(br ? wB : wA);
  const rsrc_t rx = mkrsrc((const char*)(br ? xB : xA) + (size_t)mt * 128 * 1280, 128 * 1280);
  const rsrc_t rw = mkrsrc(wg + nt * 160, 640 * 3840 - nt * 160);
  const unsigned vx0 = (unsigned)((2 * (lane >> 3)) * 1280 + (((lane & 7) ^ g) * 16));
  const unsigned vx1 = (unsigned)((16 + 2 * (lane >> 3)) * 1280 + (((lane & 7) ^ (4 + g)) * 16));
  const int wr = lane >= 30 ? 1 : 0, wc = lane - 30 * wr, wsg = (wc >= 10) + (wc >= 20);
  const unsigned vw = (unsigned)(wr * 3840 + wsg * 1280 + (wc - 10 * wsg) * 16);
  const unsigned sx = (unsigned)((32 * (wid >> 1) + (wid & 1)) * 1280);
  const unsigned sw = (unsigned)((16 * (wid >> 1) + 4 * (wid & 1)) * 3840);
  const unsigned lx = lds0 + wid * 2048, lw = lds0 + 16384 + wid * 3840;
#define G_DMA(i, bo, xo, wo) do { \
    if ((i) == 0 || (i) == 3) dma16(rx, lx + (bo) + ((i) ? 1024 : 0), (i) ? vx1 : vx0, (xo)); \
    else { const int n_ = (i) < 3 ? (i) - 1 : (i) - 2; \
      dma16m<0xFFFFFFFFu, 0x0FFFFFFFu>(rw, lw + (bo) + n_ * 960, vw, (wo) + ((n_ >> 1) * 8 + (n_ & 1) * 2) * 3840); } } while (0)
#pragma unroll
  for (int i = 0; i < 6; ++i) G_DMA(i, 0, sx, sw);
  const int bi = mt * 8 + nt;
  const rsrc_t rt = (wid & 4) ? mkrsrc(wg, 640 * 3840) : mkrsrc(br ? xB : xA, SEQ * 1280);
  const unsigned tst = (wid & 4) ? 245760u : 128u;
  unsigned vt = (wid & 4) ? (fr < 15 ? (unsigned)((bi * 15 + fr) * 128 + g * 245760) : OOB) : (unsigned)((bi * 16 + fr) * 1280 + g * 128);
  if ((wid & 3) == 2) vt = g == 0 ? vt + 4 * tst : OOB;
  if ((wid & 3) >= 2) touch(rt, lds0 + L_T, vt, tst);
  vt = (wid & 4) ? (lane < 60 ? (unsigned)(((bi >> 2) * 60 + lane) * 128) : OOB) : (unsigned)(((bi >> 2) * 64 + lane) * 1280);

  const int wm = wid >> 2, wn = wid & 3;
  const LAS char* xa0 = sm + (64 * wm + fr) * 128 + ((g ^ (fr >> 1)) * 16);
  const LAS char* xa1 = sm + (((64 * wm + fr) * 128 + ((g ^ (fr >> 1)) * 16)) ^ 64);
  const LAS char* wa = sm + 16384 + (16 * (g >> 1) + 4 * (g & 1) + q4) * 480 + 8 * p4 + wn * 128;
  f32x4 acc[4][4] = {};
#pragma unroll
  for (int i = 0; i < 4; ++i) asm volatile("" : "+v"(acc[i][0]), "+v"(acc[i][1]), "+v"(acc[i][2]), "+v"(acc[i][3]));
  int eb[4];
#pragma unroll
  for (int j = 0; j < 4; ++j) {
    const int qd = 16 * wn + 4 * j + g, r = qd - 40;
    const int v = L_LOC + SEG_V + r * VP + ((r * 13) >> 6) * 96;
    eb[j] = (qd < 20 ? L_Q + qd * 1024 : qd < 40 ? L_LOC + (qd - 20) * KP : qd < 60 ? v : L_S) + wm * 512 + fr * 16;
    asm volatile("" : "+v"(eb[j]));
  }
  int cons;
  {
    const int cm = br ? mt + 1 + lane : lane;
    const int c1 = (((((nt & 1) * 8 + (cm & 7)) << 1) | br) << 3) | (((cm >> 3) & 1) * 4 + (nt >> 1));
    const int c2 = (((((lane & 7) * 2 + (mt & 1)) << 1) | br) << 3) | (mt >> 1);
    cons = lane < 16 ? ((cm < 16 && (!br || lane < 2)) ? c1 : -1) : (lane < 24 ? c2 : lane == 63 ? me : -1);
    asm volatile("" : "+v"(cons));
  }
  const int hh = lane >> 5, r32 = lane & 31, b16 = (lane >> 4) & 1, vq = 4 * b16 + p4;
  unsigned vflag = (unsigned)(WS_IN + cons * 256 + (lane >= 16 ? 16 + nt : br ? 1 - lane : mt) * 8);
  unsigned vf1 = (lane < 16 && cons >= 0) ? vflag : OOB, vf2 = (lane >= 16 && lane < 24) ? vflag : OOB;
  const int lq = (lane >= 10) + (lane >= 20) + (lane >= 30);
  unsigned vwo = (unsigned)(lq * 1280 + (lane - 10 * lq) * 16);
  PIN(vf1); PIN(vf2); PIN(vwo);
  const int hs = wid >> 2, qi = hs ? 3 - (wid & 3) : (wid & 3);
  const int hl = wid >> 1, rw0 = 64 * (wid & 1);
  int c0, c1, c2, c3, c4, c5, c6;
  unsigned v16, v8;
  if (br) {
    c0 = hs * KH + hh * 2 * KP + r32 * 8;
    c1 = hs * KH + 4 * KP + r32 * 8;
    c2 = (vq < 5 ? SEG_V + hs * VH + vq * VP : L_ONE) + (4 * hh + q4) * 8;
    c3 = vq < 5 ? -1 : 0;
    c4 = L_Q + hs * 5120 + (32 * qi + r32) * 8;
    c5 = L_Q + (2 + hs) * 5120 + (32 * (3 - qi) + r32) * 8;
    c6 = hh * 2048;
    v16 = o_off(4 * nt + hs, mt * 128 + 32 * qi + r32);
  } else {
    c0 = L_W + hl * 800 + (8 * hh + q4) * 40 + (2 * b16 + (p4 >> 1)) * 8;
    c1 = L_W + hl * 800 + (8 * hh + q4) * 40 + 32;
    c2 = hl * 5120 + (rw0 + r32) * 8;
    c3 = A_PHI + wid * 5632 + r32 * 88 + hh * 40;
    c4 = A_PHI + wid * 5632 + (8 * hh + q4) * 88 + (16 * b16 + 4 * p4) * 2;
    c5 = (vq < 5 ? L_LOC + SEG_V + hl * VH + vq * VP : L_ONE) + (rw0 + 8 * hh + q4) * 8;
    c6 = A_PHI + wid * 5632 + min(r32, 21) * 160 + hh * 16;
    v16 = o_off(4 * nt + hl, mt * 128 + rw0 + r32);
  }
  {
    const int odd = br ? hs : (hl & 1);
    v8 = hh ? OOB : (v16 & ~1023u) + 2048 + 512 * odd + ((v16 & 1023u) >> 1);
    v16 += (hh + 3 * odd) * 1024;
  }
  PIN(c0); PIN(c1); PIN(c2); PIN(c3); PIN(c4); PIN(c5); PIN(c6); PIN(v16); PIN(v8);
  f32x16 mle, mgt;
#pragma unroll
  for (int e = 0; e < 16; ++e) {
    const bool le = (e & 3) + 8 * (e >> 2) + 4 * hh <= r32;
    mle[e] = le ? 0.f : -INFINITY;
    mgt[e] = le ? -INFINITY : 0.f;
  }
  PIN(mle); PIN(mgt);
  const rsrc_t rg = mkrsrc(ws + WS_G, WS_KVH);
  const rsrc_t rph = mkrsrc((const char*)wphi + nt * 3200, 3200);
  bf16x8 x0[4], x1[4];
  s16x4 w0[4][2], w1[4][2];
#define G_RD(X, W, xa, kk, r) do { \
    if ((r) < 2) W[0][(r)] = trread(wa + ((kk) * 32 + (r) * 8) * 480); \
    else if ((r) < 6) X[(r) - 2] = *(const LAS bf16x8*)(xa + ((r) - 2) * 2048); \
    else if ((r) < 12) W[((r) - 4) >> 1][(r) & 1] = trread(wa + ((kk) * 32 + ((r) & 1) * 8) * 480 + (((r) - 4) >> 1) * 32); } while (0)
#define G_MM(X, W, m) acc[(m) & 3][(m) >> 2] = MFMA16(cat8(W[(m) >> 2][0], W[(m) >> 2][1]), X[(m) & 3], acc[(m) & 3][(m) >> 2])
  asm volatile("s_barrier" ::: "memory");
#pragma unroll
  for (int i = 0; i < 6; ++i) G_DMA(i, G_STAGE, sx + 128, sw + 245760);
  if ((wid & 3) >= 2) { WAIT_BAR(7); } else { WAIT_BAR(6); }
  STAMP(0, 1);
#pragma unroll
  for (int r = 0; r < 12; ++r) G_RD(x0, w0, xa0, 0, r);
  int bo = 2 * G_STAGE;
  int bd = bo;
  unsigned xo = sx + 256, wo = sw + 2 * 245760;
#pragma unroll
  for (int i = 0; i < 3; ++i) G_DMA(i, bo, xo, wo);
#pragma unroll 1
  for (int t = 0; t < 10; ++t) {
    SB();
#pragma unroll
    for (int m = 0; m < 12; ++m) {
      G_MM(x0, w0, m); G_RD(x1, w1, xa1, 1, m);
      if (m % 3 == 2 && m < 9) G_DMA(3 + m / 3, bd, xo, wo);
      SB();
    }
    if (wn < 3) { G_MM(x0, w0, 12); G_MM(x0, w0, 13); G_MM(x0, w0, 14); G_MM(x0, w0, 15); }
    const int d = bo == G_STAGE ? -2 * G_STAGE : G_STAGE;
    xa0 += d; xa1 += d; wa += d;
    bo = bo == 2 * G_STAGE ? 0 : bo + G_STAGE;
    bd = t < 7 ? bo : 0x30000;
    xo = t < 7 ? xo + 128 : OOB;
    wo += 245760;
    WAIT_BAR(6);
    SB();
#pragma unroll
    for (int m = 0; m < 12; ++m) {
      G_MM(x1, w1, m); G_RD(x0, w0, xa0, 0, m);
      if (m % 3 == 2 && m < 9) G_DMA(m / 3, bd, xo, wo);
      SB();
    }
    if (wn < 3) { G_MM(x1, w1, 12); G_MM(x1, w1, 13); G_MM(x1, w1, 14); G_MM(x1, w1, 15); }
    if (t == (bi & 3) + TB) {
      if ((wid & 3) == 3) touch(rt, lds0 + L_T, vt, (6 - TB + t) * tst);
      if (wid == 1) touch(mkrsrc(br ? woB : woA, 640 * 1280), lds0 + L_T, lane < 50 ? (unsigned)((bi * 50 + lane) * 128) : OOB, 0);
      if (wid == 0) dma16(rg, lds0 + L_G, cons >= 0 ? cons * 16 : OOB, 0);
      if (wid == 5 && !br) {
#pragma unroll
        for (int i = 0; i < 4; ++i) dma16(rph, lds0 + L_W + i * 1024, (unsigned)lane * 16, i * 1024);
      }
    }
  }
  STAMP(0, 2);
  {
    const rsrc_t rh = mkrsrc(ws + WS_KVH + (mt * 8 + nt) * 40960, 40960);
    const int v0 = (16 * wn + g - 20) * 1024 + wm * 512 + fr * 16;
#pragma unroll
    for (int j = 3; j >= 0; --j) {
      if (br && 4 * wn + j < 5) {
#pragma unroll
        for (int i = 0; i < 4; ++i) acc[i][j] *= SC2;
      }
#pragma unroll
      for (int i = 0; i < 2; ++i) {
        const f32x4 a = acc[2 * i][j], b = acc[2 * i + 1][j];
        const u32x4 ev = (u32x4){pk2(a[0], a[1]), pk2(a[2], a[3]), pk2(b[0], b[1]), pk2(b[2], b[3])};
        if (br && 4 * wn + j >= 5) st16c(rh, (unsigned)(v0 + j * 4096 + i * 256), ev);
        *(LAS u32x4*)(smw + eb[j] + i * 256) = ev;
      }
    }
  }
  if (tid < 128) *(LAS u32x2*)(smw + L_ONE + tid * 8) = (u32x2){0x3F80u, 0u};
  if (tid == 511) *(LAS unsigned*)(smw + L_FB) = 0u;
  const u32x2 gc = next_tag(*(const LAS u32x2*)(sm + L_G + lane * 16));
  const u32x2 tag = {(unsigned)__builtin_amdgcn_readlane(gc[0], 63), (unsigned)__builtin_amdgcn_readlane(gc[1], 63)};
  const char* wog = (const char*)(br ? woB : woA);
  const rsrc_t ro = mkrsrc(ws + WS_O + br * (SEQ * DM * 2), SEQ * DM * 2);
  const rsrc_t rwo = mkrsrc(wog + nt3 * 160, 640 * 1280 - nt3 * 160);
  const int sg = 2 * (wid >> 2) + (wid & 1), st = (wid >> 1) & 1;
#define P_DMA2(w5, G) dma16m<0xFFFFFFFFu, 0xFFu>(rwo, lds0 + P_W + (4 * (w5) + (G)) * 5120 + wid * 640, vwo, \
      (unsigned)(160 * (G) + 4 * (w5) + ((w5) >= 2 ? 8 : 0) + 40 * sg + st * ((w5) == 2 ? 20 : 8)) * 1280u)
#define P_DMA(b) P_DMA2((b) >> 2, (b) & 3)
  if (br) {
    WAIT_BAR(0);
    if (wid == 0) st8c(rg, vf1, gc);
    STAMP(0, 3);
    Strm S[2];
#pragma unroll
    for (int r = 0; r < 2; ++r) {
      S[r].kh = sm + c0 + r * 2 * KH;
      S[r].k4 = sm + c1 + r * 2 * KH;
      const LAS char* qp = sm + (r ? c5 : c4);
      S[r].q0 = frag2(lds8(qp + c6), lds8(qp + c6 + 1024));
      const u32x2 t4 = lds8(qp + 4096);
      S[r].q1 = hh ? (u32x2){0u, 0u} : t4;
    }
#define VPTR(r, seg) (sm + c2 + (((seg) + (r) * 2 * VH) & c3))
#define HALO(tile, seg, pg) do { \
      const rsrc_t rn_ = mkrsrc(ws + WS_KVH + (((tile) >= 0 ? (tile) : 0) * 8 + nt) * 40960 + (pg) * 10240, (tile) >= 0 ? 10240 : 0); \
      const unsigned hd_ = lds0 + (seg) + ((pg) >= 2 ? SEG_V + ((pg) & 1) * 2 * VH : ((pg) & 1) * 2 * KH), hp_ = (pg) >= 2 ? VP : KP; \
      _Pragma("unroll") for (int i = 0; i < 10; ++i) dma16c(rn_, hd_ + i * hp_ + (i >= 5 ? ((pg) >> 1) * 96 : 0), lane * 16, i * 1024); } while (0)
    f32x16 o0 = {}, o1 = {};
    Pipe p0 = prime(S[0], mle, L_LOC + qi * 256, -256), p1 = prime(S[1], mle, L_LOC + (3 - qi) * 256, -256);
    run_tiles(o0, S[0], p0, VPTR(0, L_LOC) + qi * 256, L_LOC + qi * 256, -256, qi + 1);
    STAMP(0, 4);
    const int nb = qi < 2 ? 1 : 0, pg = 2 * hs + (qi & 1);
    wait_flags(rg, (unsigned)(WS_IN + me * 256 + nb * 8), mt - 2 + nb >= 0, tag);
    STAMP(0, 5);
    HALO(mt - 2 + nb, nb ? L_NEAR : L_FAR, pg);
    run_tiles(o1, S[1], p1, VPTR(1, L_LOC) + (3 - qi) * 256, L_LOC + (3 - qi) * 256, -256, 4 - qi);
    STAMP(0, 6);
    if (qi < 2) { WAIT_BAR(0); } else { LBAR(); }
    STAMP(0, 7);
    if (mt > 0) {
      const f32x16 z = {};
      KFr k = kread(S[1], L_NEAR);
      f32x16 sa = qk_tile(S[0].kh + L_NEAR, S[0].k4 + L_NEAR, S[0].q0, S[0].q1, z), sb;
      const LAS char* v0 = VPTR(0, L_NEAR);
      const LAS char* v1 = VPTR(1, L_NEAR);
#pragma unroll 1
      for (int off = 0; off < 1024; off += 256) {
        P_DMA2(0, off >> 8); P_DMA2(1, off >> 8);
        tile_step(o0, sa, v0 + off, sb, S[1], k, S[0], L_NEAR + off + 256);
        P_DMA2(2, off >> 8);
        tile_step(o1, sb, v1 + off, sa, S[0], k, S[1], L_NEAR + off + 256);
        if (off == 512) { WAIT_BAR(9); }
      }
    } else {
#pragma unroll
      for (int ks = 0; ks < 12; ++ks) P_DMA(ks);
    }
    p0 = prime(S[0], mgt, L_FAR + qi * 256, 256);
    p1 = prime(S[1], mgt, L_FAR + (3 - qi) * 256, 256);
    STAMP(0, 8);
    LBAR();
    bool redo[2];
#pragma unroll
    for (int r = 0; r < 2; ++r) {
      const int qt = r ? 3 - qi : qi;
      f32x16& o = r ? o1 : o0;
#pragma unroll
      for (int G = 0; G < 4; ++G) P_DMA2(3 + r, G);
      if (mt > 1) run_tiles(o, S[r], r ? p1 : p0, VPTR(r, L_FAR) + qt * 256, L_FAR + qt * 256, 256, 4 - qt);
      const float l = __uint_as_float(__builtin_amdgcn_permlane32_swap(__float_as_uint(o[8]), __float_as_uint(o[8]), false, false)[1]);
      const float rl = __builtin_amdgcn_rcpf(l);
      float of[12];
#pragma unroll
      for (int i = 0; i < 12; ++i) of[i] = o[i] * rl;
      const unsigned dv = r ? (unsigned)((3 - 2 * qi) * 40960 + 256) : 0u;
      store_o(ro, v16 + dv, hh ? OOB : v8 + dv - (r ? 128 : 0), of);
      float am = __builtin_fabsf(o[0]);
#pragma unroll
      for (int i = 1; i < 11; i += 2) am = fmax3(am, __builtin_fabsf(o[i]), __builtin_fabsf(o[i + 1]));
      am = __builtin_elementwise_maximum(am, __builtin_fabsf(o[11]));
      redo[r] = !__all(l >= 0x1p-80f && l <= FB_HI && am <= 0x1p126f);
    }
    if (redo[0] || redo[1]) *(LAS unsigned*)(smw + L_FB) = 1u;
    STAMP(0, 9);
    WAIT_BAR(0);
    STAMP(0, 10);
    if (__builtin_expect(__builtin_amdgcn_readfirstlane(*(const volatile LAS unsigned*)(sm + L_FB)) != 0u, 0)) {
      HALO(mt - nb, nb ? L_NEAR : L_LOC, pg);
      WAIT_BAR(0);
#pragma unroll
      for (int r = 0; r < 2; ++r) {
        if (!redo[r]) continue;
        const int qt = r ? 3 - qi : qi, w0 = mt >= 2 ? qt : mt == 1 ? 4 : 8;
        f32x16 o;
        float m = -INFINITY;
#pragma unroll 1
        for (int pass = 0; pass < 2; ++pass) {
          o = (f32x16){};
#pragma unroll 1
          for (int w = w0; w <= qt + 8; ++w) {
            const int seg = w < 4 ? L_FAR : w < 8 ? L_NEAR : L_LOC, off = (w & 3) * 256;
            f32x16 c;
#pragma unroll
            for (int e = 0; e < 16; ++e) c[e] = w == qt + 8 ? mle[e] : w == qt ? mgt[e] : 0.f;
            f32x16 s = qk_tile(S[r].kh + seg + off, S[r].k4 + seg + off, S[r].q0, S[r].q1, c);
            if (pass == 0) {
#pragma unroll
              for (int e = 0; e < 16; e += 2) m = fmax3(m, s[e], s[e + 1]);
            } else {
#pragma unroll
              for (int e = 0; e < 16; ++e) s[e] -= m;
              o = pv_tile(VPTR(r, seg) + off, s, o);
            }
          }
          auto rr = __builtin_amdgcn_permlane32_swap(__float_as_uint(m), __float_as_uint(m), false, false);
          m = __builtin_elementwise_maximum(__uint_as_float(rr[0]), __uint_as_float(rr[1]));
        }
        const float rl = __builtin_amdgcn_rcpf(__uint_as_float(__builtin_amdgcn_permlane32_swap(__float_as_uint(o[8]), __float_as_uint(o[8]), false, false)[1]));
        float of[12];
#pragma unroll
        for (int i = 0; i < 12; ++i) of[i] = o[i] * rl;
        const unsigned dv = r ? (unsigned)((3 - 2 * qi) * 40960 + 256) : 0u;
        store_o(ro, v16 + dv, hh ? OOB : v8 + dv - (r ? 128 : 0), of);
      }
      LBAR();
#pragma unroll 1
      for (int G = 0; G < 4; ++G) {
#pragma unroll
        for (int u = 0; u < 5; ++u) P_DMA2(u, G);
      }
      WAIT_BAR(0);
    }
  } else {
    WFrag wf = load_wfrag(sm + c0, sm + c1, hh, lane);
    PIN(wf.t0k0); PIN(wf.t1k0); PIN(wf.t0k1); PIN(wf.t1k1);
#pragma unroll
    for (int ks = 13; ks < 20; ++ks) P_DMA(ks);
    LBAR();
    STAMP(0, 3);
    const LAS char* xp = sm + c2 + hh * 2048;
    const LAS char* x4 = sm + c2 + 4096;
    const HRow rk0 = hh_read(xp + L_LOC, x4 + L_LOC), rk1 = hh_read(xp + L_LOC + 256, x4 + L_LOC + 256);
    const HRow rq0 = hh_read(xp + L_Q, x4 + L_Q), rq1 = hh_read(xp + L_Q + 256, x4 + L_Q + 256);
    const HProj hk0 = hh_proj(wf, rk0);
    bf16x8 vf[4];
#pragma unroll
    for (int ks = 0; ks < 4; ++ks) vf[ks] = cat8(trread(sm + c5 + ks * 128), trread(sm + c5 + ks * 128 + 32));
    SB();
    f32x16 d0 = {}, d1 = {};
    HProj hq0, hq1;
    {
      unsigned ph[10];
      HProj hk1 = hh_proj(wf, rk1);
      hh_soft(hk0, ph);
#pragma unroll
      for (int i = 0; i < 5; ++i) *(LAS u32x2*)(smw + c3 + 8 * i) = (u32x2){ph[2 * i], ph[2 * i + 1]};
      PIN(hk1.p0); PIN(hk1.p1);
      mix_mfma<4, 8>();
      SB();
      bf16x8 pf[4];
#pragma unroll
      for (int i = 0; i < 4; ++i) pf[i] = cat8(trread(sm + c4 + (i >> 1) * 16 * 88 + (i & 1) * 64), trread(sm + c4 + ((i >> 1) * 16 + 4) * 88 + (i & 1) * 64));
      SB();
      hq0 = hh_proj(wf, rq0);
      d0 = MFMA32(pf[0], vf[0], d0); d1 = MFMA32(pf[1], vf[0], d1);
      d0 = MFMA32(pf[2], vf[1], d0); d1 = MFMA32(pf[3], vf[1], d1);
      hh_soft(hk1, ph);
#pragma unroll
      for (int i = 0; i < 5; ++i) *(LAS u32x2*)(smw + c3 + 32 * 88 + 8 * i) = (u32x2){ph[2 * i], ph[2 * i + 1]};
      PIN(hq0.p0); PIN(hq0.p1); PIN(d0); PIN(d1);
      mix_mfma<8, 8>();
      SB();
#pragma unroll
      for (int i = 0; i < 4; ++i) pf[i] = cat8(trread(sm + c4 + (32 + (i >> 1) * 16) * 88 + (i & 1) * 64), trread(sm + c4 + (36 + (i >> 1) * 16) * 88 + (i & 1) * 64));
      d0 = MFMA32(pf[0], vf[2], d0); d1 = MFMA32(pf[1], vf[2], d1);
      d0 = MFMA32(pf[2], vf[3], d0); d1 = MFMA32(pf[3], vf[3], d1);
    }
    *(LAS f32x4*)(smw + c6) = (f32x4){d0[0], d0[1], d0[2], d0[3]};
    *(LAS f32x4*)(smw + c6 + 32) = (f32x4){d0[4], d0[5], d0[6], d0[7]};
    *(LAS f32x4*)(smw + c6 + 64) = (f32x4){d0[8], d0[9], d0[10], d0[11]};
    *(LAS f32x4*)(smw + c6 + 96) = (f32x4){d0[12], d0[13], d0[14], d0[15]};
    *(LAS f32x4*)(smw + c6 + 128) = (f32x4){d1[0], d1[1], d1[2], d1[3]};
    SB();
    hq1 = hh_proj(wf, rq1);
    PIN(hq1.p0); PIN(hq1.p1);
    LBAR();
    STAMP(0, 4);
    const rsrc_t rkv = mkrsrc(ws + WS_KVP + nt * 16 * 6784, 16 * 6784);
    {
      const int e = (wid & 1) * 64 + lane;
      const LAS char* o = sm + A_PHI + (wid & ~1) * 5632 + e * 32;
      const f32x4 a = *(const LAS f32x4*)o + *(const LAS f32x4*)(o + 5632), b = *(const LAS f32x4*)(o + 16) + *(const LAS f32x4*)(o + 5648);
      st16c(rkv, e < 105 ? (unsigned)(mt * 6784 + hl * 1680 + e * 16) : OOB, (u32x4){pk2(a[0], a[1]), pk2(a[2], a[3]), pk2(b[0], b[1]), pk2(b[2], b[3])});
    }
#pragma unroll
    for (int ks = 0; ks < 6; ++ks) P_DMA(ks);
    SB();
    unsigned phq[2][10];
    hh_soft(hq0, phq[0]);
#pragma unroll
    for (int i = 0; i < 10; ++i) PIN(phq[0][i]);
    WAIT_BAR(6);
    if (wid == 0) st8c(rg, vf1, gc);
    if (tid >= 256) *(LAS u32x2*)(smw + A_KV + ((tid - 256) >> 1) * 112 + (tid & 1) * 48 + 40) = (u32x2){0u, 0u};
    STAMP(0, 5);
    hh_soft(hq1, phq[1]);
#pragma unroll
    for (int i = 0; i < 10; ++i) PIN(phq[1][i]);
    int kd0, kd1;
    {
      const int row = tid / 5, oc = tid - row * 5, h4 = row / 21, d = row - h4 * 21;
      kd0 = tid < 420 ? A_KV + h4 * 3584 + d * 112 + oc * 16 + (oc > 2 ? 8 : 0) : L_S;
      kd1 = kd0 + (oc == 2 ? 16 : 8);
      PIN(kd0); PIN(kd1);
    }
    wait_flags(rg, (unsigned)(WS_IN + me * 256 + fr * 8), true, tag);
    STAMP(0, 6);
    if (wid < 7) {
      u32x4 p[16];
#pragma unroll
      for (int i = 0; i < 16; ++i) p[i] = __builtin_amdgcn_raw_buffer_load_b128(rkv, tid * 16, i * 6784, 16);
      f32x2 lo[2] = {}, hi[2] = {};
#pragma unroll
      for (int i = 0; i < 16; ++i)
#pragma unroll
        for (int u = 0; u < 2; ++u) {
          lo[u] += (f32x2){__uint_as_float(p[i][2 * u] << 16), __uint_as_float(p[i][2 * u + 1] << 16)};
          hi[u] += (f32x2){__uint_as_float(p[i][2 * u] & 0xFFFF0000u), __uint_as_float(p[i][2 * u + 1] & 0xFFFF0000u)};
        }
      *(LAS u32x2*)(smw + kd0) = (u32x2){pk2(lo[0][0], hi[0][0]), pk2(lo[0][1], hi[0][1])};
      *(LAS u32x2*)(smw + kd1) = (u32x2){pk2(lo[1][0], hi[1][0]), pk2(lo[1][1], hi[1][1])};
    }
#pragma unroll
    for (int ks = 6; ks < 13; ++ks) P_DMA(ks);
    LBAR();
    STAMP(0, 7);
    const LAS char* kp = sm + A_KV + hl * 3584 + (r32 == 24 ? 20 : r32) * 112 + hh * 48;
    const bf16x8 f0 = *(const LAS bf16x8*)(kp), f1 = *(const LAS bf16x8*)(kp + 16), f2 = *(const LAS bf16x8*)(kp + 32);
    f32x16 n[2];
#pragma unroll
    for (int t = 0; t < 2; ++t) n[t] = MFMA32(f0, asfrag((u32x4){phq[t][0], phq[t][1], phq[t][2], phq[t][3]}), (f32x16){});
#pragma unroll
    for (int t = 0; t < 2; ++t) n[t] = MFMA32(f1, asfrag((u32x4){phq[t][4], phq[t][5], phq[t][6], phq[t][7]}), n[t]);
#pragma unroll
    for (int t = 0; t < 2; ++t) n[t] = MFMA32(f2, asfrag((u32x4){phq[t][8], phq[t][9], 0u, 0u}), n[t]);
#pragma unroll
    for (int t = 0; t < 2; ++t) {
      const float den = rbf(rbf(hh ? n[t][8] : n[t][12]) + 1e-6f);
      const float r = __builtin_amdgcn_rcpf(den);
      float o[12];
#pragma unroll
      for (int i = 0; i < 12; ++i) o[i] = rbf(n[t][i]) * r;
      store_o(ro, v16 + t * 40960, hh ? OOB : v8 + t * 40960, o);
    }
    STAMP(0, 9);
    WAIT_BAR(0);
    STAMP(0, 10);
  }
  {
    if (wid == 0) st8c(rg, vf2, gc);
    STAMP(0, 11);
    int oa[3];
    unsigned og[3];
#pragma unroll
    for (int i = 0; i < 3; ++i) {
      const int c = min(lane + 64 * i, 160), row = c / 10, ch = c - row * 10;
      oa[i] = wid * 2816 + row * 176 + ch * 16;
      og[i] = (unsigned)(row * 2560 + ch * 16);
      PIN(oa[i]); PIN(og[i]);
    }
    const int pm = wid & 3, kh = wid >> 2;
    wait_flags(rg, (unsigned)(WS_IN + me * 256 + 128 + (4 * kh + (lane & 3)) * 8), true, tag);
    STAMP(0, 12);
    const rsrc_t rf = mkrsrc(ws + WS_O + (unsigned)((br * 128 + mt3 * 8 + 2 * pm) * 20 + 10 * kh) * 1024u, 30 * 1024);
    const unsigned vk = lane * 16 + kh * 20480, vh = lane * 16 + (1 - kh) * 20480;
    u32x4 xf[10][2];
    const unsigned vk8 = lane * 8 + kh * 20480, vh8 = lane * 8 + (1 - kh) * 20480;
#define P_LD1(v, v8_, tau) ((tau) % 5 == 2 ? __builtin_bit_cast(u32x4, frag2(__builtin_amdgcn_raw_buffer_load_b64(rf, v8_, (tau) * 1024, 16), \
                                                                             __builtin_amdgcn_raw_buffer_load_b64(rf, v8_, (tau) * 1024 + 512, 16))) \
                                           : __builtin_amdgcn_raw_buffer_load_b128(rf, v, (tau) * 1024, 16))
#define P_LD(tau) do { xf[tau][0] = P_LD1(vk, vk8, tau); xf[tau][1] = P_LD1(vh, vh8, tau); } while (0)
    P_LD(0); P_LD(1); P_LD(2);
    const LAS char* wb = sm + P_W + kh * 10240 + (16 * (g >> 1) + 4 * (g & 1) + q4) * 160 + 8 * p4;
    const LAS char* wb2 = wb + 12 * 5120;
    f32x4 pc[2][5] = {};
    bf16x8 wf[2][5];
#pragma unroll
    for (int j = 0; j < 5; ++j) wf[0][j] = cat8(trread(wb + j * 32), trread(wb + 8 * 160 + j * 32));
#pragma unroll
    for (int tau = 0; tau < 10; ++tau) {
      SB();
      if (tau < 7) P_LD(tau + 3);
#pragma unroll
      for (int j = 0; j < 5; ++j) {
        if (tau < 9) {
          const int blk = ((tau + 1) % 5) * 4 + (tau + 1) / 5;
          const LAS char* wp = (blk >= 12 ? wb2 : wb) + (blk >= 12 ? blk - 12 : blk) * 5120 + j * 32;
          wf[(tau + 1) & 1][j] = cat8(trread(wp), trread(wp + 8 * 160));
        }
        pc[1][j] = MFMA16(wf[tau & 1][j], asfrag(xf[tau][1]), pc[1][j]);
        if (tau < 9) pc[0][j] = MFMA16(wf[tau & 1][j], asfrag(xf[tau][0]), pc[0][j]);
        SB();
      }
    }
#pragma unroll
    for (int j = 0; j < 5; ++j) {
      pc[0][j] = MFMA16(wf[1][j], asfrag(xf[9][0]), pc[0][j]);
      *(LAS f32x4*)(smw + P_X + (wid * 5 + j) * 1024 + lane * 16) = pc[1][j];
    }
    STAMP(0, 13);
    LBAR();
    if (tid == 0) st8c(rg, me * 16, tag);
    LAS char* ow = smw + wid * 2816 + fr * 176 + g * 8;
#pragma unroll
    for (int j = 0; j < 5; ++j) {
      const f32x4 a = pc[0][j] + *(const LAS f32x4*)(sm + P_X + ((wid ^ 4) * 5 + j) * 1024 + lane * 16);
      *(LAS u32x2*)(ow + j * 32) = (u32x2){pk2(a[0], a[1]), pk2(a[2], a[3])};
    }
    const rsrc_t rout = mkrsrc((char*)out + (size_t)(mt3 * 128 + 32 * pm + 16 * kh) * 2560 + br * 1280 + nt3 * 160, 15 * 2560 + 160);
#pragma unroll
    for (int i = 0; i < 3; ++i) __builtin_amdgcn_raw_buffer_store_b128(*(const LAS u32x4*)(sm + oa[i]), rout, og[i], 0, OUT_AUX);
  }
  STAMP(0, 15);
  STAMP_FLUSH(0);
}

extern "C" void kernel_launch(void* const* d_in, const int* in_sizes, int n_in, void* d_out, int out_size, void* d_ws,
                              size_t ws_size, hipStream_t stream) {
  k1_fused<<<256, 512, 0, stream>>>((const __hip_bfloat16*)d_in[0], (const __hip_bfloat16*)d_in[1], (const __hip_bfloat16*)d_in[4],
                                    (const __hip_bfloat16*)d_in[5], (const __hip_bfloat16*)d_in[2], (const __hip_bfloat16*)d_in[3],
                                    (const __hip_bfloat16*)d_in[6], (char*)d_ws, (__hip_bfloat16*)d_out);
}
```

```cpp
#define PSEL 0
#include <hip/hip_runtime.h>
#include <hip/hip_bf16.h>

typedef short bf16x8 __attribute__((ext_vector_type(8)));
typedef short s16x4 __attribute__((ext_vector_type(4)));
typedef float f32x2 __attribute__((ext_vector_type(2)));
typedef float f32x4 __attribute__((ext_vector_type(4)));
typedef float f32x16 __attribute__((ext_vector_type(16)));
typedef unsigned u32x2 __attribute__((ext_vector_type(2)));
typedef unsigned u32x4 __attribute__((ext_vector_type(4)));
typedef __bf16 bf16x2_t __attribute__((ext_vector_type(2)));
typedef __amdgpu_buffer_rsrc_t rsrc_t;
#define LAS __attribute__((address_space(3)))
#define GAS __attribute__((address_space(1)))
#define DI __device__ __forceinline__

#define PFV 6
#define OUT_AUX 0
#define TB 1
#define FB_HI 0x1p100f
#define CPF 1
constexpr int SEQ = 2048, DM = 640, NH = 32;
constexpr unsigned WS_G = 0;
constexpr unsigned WS_IN = 4096;
constexpr unsigned WS_KVH = WS_IN + 256 * 256;
constexpr unsigned WS_KVP = WS_KVH + 128 * 40960;
constexpr unsigned WS_O = WS_KVP + 128 * 6784;
constexpr unsigned WS_END = WS_O + 2 * SEQ * DM * 2;
constexpr unsigned OOB = 0x7FFF0000u;

DI unsigned pk2(float lo, float hi) {
  bf16x2_t r = __builtin_convertvector((f32x2){lo, hi}, bf16x2_t);
  return __builtin_bit_cast(unsigned, r);
}
DI float rbf(float x) { return __builtin_bit_cast(float, pk2(0.f, x)); }
DI float fmax3(float a, float b, float c) { return __builtin_elementwise_maximum(__builtin_elementwise_maximum(a, b), c); }
DI s16x4 trread(const LAS char* p) { return __builtin_amdgcn_ds_read_tr16_b64_v4i16((LAS s16x4*)p); }
DI bf16x8 cat8(s16x4 a, s16x4 b) { return __builtin_shufflevector(a, b, 0, 1, 2, 3, 4, 5, 6, 7); }
DI bf16x8 asfrag(u32x4 v) { return __builtin_bit_cast(bf16x8, v); }
DI bf16x8 frag2(u32x2 a, u32x2 b) { return asfrag((u32x4){a[0], a[1], b[0], b[1]}); }
DI rsrc_t mkrsrc(const void* p, unsigned bytes) { return __builtin_amdgcn_make_buffer_rsrc((void*)p, (short)0, (int)bytes, 0x00020000); }
DI void dma16(rsrc_t rs, unsigned lds, unsigned voff, unsigned soff) {
  asm volatile("s_mov_b32 m0, %0\n\ts_nop 0\n\tbuffer_load_dwordx4 %1, %2, %3 offen lds"
               :: "s"(lds), "v"(voff), "s"(rs), "s"(soff) : "memory");
}
DI void dma16c(rsrc_t rs, unsigned lds, unsigned voff, unsigned soff) {
  asm volatile("s_mov_b32 m0, %0\n\ts_nop 0\n\tbuffer_load_dwordx4 %1, %2, %3 offen sc1 lds"
               :: "s"(lds), "v"(voff), "s"(rs), "s"(soff) : "memory");
}
DI void touch(rsrc_t rs, unsigned lds, unsigned voff, unsigned soff) {
  asm volatile("s_mov_b32 m0, %0\n\ts_nop 0\n\tbuffer_load_dword %1, %2, %3 offen lds"
               :: "s"(lds), "v"(voff), "s"(rs), "s"(soff) : "memory");
}
template <unsigned LO, unsigned HI>
DI void dma16m(rsrc_t rs, unsigned lds, unsigned voff, unsigned soff) {
  asm volatile("s_mov_b32 m0, %0\n\ts_mov_b32 exec_lo, %4\n\ts_mov_b32 exec_hi, %5\n\tbuffer_load_dwordx4 %1, %2, %3 offen lds\n\ts_mov_b64 exec, -1"
               :: "s"(lds), "v"(voff), "s"(rs), "s"(soff), "i"(LO), "i"(HI) : "memory");
}
DI void ld8(u32x2& d, rsrc_t rs, unsigned voff, unsigned soff) {
  asm volatile("buffer_load_dwordx2 %0, %1, %2, %3 offen" : "=v"(d) : "v"(voff), "s"(rs), "s"(soff) : "memory");
}
DI void ld16(u32x4& d, rsrc_t rs, unsigned voff, unsigned soff) {
  asm volatile("buffer_load_dwordx4 %0, %1, %2, %3 offen" : "=v"(d) : "v"(voff), "s"(rs), "s"(soff) : "memory");
}
DI void ld16c(u32x4& d, rsrc_t rs, unsigned voff, unsigned soff) {
  asm volatile("buffer_load_dwordx4 %0, %1, %2, %3 offen sc1" : "=v"(d) : "v"(voff), "s"(rs), "s"(soff) : "memory");
}
DI void st16c(rsrc_t rs, unsigned voff, u32x4 v) { __builtin_amdgcn_raw_buffer_store_b128(v, rs, voff, 0, 16); }
DI void st8c(rsrc_t rs, unsigned voff, u32x2 v) { __builtin_amdgcn_raw_buffer_store_b64(v, rs, voff, 0, 16); }
DI u32x2 lds8(const LAS char* p) { return *(const volatile LAS u32x2*)p; }
DI u32x2 next_tag(u32x2 g) { return (u32x2){g[0] * 1664525u + 1013904223u, g[1] * 22695477u + 2654435761u}; }
#define MFMA16(a, b, c) __builtin_amdgcn_mfma_f32_16x16x32_bf16((a), (b), (c), 0, 0, 0)
#define MFMA32(a, b, c) __builtin_amdgcn_mfma_f32_32x32x16_bf16((a), (b), (c), 0, 0, 0)
#define MFMA32K8(a, b, c) __builtin_amdgcn_mfma_f32_32x32x8bf16_1k(__builtin_bit_cast(s16x4, a), __builtin_bit_cast(s16x4, b), (c), 0, 0, 0)
#define WAIT_BAR(N) asm volatile("s_waitcnt vmcnt(" #N ") lgkmcnt(0)\n\ts_barrier" ::: "memory")
#define LBAR() asm volatile("s_waitcnt lgkmcnt(0)\n\ts_barrier" ::: "memory")
#define SB() __builtin_amdgcn_sched_barrier(0)
#define PIN8(a) asm volatile("" : "+v"(a[0]), "+v"(a[1]), "+v"(a[2]), "+v"(a[3]), "+v"(a[4]), "+v"(a[5]), "+v"(a[6]), "+v"(a[7]))

#define STAMP_DECL unsigned long long ta_ = 0
#define STAMP(kid, i) do { if ((i) == 0) { asm volatile("s_memrealtime %0\n\ts_waitcnt lgkmcnt(0)" : "=s"(ta_) :: "memory"); \
    if (threadIdx.x == 0) __hip_atomic_store((GAS unsigned*)(ws + (32u << 20)) + blockIdx.x, (unsigned)ta_, __ATOMIC_RELAXED, __HIP_MEMORY_SCOPE_AGENT); } } while (0)
#define STAMP_FLUSH(kid) do { if (blockIdx.x == 77 && threadIdx.x < 64) { unsigned long long n_, e_; int lo_ = 1 << 30, hi_ = -(1 << 30); \
    for (int k_ = 0; k_ < 4; ++k_) { int d_ = (int)(__hip_atomic_load((GAS unsigned*)(ws + (32u << 20)) + threadIdx.x + 64 * k_, __ATOMIC_RELAXED, __HIP_MEMORY_SCOPE_AGENT) - (unsigned)ta_); lo_ = min(lo_, d_); hi_ = max(hi_, d_); } \
    for (int o_ = 1; o_ < 64; o_ <<= 1) { lo_ = min(lo_, __shfl_xor(lo_, o_)); hi_ = max(hi_, __shfl_xor(hi_, o_)); } \
    int x_ = __builtin_amdgcn_readfirstlane(PSEL ? hi_ - lo_ : -lo_); x_ = min(max(x_, 0), 5000); \
    asm volatile("s_memrealtime %0\n\ts_waitcnt lgkmcnt(0)" : "=s"(n_) :: "memory"); e_ = n_ + 8 * x_; \
    for (int w_ = 0; w_ < 100000; ++w_) { asm volatile("s_memrealtime %0\n\ts_waitcnt lgkmcnt(0)" : "=s"(n_) :: "memory"); if (n_ >= e_) break; } } } while (0)

constexpr float LOG2E = 1.4426950408889634f;

struct WFrag { bf16x8 t0k0, t1k0; u32x2 t0k1, t1k1; };
DI WFrag load_wfrag(const LAS char* a0  , const LAS char* a1  , int hh, int lane) {
  u32x2 r0 = __builtin_bit_cast(u32x2, trread(a0)), r1 = __builtin_bit_cast(u32x2, trread(a0 + 160));
  u32x2 r2 = __builtin_bit_cast(u32x2, trread(a1)), r3 = __builtin_bit_cast(u32x2, trread(a1 + 160));
  u32x2 r4 = __builtin_bit_cast(u32x2, trread(a0 + 640)), r5 = __builtin_bit_cast(u32x2, trread(a1 + 640));
  const unsigned sg = (lane & 4) ? 0x80008000u : 0u;
  const unsigned km = hh ? 0u : 0xFFFFFFFFu;
  WFrag w;
  w.t0k0 = asfrag((u32x4){r0[0] ^ sg, r0[1] ^ sg, r1[0] ^ sg, r1[1] ^ sg});
  w.t1k0 = asfrag((u32x4){r2[0] ^ sg, r2[1] ^ sg, r3[0] ^ sg, r3[1] ^ sg});
  w.t0k1 = (u32x2){(r4[0] ^ sg) & km, (r4[1] ^ sg) & km};
  w.t1k1 = (u32x2){(r5[0] ^ sg) & km, (r5[1] ^ sg) & km};
  return w;
}
struct HProj { f32x16 p0, p1; };
struct HRow { bf16x8 x0; u32x2 x1; };
DI HRow hh_read(const LAS char* p, const LAS char* p4) { HRow r; r.x0 = frag2(lds8(p), lds8(p + 1024)); r.x1 = lds8(p4); return r; }
DI HProj hh_proj(const WFrag& w, const HRow& x) {
  const f32x16 z = {};
  HProj h;
  h.p0 = MFMA32(w.t0k0, x.x0, z);
  h.p1 = MFMA32(w.t1k0, x.x0, z);
  h.p0 = MFMA32K8(w.t0k1, x.x1, h.p0);
  h.p1 = MFMA32K8(w.t1k1, x.x1, h.p1);
  return h;
}
template <int N, int NV> DI void mix_mfma() {
#pragma unroll
  for (int i = 0; i < N; ++i) { __builtin_amdgcn_sched_group_barrier(0x8, 1, 0); __builtin_amdgcn_sched_group_barrier(0x402, NV, 0); }
}
DI void hh_soft(const HProj& h, unsigned (&ph)[10]) {
  f32x2 v[10];
#pragma unroll
  for (int i = 0; i < 8; ++i) v[i] = (f32x2){rbf(h.p0[2 * i]), rbf(h.p0[2 * i + 1])};
#pragma unroll
  for (int i = 0; i < 2; ++i) v[8 + i] = (f32x2){rbf(h.p1[2 * i]), rbf(h.p1[2 * i + 1])};
  float m = __builtin_elementwise_maximum(v[0][0], v[0][1]);
#pragma unroll
  for (int i = 1; i < 10; ++i) m = fmax3(m, v[i][0], v[i][1]);
  const float nm = -m * LOG2E;
#pragma unroll
  for (int i = 0; i < 10; ++i) {
    v[i] = __builtin_elementwise_fma(v[i], (f32x2){LOG2E, LOG2E}, (f32x2){nm, nm});
    v[i] = (f32x2){__builtin_amdgcn_exp2f(v[i][0]), __builtin_amdgcn_exp2f(v[i][1])};
  }
  const f32x2 s = (((v[0] + v[1]) + (v[2] + v[3])) + ((v[4] + v[5]) + (v[6] + v[7]))) + (v[8] + v[9]);
  const float r = __builtin_amdgcn_rcpf(s[0] + s[1]);
#pragma unroll
  for (int i = 0; i < 10; ++i) { v[i] *= (f32x2){r, r}; ph[i] = pk2(v[i][0], v[i][1]); }
}

constexpr float SC2 = 0.22360679774997896f * 1.4426950408889634f;

DI unsigned o_off(int h, int srow) { return (unsigned)((srow >> 4) * 20480 + (srow & 15) * 16 + 5 * (h >> 3) * 1024 + ((h >> 1) & 3) * 256); }
DI void store_o(rsrc_t ro, unsigned v16, unsigned v8, const float (&o)[12]) {
  st16c(ro, v16, (u32x4){pk2(o[0], o[1]), pk2(o[2], o[3]), pk2(o[4], o[5]), pk2(o[6], o[7])});
  st8c(ro, v8, (u32x2){pk2(o[8], o[9]), pk2(o[10], o[11])});
}
#define PIN(x) asm volatile("" : "+v"(x))
DI void wait_flags(rsrc_t rin, unsigned voff, bool need, u32x2 tag) {
#pragma unroll 1
  for (int spin = 0; spin < (1 << 20); ++spin) {
    u32x2 f;
    asm volatile("buffer_load_dwordx2 %0, %1, %2, 0 offen sc1\n\ts_waitcnt vmcnt(0)" : "=&v"(f) : "v"(voff), "s"(rin) : "memory");
    if (__all(!need || (f[0] == tag[0] && f[1] == tag[1]))) break;
    __builtin_amdgcn_s_sleep(1);
  }
}

constexpr unsigned CODE_BYTES = 24 * 1024;
constexpr int G_STAGE = 16384 + 30720;
constexpr int KP = 1024, KH = 5 * KP, VP = 1056, VH = 5376, SEG_V = 4 * KH, SEG = SEG_V + 4 * VH;
constexpr int L_LOC = 512, L_Q = L_LOC + SEG, L_NEAR = L_Q + 20480, L_FAR = L_NEAR + SEG;
constexpr int A_PHI = L_FAR, A_KV = L_FAR;
constexpr int L_ONE = L_FAR + 8 * 5632 + 160, L_W = L_ONE + 1024 + 96, L_G = L_W + 4096, L_T = L_G + 1024, L_FB = L_T + 256, L_S = L_FB + 16, K_LDS = L_S + 1024;
constexpr int P_W = 512, P_X = L_FAR;
static_assert(3 * G_STAGE + 64 <= L_W && K_LDS <= 160 * 1024 - 2048 && P_W + 20 * 5120 + 64 <= L_FAR && P_W + 12 * 5120 <= L_NEAR && P_W + 13 * 5120 >= L_NEAR, "LDS map");
static_assert(L_LOC % 256 == 0 && SEG % 256 == 0 && SEG_V % 256 == 0 && VH % 256 == 0 && L_NEAR % 256 == 0 && L_ONE % 256 == 160 && L_W % 16 == 0, "banks");

DI f32x16 qk_tile(const LAS char* kh, const LAS char* k4, bf16x8 q0, u32x2 q1, f32x16 c) {
  const u32x2 a = lds8(kh), b = lds8(kh + KP), d = lds8(k4);
  c = MFMA32(frag2(a, b), q0, c);
  return MFMA32K8(d, q1, c);
}
DI f32x16 pv_tile(const LAS char* vp, const f32x16& s, f32x16 o) {
  const bf16x8 v0 = cat8(trread(vp), trread(vp + 64)), v1 = cat8(trread(vp + 128), trread(vp + 192));
  unsigned pw[8];
#pragma unroll
  for (int i = 0; i < 8; ++i) pw[i] = pk2(__builtin_amdgcn_exp2f(s[2 * i]), __builtin_amdgcn_exp2f(s[2 * i + 1]));
  o = MFMA32(v0, asfrag((u32x4){pw[0], pw[1], pw[2], pw[3]}), o);
  return MFMA32(v1, asfrag((u32x4){pw[4], pw[5], pw[6], pw[7]}), o);
}
struct Strm { const LAS char* kh; const LAS char* k4; bf16x8 q0; u32x2 q1; };
struct KFr { u32x2 a, b, d; };
DI KFr kread(const Strm& s, int off) { KFr k; k.a = lds8(s.kh + off); k.b = lds8(s.kh + off + KP); k.d = lds8(s.k4 + off); return k; }
DI void tile_step(f32x16& o, const f32x16& sc, const LAS char* vp, f32x16& sn, const Strm& n, KFr& k, const Strm& ks, int koff) {
  const f32x16 z = {};
  const bf16x8 v0 = cat8(trread(vp), trread(vp + 64)), v1 = cat8(trread(vp + 128), trread(vp + 192));
  float e[16];
#pragma unroll
  for (int i = 0; i < 3; ++i) e[i] = __builtin_amdgcn_exp2f(sc[i]);
  asm volatile("" : "+v"(e[0]), "+v"(e[1]), "+v"(e[2]));
  SB();
  sn = MFMA32(frag2(k.a, k.b), n.q0, z);
#pragma unroll
  for (int i = 3; i < 7; ++i) e[i] = __builtin_amdgcn_exp2f(sc[i]);
  asm volatile("" : "+v"(e[3]), "+v"(e[4]), "+v"(e[5]), "+v"(e[6]));
  SB();
  sn = MFMA32K8(k.d, n.q1, sn);
  k = kread(ks, koff);
#pragma unroll
  for (int i = 7; i < 10; ++i) e[i] = __builtin_amdgcn_exp2f(sc[i]);
  u32x4 p0 = {pk2(e[0], e[1]), pk2(e[2], e[3]), pk2(e[4], e[5]), pk2(e[6], e[7])};
  asm volatile("" : "+v"(e[8]), "+v"(e[9]), "+v"(p0));
  SB();
  o = MFMA32(v0, asfrag(p0), o);
#pragma unroll
  for (int i = 10; i < 16; ++i) e[i] = __builtin_amdgcn_exp2f(sc[i]);
  o = MFMA32(v1, asfrag((u32x4){pk2(e[8], e[9]), pk2(e[10], e[11]), pk2(e[12], e[13]), pk2(e[14], e[15])}), o);
  SB();
}
struct Pipe { f32x16 s; KFr k; };
DI Pipe prime(const Strm& s, const f32x16& c0, int off, int step) {
  Pipe p;
  p.k = kread(s, off + step);
  p.s = qk_tile(s.kh + off, s.k4 + off, s.q0, s.q1, c0);
  return p;
}
DI void run_tiles(f32x16& o, const Strm& s, Pipe& p, const LAS char* vp, int off, int step, int n) {
  f32x16 sb;
#pragma unroll 1
  for (;;) {
    tile_step(o, p.s, vp, sb, s, p.k, s, off + 2 * step);
    if (n == 1) break;
    tile_step(o, sb, vp + step, p.s, s, p.k, s, off + 3 * step);
    if (n == 2) break;
    n -= 2; off += 2 * step; vp += 2 * step;
  }
}

__global__ __launch_bounds__(512, 2) void k1_fused(const __hip_bfloat16* __restrict__ xA, const __hip_bfloat16* __restrict__ wA,
                                                   const __hip_bfloat16* __restrict__ xB, const __hip_bfloat16* __restrict__ wB,
                                                   const __hip_bfloat16* __restrict__ wphi, const __hip_bfloat16* __restrict__ woA,
                                                   const __hip_bfloat16* __restrict__ woB, char* __restrict__ ws,
                                                   __hip_bfloat16* __restrict__ out) {
  __shared__ __attribute__((aligned(16))) char smem[K_LDS];
  const LAS char* sm = (const LAS char*)smem;
  LAS char* smw = (LAS char*)smem;
  const unsigned lds0 = (unsigned)(size_t)(LAS char*)smem;
  const int tid = threadIdx.x, lane = tid & 63;
  const int wid = __builtin_amdgcn_readfirstlane(tid >> 6);
  const int xcd = blockIdx.x & 7, idx = blockIdx.x >> 3;
  const int br = idx & 1, sub = idx >> 1;
  const int mt = (xcd >> 2) * 8 + (sub & 7), nt = (xcd & 3) * 2 + (sub >> 3);
  const int mt3 = xcd * 2 + (sub & 1), nt3 = sub >> 1;
  const int me = blockIdx.x;
  const int g = lane >> 4, fr = lane & 15, q4 = fr >> 2, p4 = lane & 3;
  STAMP_DECL;
  STAMP(0, 0);
  {
    unsigned long long pc;
    asm volatile("s_getpc_b64 %0" : "=s"(pc));
    if (wid == 0) touch(mkrsrc((const void*)pc, CODE_BYTES), lds0 + L_T, lane < 6 ? (unsigned)((lane * 32 + idx) * 128) : OOB, 0);
  }
  const char* wg = (const char*)(br ? wB : wA);
  const rsrc_t rx = mkrsrc((const char*)(br ? xB : xA) + (size_t)mt * 128 * 1280, 128 * 1280);
  const rsrc_t rw = mkrsrc(wg + nt * 160, 640 * 3840 - nt * 160);
  const unsigned vx0 = (unsigned)((2 * (lane >> 3)) * 1280 + (((lane & 7) ^ g) * 16));
  const unsigned vx1 = (unsigned)((16 + 2 * (lane >> 3)) * 1280 + (((lane & 7) ^ (4 + g)) * 16));
  const int wr = lane >= 30 ? 1 : 0, wc = lane - 30 * wr, wsg = (wc >= 10) + (wc >= 20);
  const unsigned vw = (unsigned)(wr * 3840 + wsg * 1280 + (wc - 10 * wsg) * 16);
  const unsigned sx = (unsigned)((32 * (wid >> 1) + (wid & 1)) * 1280);
  const unsigned sw = (unsigned)((16 * (wid >> 1) + 4 * (wid & 1)) * 3840);
  const unsigned lx = lds0 + wid * 2048, lw = lds0 + 16384 + wid * 3840;
#define G_DMA(i, bo, xo, wo) do { \
    if ((i) == 0 || (i) == 3) dma16(rx, lx + (bo) + ((i) ? 1024 : 0), (i) ? vx1 : vx0, (xo)); \
    else { const int n_ = (i) < 3 ? (i) - 1 : (i) - 2; \
      dma16m<0xFFFFFFFFu, 0x0FFFFFFFu>(rw, lw + (bo) + n_ * 960, vw, (wo) + ((n_ >> 1) * 8 + (n_ & 1) * 2) * 3840); } } while (0)
#pragma unroll
  for (int i = 0; i < 6; ++i) G_DMA(i, 0, sx, sw);
  const int bi = mt * 8 + nt;
  const rsrc_t rt = (wid & 4) ? mkrsrc(wg, 640 * 3840) : mkrsrc(br ? xB : xA, SEQ * 1280);
  const unsigned tst = (wid & 4) ? 245760u : 128u;
  unsigned vt = (wid & 4) ? (fr < 15 ? (unsigned)((bi * 15 + fr) * 128 + g * 245760) : OOB) : (unsigned)((bi * 16 + fr) * 1280 + g * 128);
  if ((wid & 3) == 2) vt = g == 0 ? vt + 4 * tst : OOB;
  if ((wid & 3) >= 2) touch(rt, lds0 + L_T, vt, tst);
  vt = (wid & 4) ? (lane < 60 ? (unsigned)(((bi >> 2) * 60 + lane) * 128) : OOB) : (unsigned)(((bi >> 2) * 64 + lane) * 1280);

  const int wm = wid >> 2, wn = wid & 3;
  const LAS char* xa0 = sm + (64 * wm + fr) * 128 + ((g ^ (fr >> 1)) * 16);
  const LAS char* xa1 = sm + (((64 * wm + fr) * 128 + ((g ^ (fr >> 1)) * 16)) ^ 64);
  const LAS char* wa = sm + 16384 + (16 * (g >> 1) + 4 * (g & 1) + q4) * 480 + 8 * p4 + wn * 128;
  f32x4 acc[4][4] = {};
#pragma unroll
  for (int i = 0; i < 4; ++i) asm volatile("" : "+v"(acc[i][0]), "+v"(acc[i][1]), "+v"(acc[i][2]), "+v"(acc[i][3]));
  int eb[4];
#pragma unroll
  for (int j = 0; j < 4; ++j) {
    const int qd = 16 * wn + 4 * j + g, r = qd - 40;
    const int v = L_LOC + SEG_V + r * VP + ((r * 13) >> 6) * 96;
    eb[j] = (qd < 20 ? L_Q + qd * 1024 : qd < 40 ? L_LOC + (qd - 20) * KP : qd < 60 ? v : L_S) + wm * 512 + fr * 16;
    asm volatile("" : "+v"(eb[j]));
  }
  int cons;
  {
    const int cm = br ? mt + 1 + lane : lane;
    const int c1 = (((((nt & 1) * 8 + (cm & 7)) << 1) | br) << 3) | (((cm >> 3) & 1) * 4 + (nt >> 1));
    const int c2 = (((((lane & 7) * 2 + (mt & 1)) << 1) | br) << 3) | (mt >> 1);
    cons = lane < 16 ? ((cm < 16 && (!br || lane < 2)) ? c1 : -1) : (lane < 24 ? c2 : lane == 63 ? me : -1);
    asm volatile("" : "+v"(cons));
  }
  const int hh = lane >> 5, r32 = lane & 31, b16 = (lane >> 4) & 1, vq = 4 * b16 + p4;
  unsigned vflag = (unsigned)(WS_IN + cons * 256 + (lane >= 16 ? 16 + nt : br ? 1 - lane : mt) * 8);
  unsigned vf1 = (lane < 16 && cons >= 0) ? vflag : OOB, vf2 = (lane >= 16 && lane < 24) ? vflag : OOB;
  const int lq = (lane >= 10) + (lane >= 20) + (lane >= 30);
  unsigned vwo = (unsigned)(lq * 1280 + (lane - 10 * lq) * 16);
  PIN(vf1); PIN(vf2); PIN(vwo);
  const int hs = wid >> 2, qi = hs ? 3 - (wid & 3) : (wid & 3);
  const int hl = wid >> 1, rw0 = 64 * (wid & 1);
  int c0, c1, c2, c3, c4, c5, c6;
  unsigned v16, v8;
  if (br) {
    c0 = hs * KH + hh * 2 * KP + r32 * 8;
    c1 = hs * KH + 4 * KP + r32 * 8;
    c2 = (vq < 5 ? SEG_V + hs * VH + vq * VP : L_ONE) + (4 * hh + q4) * 8;
    c3 = vq < 5 ? -1 : 0;
    c4 = L_Q + hs * 5120 + (32 * qi + r32) * 8;
    c5 = L_Q + (2 + hs) * 5120 + (32 * (3 - qi) + r32) * 8;
    c6 = hh * 2048;
    v16 = o_off(4 * nt + hs, mt * 128 + 32 * qi + r32);
  } else {
    c0 = L_W + hl * 800 + (8 * hh + q4) * 40 + (2 * b16 + (p4 >> 1)) * 8;
    c1 = L_W + hl * 800 + (8 * hh + q4) * 40 + 32;
    c2 = hl * 5120 + (rw0 + r32) * 8;
    c3 = A_PHI + wid * 5632 + r32 * 88 + hh * 40;
    c4 = A_PHI + wid * 5632 + (8 * hh + q4) * 88 + (16 * b16 + 4 * p4) * 2;
    c5 = (vq < 5 ? L_LOC + SEG_V + hl * VH + vq * VP : L_ONE) + (rw0 + 8 * hh + q4) * 8;
    c6 = A_PHI + wid * 5632 + min(r32, 21) * 160 + hh * 16;
    v16 = o_off(4 * nt + hl, mt * 128 + rw0 + r32);
  }
  {
    const int odd = br ? hs : (hl & 1);
    v8 = hh ? OOB : (v16 & ~1023u) + 2048 + 512 * odd + ((v16 & 1023u) >> 1);
    v16 += (hh + 3 * odd) * 1024;
  }
  PIN(c0); PIN(c1); PIN(c2); PIN(c3); PIN(c4); PIN(c5); PIN(c6); PIN(v16); PIN(v8);
  f32x16 mle, mgt;
#pragma unroll
  for (int e = 0; e < 16; ++e) {
    const bool le = (e & 3) + 8 * (e >> 2) + 4 * hh <= r32;
    mle[e] = le ? 0.f : -INFINITY;
    mgt[e] = le ? -INFINITY : 0.f;
  }
  PIN(mle); PIN(mgt);
  const rsrc_t rg = mkrsrc(ws + WS_G, WS_KVH);
  const rsrc_t rph = mkrsrc((const char*)wphi + nt * 3200, 3200);
  bf16x8 x0[4], x1[4];
  s16x4 w0[4][2], w1[4][2];
#define G_RD(X, W, xa, kk, r) do { \
    if ((r) < 2) W[0][(r)] = trread(wa + ((kk) * 32 + (r) * 8) * 480); \
    else if ((r) < 6) X[(r) - 2] = *(const LAS bf16x8*)(xa + ((r) - 2) * 2048); \
    else if ((r) < 12) W[((r) - 4) >> 1][(r) & 1] = trread(wa + ((kk) * 32 + ((r) & 1) * 8) * 480 + (((r) - 4) >> 1) * 32); } while (0)
#define G_MM(X, W, m) acc[(m) & 3][(m) >> 2] = MFMA16(cat8(W[(m) >> 2][0], W[(m) >> 2][1]), X[(m) & 3], acc[(m) & 3][(m) >> 2])
  asm volatile("s_barrier" ::: "memory");
#pragma unroll
  for (int i = 0; i < 6; ++i) G_DMA(i, G_STAGE, sx + 128, sw + 245760);
  if ((wid & 3) >= 2) { WAIT_BAR(7); } else { WAIT_BAR(6); }
  STAMP(0, 1);
#pragma unroll
  for (int r = 0; r < 12; ++r) G_RD(x0, w0, xa0, 0, r);
  int bo = 2 * G_STAGE;
  int bd = bo;
  unsigned xo = sx + 256, wo = sw + 2 * 245760;
#pragma unroll
  for (int i = 0; i < 3; ++i) G_DMA(i, bo, xo, wo);
#pragma unroll 1
  for (int t = 0; t < 10; ++t) {
    SB();
#pragma unroll
    for (int m = 0; m < 12; ++m) {
      G_MM(x0, w0, m); G_RD(x1, w1, xa1, 1, m);
      if (m % 3 == 2 && m < 9) G_DMA(3 + m / 3, bd, xo, wo);
      SB();
    }
    if (wn < 3) { G_MM(x0, w0, 12); G_MM(x0, w0, 13); G_MM(x0, w0, 14); G_MM(x0, w0, 15); }
    const int d = bo == G_STAGE ? -2 * G_STAGE : G_STAGE;
    xa0 += d; xa1 += d; wa += d;
    bo = bo == 2 * G_STAGE ? 0 : bo + G_STAGE;
    bd = t < 7 ? bo : 0x30000;
    xo = t < 7 ? xo + 128 : OOB;
    wo += 245760;
    WAIT_BAR(6);
    SB();
#pragma unroll
    for (int m = 0; m < 12; ++m) {
      G_MM(x1, w1, m); G_RD(x0, w0, xa0, 0, m);
      if (m % 3 == 2 && m < 9) G_DMA(m / 3, bd, xo, wo);
      SB();
    }
    if (wn < 3) { G_MM(x1, w1, 12); G_MM(x1, w1, 13); G_MM(x1, w1, 14); G_MM(x1, w1, 15); }
    if (t == (bi & 3) + TB) {
      if ((wid & 3) == 3) touch(rt, lds0 + L_T, vt, (6 - TB + t) * tst);
      if (wid == 1) touch(mkrsrc(br ? woB : woA, 640 * 1280), lds0 + L_T, lane < 50 ? (unsigned)((bi * 50 + lane) * 128) : OOB, 0);
      if (wid == 0) dma16(rg, lds0 + L_G, cons >= 0 ? cons * 16 : OOB, 0);
      if (wid == 5 && !br) {
#pragma unroll
        for (int i = 0; i < 4; ++i) dma16(rph, lds0 + L_W + i * 1024, (unsigned)lane * 16, i * 1024);
      }
    }
  }
  STAMP(0, 2);
  {
    const rsrc_t rh = mkrsrc(ws + WS_KVH + (mt * 8 + nt) * 40960, 40960);
    const int v0 = (16 * wn + g - 20) * 1024 + wm * 512 + fr * 16;
#pragma unroll
    for (int j = 3; j >= 0; --j) {
      if (br && 4 * wn + j < 5) {
#pragma unroll
        for (int i = 0; i < 4; ++i) acc[i][j] *= SC2;
      }
#pragma unroll
      for (int i = 0; i < 2; ++i) {
        const f32x4 a = acc[2 * i][j], b = acc[2 * i + 1][j];
        const u32x4 ev = (u32x4){pk2(a[0], a[1]), pk2(a[2], a[3]), pk2(b[0], b[1]), pk2(b[2], b[3])};
        if (br && 4 * wn + j >= 5) st16c(rh, (unsigned)(v0 + j * 4096 + i * 256), ev);
        *(LAS u32x4*)(smw + eb[j] + i * 256) = ev;
      }
    }
  }
  if (tid < 128) *(LAS u32x2*)(smw + L_ONE + tid * 8) = (u32x2){0x3F80u, 0u};
  if (tid == 511) *(LAS unsigned*)(smw + L_FB) = 0u;
  const u32x2 gc = next_tag(*(const LAS u32x2*)(sm + L_G + lane * 16));
  const u32x2 tag = {(unsigned)__builtin_amdgcn_readlane(gc[0], 63), (unsigned)__builtin_amdgcn_readlane(gc[1], 63)};
  const char* wog = (const char*)(br ? woB : woA);
  const rsrc_t ro = mkrsrc(ws + WS_O + br * (SEQ * DM * 2), SEQ * DM * 2);
  const rsrc_t rwo = mkrsrc(wog + nt3 * 160, 640 * 1280 - nt3 * 160);
  const int sg = 2 * (wid >> 2) + (wid & 1), st = (wid >> 1) & 1;
#define P_DMA2(w5, G) dma16m<0xFFFFFFFFu, 0xFFu>(rwo, lds0 + P_W + (4 * (w5) + (G)) * 5120 + wid * 640, vwo, \
      (unsigned)(160 * (G) + 4 * (w5) + ((w5) >= 2 ? 8 : 0) + 40 * sg + st * ((w5) == 2 ? 20 : 8)) * 1280u)
#define P_DMA(b) P_DMA2((b) >> 2, (b) & 3)
  if (br) {
    WAIT_BAR(0);
    if (wid == 0) st8c(rg, vf1, gc);
    STAMP(0, 3);
    Strm S[2];
#pragma unroll
    for (int r = 0; r < 2; ++r) {
      S[r].kh = sm + c0 + r * 2 * KH;
      S[r].k4 = sm + c1 + r * 2 * KH;
      const LAS char* qp = sm + (r ? c5 : c4);
      S[r].q0 = frag2(lds8(qp + c6), lds8(qp + c6 + 1024));
      const u32x2 t4 = lds8(qp + 4096);
      S[r].q1 = hh ? (u32x2){0u, 0u} : t4;
    }
#define VPTR(r, seg) (sm + c2 + (((seg) + (r) * 2 * VH) & c3))
#define HALO(tile, seg, pg) do { \
      const rsrc_t rn_ = mkrsrc(ws + WS_KVH + (((tile) >= 0 ? (tile) : 0) * 8 + nt) * 40960 + (pg) * 10240, (tile) >= 0 ? 10240 : 0); \
      const unsigned hd_ = lds0 + (seg) + ((pg) >= 2 ? SEG_V + ((pg) & 1) * 2 * VH : ((pg) & 1) * 2 * KH), hp_ = (pg) >= 2 ? VP : KP; \
      _Pragma("unroll") for (int i = 0; i < 10; ++i) dma16c(rn_, hd_ + i * hp_ + (i >= 5 ? ((pg) >> 1) * 96 : 0), lane * 16, i * 1024); } while (0)
    f32x16 o0 = {}, o1 = {};
    Pipe p0 = prime(S[0], mle, L_LOC + qi * 256, -256), p1 = prime(S[1], mle, L_LOC + (3 - qi) * 256, -256);
    run_tiles(o0, S[0], p0, VPTR(0, L_LOC) + qi * 256, L_LOC + qi * 256, -256, qi + 1);
    STAMP(0, 4);
    const int nb = qi < 2 ? 1 : 0, pg = 2 * hs + (qi & 1);
    wait_flags(rg, (unsigned)(WS_IN + me * 256 + nb * 8), mt - 2 + nb >= 0, tag);
    STAMP(0, 5);
    HALO(mt - 2 + nb, nb ? L_NEAR : L_FAR, pg);
    run_tiles(o1, S[1], p1, VPTR(1, L_LOC) + (3 - qi) * 256, L_LOC + (3 - qi) * 256, -256, 4 - qi);
    STAMP(0, 6);
    if (qi < 2) { WAIT_BAR(0); } else { LBAR(); }
    STAMP(0, 7);
    if (mt > 0) {
      const f32x16 z = {};
      KFr k = kread(S[1], L_NEAR);
      f32x16 sa = qk_tile(S[0].kh + L_NEAR, S[0].k4 + L_NEAR, S[0].q0, S[0].q1, z), sb;
      const LAS char* v0 = VPTR(0, L_NEAR);
      const LAS char* v1 = VPTR(1, L_NEAR);
#pragma unroll 1
      for (int off = 0; off < 1024; off += 256) {
        P_DMA2(0, off >> 8); P_DMA2(1, off >> 8);
        tile_step(o0, sa, v0 + off, sb, S[1], k, S[0], L_NEAR + off + 256);
        P_DMA2(2, off >> 8);
        tile_step(o1, sb, v1 + off, sa, S[0], k, S[1], L_NEAR + off + 256);
        if (off == 512) { WAIT_BAR(9); }
      }
    } else {
#pragma unroll
      for (int ks = 0; ks < 12; ++ks) P_DMA(ks);
    }
    p0 = prime(S[0], mgt, L_FAR + qi * 256, 256);
    p1 = prime(S[1], mgt, L_FAR + (3 - qi) * 256, 256);
    STAMP(0, 8);
    LBAR();
    bool redo[2];
#pragma unroll
    for (int r = 0; r < 2; ++r) {
      const int qt = r ? 3 - qi : qi;
      f32x16& o = r ? o1 : o0;
#pragma unroll
      for (int G = 0; G < 4; ++G) P_DMA2(3 + r, G);
      if (mt > 1) run_tiles(o, S[r], r ? p1 : p0, VPTR(r, L_FAR) + qt * 256, L_FAR + qt * 256, 256, 4 - qt);
      const float l = __uint_as_float(__builtin_amdgcn_permlane32_swap(__float_as_uint(o[8]), __float_as_uint(o[8]), false, false)[1]);
      const float rl = __builtin_amdgcn_rcpf(l);
      float of[12];
#pragma unroll
      for (int i = 0; i < 12; ++i) of[i] = o[i] * rl;
      const unsigned dv = r ? (unsigned)((3 - 2 * qi) * 40960 + 256) : 0u;
      store_o(ro, v16 + dv, hh ? OOB : v8 + dv - (r ? 128 : 0), of);
      float am = __builtin_fabsf(o[0]);
#pragma unroll
      for (int i = 1; i < 11; i += 2) am = fmax3(am, __builtin_fabsf(o[i]), __builtin_fabsf(o[i + 1]));
      am = __builtin_elementwise_maximum(am, __builtin_fabsf(o[11]));
      redo[r] = !__all(l >= 0x1p-80f && l <= FB_HI && am <= 0x1p126f);
    }
    if (redo[0] || redo[1]) *(LAS unsigned*)(smw + L_FB) = 1u;
    STAMP(0, 9);
    WAIT_BAR(0);
    STAMP(0, 10);
    if (__builtin_expect(__builtin_amdgcn_readfirstlane(*(const volatile LAS unsigned*)(sm + L_FB)) != 0u, 0)) {
      HALO(mt - nb, nb ? L_NEAR : L_LOC, pg);
      WAIT_BAR(0);
#pragma unroll
      for (int r = 0; r < 2; ++r) {
        if (!redo[r]) continue;
        const int qt = r ? 3 - qi : qi, w0 = mt >= 2 ? qt : mt == 1 ? 4 : 8;
        f32x16 o;
        float m = -INFINITY;
#pragma unroll 1
        for (int pass = 0; pass < 2; ++pass) {
          o = (f32x16){};
#pragma unroll 1
          for (int w = w0; w <= qt + 8; ++w) {
            const int seg = w < 4 ? L_FAR : w < 8 ? L_NEAR : L_LOC, off = (w & 3) * 256;
            f32x16 c;
#pragma unroll
            for (int e = 0; e < 16; ++e) c[e] = w == qt + 8 ? mle[e] : w == qt ? mgt[e] : 0.f;
            f32x16 s = qk_tile(S[r].kh + seg + off, S[r].k4 + seg + off, S[r].q0, S[r].q1, c);
            if (pass == 0) {
#pragma unroll
              for (int e = 0; e < 16; e += 2) m = fmax3(m, s[e], s[e + 1]);
            } else {
#pragma unroll
              for (int e = 0; e < 16; ++e) s[e] -= m;
              o = pv_tile(VPTR(r, seg) + off, s, o);
            }
          }
          auto rr = __builtin_amdgcn_permlane32_swap(__float_as_uint(m), __float_as_uint(m), false, false);
          m = __builtin_elementwise_maximum(__uint_as_float(rr[0]), __uint_as_float(rr[1]));
        }
        const float rl = __builtin_amdgcn_rcpf(__uint_as_float(__builtin_amdgcn_permlane32_swap(__float_as_uint(o[8]), __float_as_uint(o[8]), false, false)[1]));
        float of[12];
#pragma unroll
        for (int i = 0; i < 12; ++i) of[i] = o[i] * rl;
        const unsigned dv = r ? (unsigned)((3 - 2 * qi) * 40960 + 256) : 0u;
        store_o(ro, v16 + dv, hh ? OOB : v8 + dv - (r ? 128 : 0), of);
      }
      LBAR();
#pragma unroll 1
      for (int G = 0; G < 4; ++G) {
#pragma unroll
        for (int u = 0; u < 5; ++u) P_DMA2(u, G);
      }
      WAIT_BAR(0);
    }
  } else {
    WFrag wf = load_wfrag(sm + c0, sm + c1, hh, lane);
    PIN(wf.t0k0); PIN(wf.t1k0); PIN(wf.t0k1); PIN(wf.t1k1);
#pragma unroll
    for (int ks = 13; ks < 20; ++ks) P_DMA(ks);
    LBAR();
    STAMP(0, 3);
    const LAS char* xp = sm + c2 + hh * 2048;
    const LAS char* x4 = sm + c2 + 4096;
    const HRow rk0 = hh_read(xp + L_LOC, x4 + L_LOC), rk1 = hh_read(xp + L_LOC + 256, x4 + L_LOC + 256);
    const HRow rq0 = hh_read(xp + L_Q, x4 + L_Q), rq1 = hh_read(xp + L_Q + 256, x4 + L_Q + 256);
    const HProj hk0 = hh_proj(wf, rk0);
    bf16x8 vf[4];
#pragma unroll
    for (int ks = 0; ks < 4; ++ks) vf[ks] = cat8(trread(sm + c5 + ks * 128), trread(sm + c5 + ks * 128 + 32));
    SB();
    f32x16 d0 = {}, d1 = {};
    HProj hq0, hq1;
    {
      unsigned ph[10];
      HProj hk1 = hh_proj(wf, rk1);
      hh_soft(hk0, ph);
#pragma unroll
      for (int i = 0; i < 5; ++i) *(LAS u32x2*)(smw + c3 + 8 * i) = (u32x2){ph[2 * i], ph[2 * i + 1]};
      PIN(hk1.p0); PIN(hk1.p1);
      mix_mfma<4, 8>();
      SB();
      bf16x8 pf[4];
#pragma unroll
      for (int i = 0; i < 4; ++i) pf[i] = cat8(trread(sm + c4 + (i >> 1) * 16 * 88 + (i & 1) * 64), trread(sm + c4 + ((i >> 1) * 16 + 4) * 88 + (i & 1) * 64));
      SB();
      hq0 = hh_proj(wf, rq0);
      d0 = MFMA32(pf[0], vf[0], d0); d1 = MFMA32(pf[1], vf[0], d1);
      d0 = MFMA32(pf[2], vf[1], d0); d1 = MFMA32(pf[3], vf[1], d1);
      hh_soft(hk1, ph);
#pragma unroll
      for (int i = 0; i < 5; ++i) *(LAS u32x2*)(smw + c3 + 32 * 88 + 8 * i) = (u32x2){ph[2 * i], ph[2 * i + 1]};
      PIN(hq0.p0); PIN(hq0.p1); PIN(d0); PIN(d1);
      mix_mfma<8, 8>();
      SB();
#pragma unroll
      for (int i = 0; i < 4; ++i) pf[i] = cat8(trread(sm + c4 + (32 + (i >> 1) * 16) * 88 + (i & 1) * 64), trread(sm + c4 + (36 + (i >> 1) * 16) * 88 + (i & 1) * 64));
      d0 = MFMA32(pf[0], vf[2], d0); d1 = MFMA32(pf[1], vf[2], d1);
      d0 = MFMA32(pf[2], vf[3], d0); d1 = MFMA32(pf[3], vf[3], d1);
    }
    *(LAS f32x4*)(smw + c6) = (f32x4){d0[0], d0[1], d0[2], d0[3]};
    *(LAS f32x4*)(smw + c6 + 32) = (f32x4){d0[4], d0[5], d0[6], d0[7]};
    *(LAS f32x4*)(smw + c6 + 64) = (f32x4){d0[8], d0[9], d0[10], d0[11]};
    *(LAS f32x4*)(smw + c6 + 96) = (f32x4){d0[12], d0[13], d0[14], d0[15]};
    *(LAS f32x4*)(smw + c6 + 128) = (f32x4){d1[0], d1[1], d1[2], d1[3]};
    SB();
    hq1 = hh_proj(wf, rq1);
    PIN(hq1.p0); PIN(hq1.p1);
    LBAR();
    STAMP(0, 4);
    const rsrc_t rkv = mkrsrc(ws + WS_KVP + nt * 16 * 6784, 16 * 6784);
    {
      const int e = (wid & 1) * 64 + lane;
      const LAS char* o = sm + A_PHI + (wid & ~1) * 5632 + e * 32;
      const f32x4 a = *(const LAS f32x4*)o + *(const LAS f32x4*)(o + 5632), b = *(const LAS f32x4*)(o + 16) + *(const LAS f32x4*)(o + 5648);
      st16c(rkv, e < 105 ? (unsigned)(mt * 6784 + hl * 1680 + e * 16) : OOB, (u32x4){pk2(a[0], a[1]), pk2(a[2], a[3]), pk2(b[0], b[1]), pk2(b[2], b[3])});
    }
#pragma unroll
    for (int ks = 0; ks < 6; ++ks) P_DMA(ks);
    SB();
    unsigned phq[2][10];
    hh_soft(hq0, phq[0]);
#pragma unroll
    for (int i = 0; i < 10; ++i) PIN(phq[0][i]);
    WAIT_BAR(6);
    if (wid == 0) st8c(rg, vf1, gc);
    if (tid >= 256) *(LAS u32x2*)(smw + A_KV + ((tid - 256) >> 1) * 112 + (tid & 1) * 48 + 40) = (u32x2){0u, 0u};
    STAMP(0, 5);
    hh_soft(hq1, phq[1]);
#pragma unroll
    for (int i = 0; i < 10; ++i) PIN(phq[1][i]);
    int kd0, kd1;
    {
      const int row = tid / 5, oc = tid - row * 5, h4 = row / 21, d = row - h4 * 21;
      kd0 = tid < 420 ? A_KV + h4 * 3584 + d * 112 + oc * 16 + (oc > 2 ? 8 : 0) : L_S;
      kd1 = kd0 + (oc == 2 ? 16 : 8);
      PIN(kd0); PIN(kd1);
    }
    wait_flags(rg, (unsigned)(WS_IN + me * 256 + fr * 8), true, tag);
    STAMP(0, 6);
    if (wid < 7) {
      u32x4 p[16];
#pragma unroll
      for (int i = 0; i < 16; ++i) p[i] = __builtin_amdgcn_raw_buffer_load_b128(rkv, tid * 16, i * 6784, 16);
      f32x2 lo[2] = {}, hi[2] = {};
#pragma unroll
      for (int i = 0; i < 16; ++i)
#pragma unroll
        for (int u = 0; u < 2; ++u) {
          lo[u] += (f32x2){__uint_as_float(p[i][2 * u] << 16), __uint_as_float(p[i][2 * u + 1] << 16)};
          hi[u] += (f32x2){__uint_as_float(p[i][2 * u] & 0xFFFF0000u), __uint_as_float(p[i][2 * u + 1] & 0xFFFF0000u)};
        }
      *(LAS u32x2*)(smw + kd0) = (u32x2){pk2(lo[0][0], hi[0][0]), pk2(lo[0][1], hi[0][1])};
      *(LAS u32x2*)(smw + kd1) = (u32x2){pk2(lo[1][0], hi[1][0]), pk2(lo[1][1], hi[1][1])};
    }
#pragma unroll
    for (int ks = 6; ks < 13; ++ks) P_DMA(ks);
    LBAR();
    STAMP(0, 7);
    const LAS char* kp = sm + A_KV + hl * 3584 + (r32 == 24 ? 20 : r32) * 112 + hh * 48;
    const bf16x8 f0 = *(const LAS bf16x8*)(kp), f1 = *(const LAS bf16x8*)(kp + 16), f2 = *(const LAS bf16x8*)(kp + 32);
    f32x16 n[2];
#pragma unroll
    for (int t = 0; t < 2; ++t) n[t] = MFMA32(f0, asfrag((u32x4){phq[t][0], phq[t][1], phq[t][2], phq[t][3]}), (f32x16){});
#pragma unroll
    for (int t = 0; t < 2; ++t) n[t] = MFMA32(f1, asfrag((u32x4){phq[t][4], phq[t][5], phq[t][6], phq[t][7]}), n[t]);
#pragma unroll
    for (int t = 0; t < 2; ++t) n[t] = MFMA32(f2, asfrag((u32x4){phq[t][8], phq[t][9], 0u, 0u}), n[t]);
#pragma unroll
    for (int t = 0; t < 2; ++t) {
      const float den = rbf(rbf(hh ? n[t][8] : n[t][12]) + 1e-6f);
      const float r = __builtin_amdgcn_rcpf(den);
      float o[12];
#pragma unroll
      for (int i = 0; i < 12; ++i) o[i] = rbf(n[t][i]) * r;
      store_o(ro, v16 + t * 40960, hh ? OOB : v8 + t * 40960, o);
    }
    STAMP(0, 9);
    WAIT_BAR(0);
    STAMP(0, 10);
  }
  {
    if (wid == 0) st8c(rg, vf2, gc);
    STAMP(0, 11);
    int oa[3];
    unsigned og[3];
#pragma unroll
    for (int i = 0; i < 3; ++i) {
      const int c = min(lane + 64 * i, 160), row = c / 10, ch = c - row * 10;
      oa[i] = wid * 2816 + row * 176 + ch * 16;
      og[i] = (unsigned)(row * 2560 + ch * 16);
      PIN(oa[i]); PIN(og[i]);
    }
    const int pm = wid & 3, kh = wid >> 2;
    wait_flags(rg, (unsigned)(WS_IN + me * 256 + 128 + (4 * kh + (lane & 3)) * 8), true, tag);
    STAMP(0, 12);
    const rsrc_t rf = mkrsrc(ws + WS_O + (unsigned)((br * 128 + mt3 * 8 + 2 * pm) * 20 + 10 * kh) * 1024u, 30 * 1024);
    const unsigned vk = lane * 16 + kh * 20480, vh = lane * 16 + (1 - kh) * 20480;
    u32x4 xf[10][2];
    const unsigned vk8 = lane * 8 + kh * 20480, vh8 = lane * 8 + (1 - kh) * 20480;
#define P_LD1(v, v8_, tau) ((tau) % 5 == 2 ? __builtin_bit_cast(u32x4, frag2(__builtin_amdgcn_raw_buffer_load_b64(rf, v8_, (tau) * 1024, 16), \
                                                                             __builtin_amdgcn_raw_buffer_load_b64(rf, v8_, (tau) * 1024 + 512, 16))) \
                                           : __builtin_amdgcn_raw_buffer_load_b128(rf, v, (tau) * 1024, 16))
#define P_LD(tau) do { xf[tau][0] = P_LD1(vk, vk8, tau); xf[tau][1] = P_LD1(vh, vh8, tau); } while (0)
    P_LD(0); P_LD(1); P_LD(2);
    const LAS char* wb = sm + P_W + kh * 10240 + (16 * (g >> 1) + 4 * (g & 1) + q4) * 160 + 8 * p4;
    const LAS char* wb2 = wb + 12 * 5120;
    f32x4 pc[2][5] = {};
    bf16x8 wf[2][5];
#pragma unroll
    for (int j = 0; j < 5; ++j) wf[0][j] = cat8(trread(wb + j * 32), trread(wb + 8 * 160 + j * 32));
#pragma unroll
    for (int tau = 0; tau < 10; ++tau) {
      SB();
      if (tau < 7) P_LD(tau + 3);
#pragma unroll
      for (int j = 0; j < 5; ++j) {
        if (tau < 9) {
          const int blk = ((tau + 1) % 5) * 4 + (tau + 1) / 5;
          const LAS char* wp = (blk >= 12 ? wb2 : wb) + (blk >= 12 ? blk - 12 : blk) * 5120 + j * 32;
          wf[(tau + 1) & 1][j] = cat8(trread(wp), trread(wp + 8 * 160));
        }
        pc[1][j] = MFMA16(wf[tau & 1][j], asfrag(xf[tau][1]), pc[1][j]);
        if (tau < 9) pc[0][j] = MFMA16(wf[tau & 1][j], asfrag(xf[tau][0]), pc[0][j]);
        SB();
      }
    }
#pragma unroll
    for (int j = 0; j < 5; ++j) {
      pc[0][j] = MFMA16(wf[1][j], asfrag(xf[9][0]), pc[0][j]);
      *(LAS f32x4*)(smw + P_X + (wid * 5 + j) * 1024 + lane * 16) = pc[1][j];
    }
    STAMP(0, 13);
    LBAR();
    if (tid == 0) st8c(rg, me * 16, tag);
    LAS char* ow = smw + wid * 2816 + fr * 176 + g * 8;
#pragma unroll
    for (int j = 0; j < 5; ++j) {
      const f32x4 a = pc[0][j] + *(const LAS f32x4*)(sm + P_X + ((wid ^ 4) * 5 + j) * 1024 + lane * 16);
      *(LAS u32x2*)(ow + j * 32) = (u32x2){pk2(a[0], a[1]), pk2(a[2], a[3])};
    }
    const rsrc_t rout = mkrsrc((char*)out + (size_t)(mt3 * 128 + 32 * pm + 16 * kh) * 2560 + br * 1280 + nt3 * 160, 15 * 2560 + 160);
#pragma unroll
    for (int i = 0; i < 3; ++i) __builtin_amdgcn_raw_buffer_store_b128(*(const LAS u32x4*)(sm + oa[i]), rout, og[i], 0, OUT_AUX);
  }
  STAMP(0, 15);
  STAMP_FLUSH(0);
}

extern "C" void kernel_launch(void* const* d_in, const int* in_sizes, int n_in, void* d_out, int out_size, void* d_ws,
                              size_t ws_size, hipStream_t stream) {
  k1_fused<<<256, 512, 0, stream>>>((const __hip_bfloat16*)d_in[0], (const __hip_bfloat16*)d_in[1], (const __hip_bfloat16*)d_in[4],
                                    (const __hip_bfloat16*)d_in[5], (const __hip_bfloat16*)d_in[2], (const __hip_bfloat16*)d_in[3],
                                    (const __hip_bfloat16*)d_in[6], (char*)d_ws, (__hip_bfloat16*)d_out);
}
```

```cpp
#include <hip/hip_runtime.h>
#include <hip/hip_bf16.h>

typedef short bf16x8 __attribute__((ext_vector_type(8)));
typedef short s16x4 __attribute__((ext_vector_type(4)));
typedef float f32x2 __attribute__((ext_vector_type(2)));
typedef float f32x4 __attribute__((ext_vector_type(4)));
typedef float f32x16 __attribute__((ext_vector_type(16)));
typedef unsigned u32x2 __attribute__((ext_vector_type(2)));
typedef unsigned u32x4 __attribute__((ext_vector_type(4)));
typedef __bf16 bf16x2_t __attribute__((ext_vector_type(2)));
typedef __amdgpu_buffer_rsrc_t rsrc_t;
#define LAS __attribute__((address_space(3)))
#define GAS __attribute__((address_space(1)))
#define DI __device__ __forceinline__

#define PFV 6
#define OUT_AUX 16
#define TB 1
#define FB_HI 0x1p100f
#define CPF 1
constexpr int SEQ = 2048, DM = 640, NH = 32;
constexpr unsigned WS_G = 0;
constexpr unsigned WS_IN = 4096;
constexpr unsigned WS_KVH = WS_IN + 256 * 256;
constexpr unsigned WS_KVP = WS_KVH + 128 * 40960;
constexpr unsigned WS_O = WS_KVP + 128 * 6784;
constexpr unsigned WS_END = WS_O + 2 * SEQ * DM * 2;
constexpr unsigned OOB = 0x7FFF0000u;

DI unsigned pk2(float lo, float hi) {
  bf16x2_t r = __builtin_convertvector((f32x2){lo, hi}, bf16x2_t);
  return __builtin_bit_cast(unsigned, r);
}
DI float rbf(float x) { return __builtin_bit_cast(float, pk2(0.f, x)); }
DI float fmax3(float a, float b, float c) { return __builtin_elementwise_maximum(__builtin_elementwise_maximum(a, b), c); }
DI s16x4 trread(const LAS char* p) { return __builtin_amdgcn_ds_read_tr16_b64_v4i16((LAS s16x4*)p); }
DI bf16x8 cat8(s16x4 a, s16x4 b) { return __builtin_shufflevector(a, b, 0, 1, 2, 3, 4, 5, 6, 7); }
DI bf16x8 asfrag(u32x4 v) { return __builtin_bit_cast(bf16x8, v); }
DI bf16x8 frag2(u32x2 a, u32x2 b) { return asfrag((u32x4){a[0], a[1], b[0], b[1]}); }
DI rsrc_t mkrsrc(const void* p, unsigned bytes) { return __builtin_amdgcn_make_buffer_rsrc((void*)p, (short)0, (int)bytes, 0x00020000); }
DI void dma16(rsrc_t rs, unsigned lds, unsigned voff, unsigned soff) {
  asm volatile("s_mov_b32 m0, %0\n\ts_nop 0\n\tbuffer_load_dwordx4 %1, %2, %3 offen lds"
               :: "s"(lds), "v"(voff), "s"(rs), "s"(soff) : "memory");
}
DI void dma16c(rsrc_t rs, unsigned lds, unsigned voff, unsigned soff) {
  asm volatile("s_mov_b32 m0, %0\n\ts_nop 0\n\tbuffer_load_dwordx4 %1, %2, %3 offen sc1 lds"
               :: "s"(lds), "v"(voff), "s"(rs), "s"(soff) : "memory");
}
DI void touch(rsrc_t rs, unsigned lds, unsigned voff, unsigned soff) {
  asm volatile("s_mov_b32 m0, %0\n\ts_nop 0\n\tbuffer_load_dword %1, %2, %3 offen lds"
               :: "s"(lds), "v"(voff), "s"(rs), "s"(soff) : "memory");
}
template <unsigned LO, unsigned HI>
DI void dma16m(rsrc_t rs, unsigned lds, unsigned voff, unsigned soff) {
  asm volatile("s_mov_b32 m0, %0\n\ts_mov_b32 exec_lo, %4\n\ts_mov_b32 exec_hi, %5\n\tbuffer_load_dwordx4 %1, %2, %3 offen lds\n\ts_mov_b64 exec, -1"
               :: "s"(lds), "v"(voff), "s"(rs), "s"(soff), "i"(LO), "i"(HI) : "memory");
}
DI void ld8(u32x2& d, rsrc_t rs, unsigned voff, unsigned soff) {
  asm volatile("buffer_load_dwordx2 %0, %1, %2, %3 offen" : "=v"(d) : "v"(voff), "s"(rs), "s"(soff) : "memory");
}
DI void ld16(u32x4& d, rsrc_t rs, unsigned voff, unsigned soff) {
  asm volatile("buffer_load_dwordx4 %0, %1, %2, %3 offen" : "=v"(d) : "v"(voff), "s"(rs), "s"(soff) : "memory");
}
DI void ld16c(u32x4& d, rsrc_t rs, unsigned voff, unsigned soff) {
  asm volatile("buffer_load_dwordx4 %0, %1, %2, %3 offen sc1" : "=v"(d) : "v"(voff), "s"(rs), "s"(soff) : "memory");
}
DI void st16c(rsrc_t rs, unsigned voff, u32x4 v) { __builtin_amdgcn_raw_buffer_store_b128(v, rs, voff, 0, 16); }
DI void st8c(rsrc_t rs, unsigned voff, u32x2 v) { __builtin_amdgcn_raw_buffer_store_b64(v, rs, voff, 0, 16); }
DI u32x2 lds8(const LAS char* p) { return *(const volatile LAS u32x2*)p; }
DI u32x2 next_tag(u32x2 g) { return (u32x2){g[0] * 1664525u + 1013904223u, g[1] * 22695477u + 2654435761u}; }
#define MFMA16(a, b, c) __builtin_amdgcn_mfma_f32_16x16x32_bf16((a), (b), (c), 0, 0, 0)
#define MFMA32(a, b, c) __builtin_amdgcn_mfma_f32_32x32x16_bf16((a), (b), (c), 0, 0, 0)
#define MFMA32K8(a, b, c) __builtin_amdgcn_mfma_f32_32x32x8bf16_1k(__builtin_bit_cast(s16x4, a), __builtin_bit_cast(s16x4, b), (c), 0, 0, 0)
#define WAIT_BAR(N) asm volatile("s_waitcnt vmcnt(" #N ") lgkmcnt(0)\n\ts_barrier" ::: "memory")
#define LBAR() asm volatile("s_waitcnt lgkmcnt(0)\n\ts_barrier" ::: "memory")
#define SB() __builtin_amdgcn_sched_barrier(0)
#define PIN8(a) asm volatile("" : "+v"(a[0]), "+v"(a[1]), "+v"(a[2]), "+v"(a[3]), "+v"(a[4]), "+v"(a[5]), "+v"(a[6]), "+v"(a[7]))

#define STAMP_DECL
#define STAMP(kid, i)
#define STAMP_FLUSH(kid)

constexpr float LOG2E = 1.4426950408889634f;

struct WFrag { bf16x8 t0k0, t1k0; u32x2 t0k1, t1k1; };
DI WFrag load_wfrag(const LAS char* a0  , const LAS char* a1  , int hh, int lane) {
  u32x2 r0 = __builtin_bit_cast(u32x2, trread(a0)), r1 = __builtin_bit_cast(u32x2, trread(a0 + 160));
  u32x2 r2 = __builtin_bit_cast(u32x2, trread(a1)), r3 = __builtin_bit_cast(u32x2, trread(a1 + 160));
  u32x2 r4 = __builtin_bit_cast(u32x2, trread(a0 + 640)), r5 = __builtin_bit_cast(u32x2, trread(a1 + 640));
  const unsigned sg = (lane & 4) ? 0x80008000u : 0u;
  const unsigned km = hh ? 0u : 0xFFFFFFFFu;
  WFrag w;
  w.t0k0 = asfrag((u32x4){r0[0] ^ sg, r0[1] ^ sg, r1[0] ^ sg, r1[1] ^ sg});
  w.t1k0 = asfrag((u32x4){r2[0] ^ sg, r2[1] ^ sg, r3[0] ^ sg, r3[1] ^ sg});
  w.t0k1 = (u32x2){(r4[0] ^ sg) & km, (r4[1] ^ sg) & km};
  w.t1k1 = (u32x2){(r5[0] ^ sg) & km, (r5[1] ^ sg) & km};
  return w;
}
struct HProj { f32x16 p0, p1; };
struct HRow { bf16x8 x0; u32x2 x1; };
DI HRow hh_read(const LAS char* p, const LAS char* p4) { HRow r; r.x0 = frag2(lds8(p), lds8(p + 1024)); r.x1 = lds8(p4); return r; }
DI HProj hh_proj(const WFrag& w, const HRow& x) {
  const f32x16 z = {};
  HProj h;
  h.p0 = MFMA32(w.t0k0, x.x0, z);
  h.p1 = MFMA32(w.t1k0, x.x0, z);
  h.p0 = MFMA32K8(w.t0k1, x.x1, h.p0);
  h.p1 = MFMA32K8(w.t1k1, x.x1, h.p1);
  return h;
}
template <int N, int NV> DI void mix_mfma() {
#pragma unroll
  for (int i = 0; i < N; ++i) { __builtin_amdgcn_sched_group_barrier(0x8, 1, 0); __builtin_amdgcn_sched_group_barrier(0x402, NV, 0); }
}
DI void hh_soft(const HProj& h, unsigned (&ph)[10]) {
  f32x2 v[10];
#pragma unroll
  for (int i = 0; i < 8; ++i) v[i] = (f32x2){rbf(h.p0[2 * i]), rbf(h.p0[2 * i + 1])};
#pragma unroll
  for (int i = 0; i < 2; ++i) v[8 + i] = (f32x2){rbf(h.p1[2 * i]), rbf(h.p1[2 * i + 1])};
  float m = __builtin_elementwise_maximum(v[0][0], v[0][1]);
#pragma unroll
  for (int i = 1; i < 10; ++i) m = fmax3(m, v[i][0], v[i][1]);
  const float nm = -m * LOG2E;
#pragma unroll
  for (int i = 0; i < 10; ++i) {
    v[i] = __builtin_elementwise_fma(v[i], (f32x2){LOG2E, LOG2E}, (f32x2){nm, nm});
    v[i] = (f32x2){__builtin_amdgcn_exp2f(v[i][0]), __builtin_amdgcn_exp2f(v[i][1])};
  }
  const f32x2 s = (((v[0] + v[1]) + (v[2] + v[3])) + ((v[4] + v[5]) + (v[6] + v[7]))) + (v[8] + v[9]);
  const float r = __builtin_amdgcn_rcpf(s[0] + s[1]);
#pragma unroll
  for (int i = 0; i < 10; ++i) { v[i] *= (f32x2){r, r}; ph[i] = pk2(v[i][0], v[i][1]); }
}

constexpr float SC2 = 0.22360679774997896f * 1.4426950408889634f;

DI unsigned o_off(int h, int srow) { return (unsigned)((srow >> 4) * 20480 + (srow & 15) * 16 + 5 * (h >> 3) * 1024 + ((h >> 1) & 3) * 256); }
DI void store_o(rsrc_t ro, unsigned v16, unsigned v8, const float (&o)[12]) {
  st16c(ro, v16, (u32x4){pk2(o[0], o[1]), pk2(o[2], o[3]), pk2(o[4], o[5]), pk2(o[6], o[7])});
  st8c(ro, v8, (u32x2){pk2(o[8], o[9]), pk2(o[10], o[11])});
}
#define PIN(x) asm volatile("" : "+v"(x))
DI void wait_flags(rsrc_t rin, unsigned voff, bool need, u32x2 tag) {
#pragma unroll 1
  for (int spin = 0; spin < (1 << 20); ++spin) {
    u32x2 f;
    asm volatile("buffer_load_dwordx2 %0, %1, %2, 0 offen sc1\n\ts_waitcnt vmcnt(0)" : "=&v"(f) : "v"(voff), "s"(rin) : "memory");
    if (__all(!need || (f[0] == tag[0] && f[1] == tag[1]))) break;
    __builtin_amdgcn_s_sleep(1);
  }
}

constexpr unsigned CODE_BYTES = 24 * 1024;
constexpr int G_STAGE = 16384 + 30720;
constexpr int KP = 1024, KH = 5 * KP, VP = 1056, VH = 5376, SEG_V = 4 * KH, SEG = SEG_V + 4 * VH;
constexpr int L_LOC = 512, L_Q = L_LOC + SEG, L_NEAR = L_Q + 20480, L_FAR = L_NEAR + SEG;
constexpr int A_PHI = L_FAR, A_KV = L_FAR;
constexpr int L_ONE = L_FAR + 8 * 5632 + 160, L_W = L_ONE + 1024 + 96, L_G = L_W + 4096, L_T = L_G + 1024, L_FB = L_T + 256, L_S = L_FB + 16, K_LDS = L_S + 1024;
constexpr int P_W = 512, P_X = L_FAR;
static_assert(3 * G_STAGE + 64 <= L_W && K_LDS <= 160 * 1024 - 2048 && P_W + 20 * 5120 + 64 <= L_FAR && P_W + 12 * 5120 <= L_NEAR && P_W + 13 * 5120 >= L_NEAR, "LDS map");
static_assert(L_LOC % 256 == 0 && SEG % 256 == 0 && SEG_V % 256 == 0 && VH % 256 == 0 && L_NEAR % 256 == 0 && L_ONE % 256 == 160 && L_W % 16 == 0, "banks");

DI f32x16 qk_tile(const LAS char* kh, const LAS char* k4, bf16x8 q0, u32x2 q1, f32x16 c) {
  const u32x2 a = lds8(kh), b = lds8(kh + KP), d = lds8(k4);
  c = MFMA32(frag2(a, b), q0, c);
  return MFMA32K8(d, q1, c);
}
DI f32x16 pv_tile(const LAS char* vp, const f32x16& s, f32x16 o) {
  const bf16x8 v0 = cat8(trread(vp), trread(vp + 64)), v1 = cat8(trread(vp + 128), trread(vp + 192));
  unsigned pw[8];
#pragma unroll
  for (int i = 0; i < 8; ++i) pw[i] = pk2(__builtin_amdgcn_exp2f(s[2 * i]), __builtin_amdgcn_exp2f(s[2 * i + 1]));
  o = MFMA32(v0, asfrag((u32x4){pw[0], pw[1], pw[2], pw[3]}), o);
  return MFMA32(v1, asfrag((u32x4){pw[4], pw[5], pw[6], pw[7]}), o);
}
struct Strm { const LAS char* kh; const LAS char* k4; bf16x8 q0; u32x2 q1; };
struct KFr { u32x2 a, b, d; };
DI KFr kread(const Strm& s, int off) { KFr k; k.a = lds8(s.kh + off); k.b = lds8(s.kh + off + KP); k.d = lds8(s.k4 + off); return k; }
DI void tile_step(f32x16& o, const f32x16& sc, const LAS char* vp, f32x16& sn, const Strm& n, KFr& k, const Strm& ks, int koff) {
  const f32x16 z = {};
  const bf16x8 v0 = cat8(trread(vp), trread(vp + 64)), v1 = cat8(trread(vp + 128), trread(vp + 192));
  float e[16];
#pragma unroll
  for (int i = 0; i < 3; ++i) e[i] = __builtin_amdgcn_exp2f(sc[i]);
  asm volatile("" : "+v"(e[0]), "+v"(e[1]), "+v"(e[2]));
  SB();
  sn = MFMA32(frag2(k.a, k.b), n.q0, z);
#pragma unroll
  for (int i = 3; i < 7; ++i) e[i] = __builtin_amdgcn_exp2f(sc[i]);
  asm volatile("" : "+v"(e[3]), "+v"(e[4]), "+v"(e[5]), "+v"(e[6]));
  SB();
  sn = MFMA32K8(k.d, n.q1, sn);
  k = kread(ks, koff);
#pragma unroll
  for (int i = 7; i < 10; ++i) e[i] = __builtin_amdgcn_exp2f(sc[i]);
  u32x4 p0 = {pk2(e[0], e[1]), pk2(e[2], e[3]), pk2(e[4], e[5]), pk2(e[6], e[7])};
  asm volatile("" : "+v"(e[8]), "+v"(e[9]), "+v"(p0));
  SB();
  o = MFMA32(v0, asfrag(p0), o);
#pragma unroll
  for (int i = 10; i < 16; ++i) e[i] = __builtin_amdgcn_exp2f(sc[i]);
  o = MFMA32(v1, asfrag((u32x4){pk2(e[8], e[9]), pk2(e[10], e[11]), pk2(e[12], e[13]), pk2(e[14], e[15])}), o);
  SB();
}
struct Pipe { f32x16 s; KFr k; };
DI Pipe prime(const Strm& s, const f32x16& c0, int off, int step) {
  Pipe p;
  p.k = kread(s, off + step);
  p.s = qk_tile(s.kh + off, s.k4 + off, s.q0, s.q1, c0);
  return p;
}
DI void run_tiles(f32x16& o, const Strm& s, Pipe& p, const LAS char* vp, int off, int step, int n) {
  f32x16 sb;
#pragma unroll 1
  for (;;) {
    tile_step(o, p.s, vp, sb, s, p.k, s, off + 2 * step);
    if (n == 1) break;
    tile_step(o, sb, vp + step, p.s, s, p.k, s, off + 3 * step);
    if (n == 2) break;
    n -= 2; off += 2 * step; vp += 2 * step;
  }
}

__global__ __launch_bounds__(512, 2) void k1_fused(const __hip_bfloat16* __restrict__ xA, const __hip_bfloat16* __restrict__ wA,
                                                   const __hip_bfloat16* __restrict__ xB, const __hip_bfloat16* __restrict__ wB,
                                                   const __hip_bfloat16* __restrict__ wphi, const __hip_bfloat16* __restrict__ woA,
                                                   const __hip_bfloat16* __restrict__ woB, char* __restrict__ ws,
                                                   __hip_bfloat16* __restrict__ out) {
  __shared__ __attribute__((aligned(16))) char smem[K_LDS];
  const LAS char* sm = (const LAS char*)smem;
  LAS char* smw = (LAS char*)smem;
  const unsigned lds0 = (unsigned)(size_t)(LAS char*)smem;
  const int tid = threadIdx.x, lane = tid & 63;
  const int wid = __builtin_amdgcn_readfirstlane(tid >> 6);
  const int xcd = blockIdx.x & 7, idx = blockIdx.x >> 3;
  const int br = idx & 1, sub = idx >> 1;
  const int mt = (xcd >> 2) * 8 + (sub & 7), nt = (xcd & 3) * 2 + (sub >> 3);
  const int mt3 = xcd * 2 + (sub & 1), nt3 = sub >> 1;
  const int me = blockIdx.x;
  const int g = lane >> 4, fr = lane & 15, q4 = fr >> 2, p4 = lane & 3;
  STAMP_DECL;
  STAMP(0, 0);
  {
    unsigned long long pc;
    asm volatile("s_getpc_b64 %0" : "=s"(pc));
    if (wid == 0) touch(mkrsrc((const void*)pc, CODE_BYTES), lds0 + L_T, lane < 6 ? (unsigned)((lane * 32 + idx) * 128) : OOB, 0);
  }
  const char* wg = (const char*)(br ? wB : wA);
  const rsrc_t rx = mkrsrc((const char*)(br ? xB : xA) + (size_t)mt * 128 * 1280, 128 * 1280);
  const rsrc_t rw = mkrsrc(wg + nt * 160, 640 * 3840 - nt * 160);
  const unsigned vx0 = (unsigned)((2 * (lane >> 3)) * 1280 + (((lane & 7) ^ g) * 16));
  const unsigned vx1 = (unsigned)((16 + 2 * (lane >> 3)) * 1280 + (((lane & 7) ^ (4 + g)) * 16));
  const int wr = lane >= 30 ? 1 : 0, wc = lane - 30 * wr, wsg = (wc >= 10) + (wc >= 20);
  const unsigned vw = (unsigned)(wr * 3840 + wsg * 1280 + (wc - 10 * wsg) * 16);
  const unsigned sx = (unsigned)((32 * (wid >> 1) + (wid & 1)) * 1280);
  const unsigned sw = (unsigned)((16 * (wid >> 1) + 4 * (wid & 1)) * 3840);
  const unsigned lx = lds0 + wid * 2048, lw = lds0 + 16384 + wid * 3840;
#define G_DMA(i, bo, xo, wo) do { \
    if ((i) == 0 || (i) == 3) dma16(rx, lx + (bo) + ((i) ? 1024 : 0), (i) ? vx1 : vx0, (xo)); \
    else { const int n_ = (i) < 3 ? (i) - 1 : (i) - 2; \
      dma16m<0xFFFFFFFFu, 0x0FFFFFFFu>(rw, lw + (bo) + n_ * 960, vw, (wo) + ((n_ >> 1) * 8 + (n_ & 1) * 2) * 3840); } } while (0)
#pragma unroll
  for (int i = 0; i < 6; ++i) G_DMA(i, 0, sx, sw);
  const int bi = mt * 8 + nt;
  const rsrc_t rt = (wid & 4) ? mkrsrc(wg, 640 * 3840) : mkrsrc(br ? xB : xA, SEQ * 1280);
  const unsigned tst = (wid & 4) ? 245760u : 128u;
  unsigned vt = (wid & 4) ? (fr < 15 ? (unsigned)((bi * 15 + fr) * 128 + g * 245760) : OOB) : (unsigned)((bi * 16 + fr) * 1280 + g * 128);
  if ((wid & 3) == 2) vt = g == 0 ? vt + 4 * tst : OOB;
  if ((wid & 3) >= 2) touch(rt, lds0 + L_T, vt, tst);
  vt = (wid & 4) ? (lane < 60 ? (unsigned)(((bi >> 2) * 60 + lane) * 128) : OOB) : (unsigned)(((bi >> 2) * 64 + lane) * 1280);

  const int wm = wid >> 2, wn = wid & 3;
  const LAS char* xa0 = sm + (64 * wm + fr) * 128 + ((g ^ (fr >> 1)) * 16);
  const LAS char* xa1 = sm + (((64 * wm + fr) * 128 + ((g ^ (fr >> 1)) * 16)) ^ 64);
  const LAS char* wa = sm + 16384 + (16 * (g >> 1) + 4 * (g & 1) + q4) * 480 + 8 * p4 + wn * 128;
  f32x4 acc[4][4] = {};
#pragma unroll
  for (int i = 0; i < 4; ++i) asm volatile("" : "+v"(acc[i][0]), "+v"(acc[i][1]), "+v"(acc[i][2]), "+v"(acc[i][3]));
  int eb[4];
#pragma unroll
  for (int j = 0; j < 4; ++j) {
    const int qd = 16 * wn + 4 * j + g, r = qd - 40;
    const int v = L_LOC + SEG_V + r * VP + ((r * 13) >> 6) * 96;
    eb[j] = (qd < 20 ? L_Q + qd * 1024 : qd < 40 ? L_LOC + (qd - 20) * KP : qd < 60 ? v : L_S) + wm * 512 + fr * 16;
    asm volatile("" : "+v"(eb[j]));
  }
  int cons;
  {
    const int cm = br ? mt + 1 + lane : lane;
    const int c1 = (((((nt & 1) * 8 + (cm & 7)) << 1) | br) << 3) | (((cm >> 3) & 1) * 4 + (nt >> 1));
    const int c2 = (((((lane & 7) * 2 + (mt & 1)) << 1) | br) << 3) | (mt >> 1);
    cons = lane < 16 ? ((cm < 16 && (!br || lane < 2)) ? c1 : -1) : (lane < 24 ? c2 : lane == 63 ? me : -1);
    asm volatile("" : "+v"(cons));
  }
  const int hh = lane >> 5, r32 = lane & 31, b16 = (lane >> 4) & 1, vq = 4 * b16 + p4;
  unsigned vflag = (unsigned)(WS_IN + cons * 256 + (lane >= 16 ? 16 + nt : br ? 1 - lane : mt) * 8);
  unsigned vf1 = (lane < 16 && cons >= 0) ? vflag : OOB, vf2 = (lane >= 16 && lane < 24) ? vflag : OOB;
  const int lq = (lane >= 10) + (lane >= 20) + (lane >= 30);
  unsigned vwo = (unsigned)(lq * 1280 + (lane - 10 * lq) * 16);
  PIN(vf1); PIN(vf2); PIN(vwo);
  const int hs = wid >> 2, qi = hs ? 3 - (wid & 3) : (wid & 3);
  const int hl = wid >> 1, rw0 = 64 * (wid & 1);
  int c0, c1, c2, c3, c4, c5, c6;
  unsigned v16, v8;
  if (br) {
    c0 = hs * KH + hh * 2 * KP + r32 * 8;
    c1 = hs * KH + 4 * KP + r32 * 8;
    c2 = (vq < 5 ? SEG_V + hs * VH + vq * VP : L_ONE) + (4 * hh + q4) * 8;
    c3 = vq < 5 ? -1 : 0;
    c4 = L_Q + hs * 5120 + (32 * qi + r32) * 8;
    c5 = L_Q + (2 + hs) * 5120 + (32 * (3 - qi) + r32) * 8;
    c6 = hh * 2048;
    v16 = o_off(4 * nt + hs, mt * 128 + 32 * qi + r32);
  } else {
    c0 = L_W + hl * 800 + (8 * hh + q4) * 40 + (2 * b16 + (p4 >> 1)) * 8;
    c1 = L_W + hl * 800 + (8 * hh + q4) * 40 + 32;
    c2 = hl * 5120 + (rw0 + r32) * 8;
    c3 = A_PHI + wid * 5632 + r32 * 88 + hh * 40;
    c4 = A_PHI + wid * 5632 + (8 * hh + q4) * 88 + (16 * b16 + 4 * p4) * 2;
    c5 = (vq < 5 ? L_LOC + SEG_V + hl * VH + vq * VP : L_ONE) + (rw0 + 8 * hh + q4) * 8;
    c6 = A_PHI + wid * 5632 + min(r32, 21) * 160 + hh * 16;
    v16 = o_off(4 * nt + hl, mt * 128 + rw0 + r32);
  }
  {
    const int odd = br ? hs : (hl & 1);
    v8 = hh ? OOB : (v16 & ~1023u) + 2048 + 512 * odd + ((v16 & 1023u) >> 1);
    v16 += (hh + 3 * odd) * 1024;
  }
  PIN(c0); PIN(c1); PIN(c2); PIN(c3); PIN(c4); PIN(c5); PIN(c6); PIN(v16); PIN(v8);
  f32x16 mle, mgt;
#pragma unroll
  for (int e = 0; e < 16; ++e) {
    const bool le = (e & 3) + 8 * (e >> 2) + 4 * hh <= r32;
    mle[e] = le ? 0.f : -INFINITY;
    mgt[e] = le ? -INFINITY : 0.f;
  }
  PIN(mle); PIN(mgt);
  const rsrc_t rg = mkrsrc(ws + WS_G, WS_KVH);
  const rsrc_t rph = mkrsrc((const char*)wphi + nt * 3200, 3200);
  bf16x8 x0[4], x1[4];
  s16x4 w0[4][2], w1[4][2];
#define G_RD(X, W, xa, kk, r) do { \
    if ((r) < 2) W[0][(r)] = trread(wa + ((kk) * 32 + (r) * 8) * 480); \
    else if ((r) < 6) X[(r) - 2] = *(const LAS bf16x8*)(xa + ((r) - 2) * 2048); \
    else if ((r) < 12) W[((r) - 4) >> 1][(r) & 1] = trread(wa + ((kk) * 32 + ((r) & 1) * 8) * 480 + (((r) - 4) >> 1) * 32); } while (0)
#define G_MM(X, W, m) acc[(m) & 3][(m) >> 2] = MFMA16(cat8(W[(m) >> 2][0], W[(m) >> 2][1]), X[(m) & 3], acc[(m) & 3][(m) >> 2])
  asm volatile("s_barrier" ::: "memory");
#pragma unroll
  for (int i = 0; i < 6; ++i) G_DMA(i, G_STAGE, sx + 128, sw + 245760);
  if ((wid & 3) >= 2) { WAIT_BAR(7); } else { WAIT_BAR(6); }
  STAMP(0, 1);
#pragma unroll
  for (int r = 0; r < 12; ++r) G_RD(x0, w0, xa0, 0, r);
  int bo = 2 * G_STAGE;
  int bd = bo;
  unsigned xo = sx + 256, wo = sw + 2 * 245760;
#pragma unroll
  for (int i = 0; i < 3; ++i) G_DMA(i, bo, xo, wo);
#pragma unroll 1
  for (int t = 0; t < 10; ++t) {
    SB();
#pragma unroll
    for (int m = 0; m < 12; ++m) {
      G_MM(x0, w0, m); G_RD(x1, w1, xa1, 1, m);
      if (m % 3 == 2 && m < 9) G_DMA(3 + m / 3, bd, xo, wo);
      SB();
    }
    if (wn < 3) { G_MM(x0, w0, 12); G_MM(x0, w0, 13); G_MM(x0, w0, 14); G_MM(x0, w0, 15); }
    const int d = bo == G_STAGE ? -2 * G_STAGE : G_STAGE;
    xa0 += d; xa1 += d; wa += d;
    bo = bo == 2 * G_STAGE ? 0 : bo + G_STAGE;
    bd = t < 7 ? bo : 0x30000;
    xo = t < 7 ? xo + 128 : OOB;
    wo += 245760;
    WAIT_BAR(6);
    SB();
#pragma unroll
    for (int m = 0; m < 12; ++m) {
      G_MM(x1, w1, m); G_RD(x0, w0, xa0, 0, m);
      if (m % 3 == 2 && m < 9) G_DMA(m / 3, bd, xo, wo);
      SB();
    }
    if (wn < 3) { G_MM(x1, w1, 12); G_MM(x1, w1, 13); G_MM(x1, w1, 14); G_MM(x1, w1, 15); }
    if (t == (bi & 3) + TB) {
      if ((wid & 3) == 3) touch(rt, lds0 + L_T, vt, (6 - TB + t) * tst);
      if (wid == 1) touch(mkrsrc(br ? woB : woA, 640 * 1280), lds0 + L_T, lane < 50 ? (unsigned)((bi * 50 + lane) * 128) : OOB, 0);
      if (wid == 0) dma16(rg, lds0 + L_G, cons >= 0 ? cons * 16 : OOB, 0);
      if (wid == 5 && !br) {
#pragma unroll
        for (int i = 0; i < 4; ++i) dma16(rph, lds0 + L_W + i * 1024, (unsigned)lane * 16, i * 1024);
      }
    }
  }
  STAMP(0, 2);
  {
    const rsrc_t rh = mkrsrc(ws + WS_KVH + (mt * 8 + nt) * 40960, 40960);
    const int v0 = (16 * wn + g - 20) * 1024 + wm * 512 + fr * 16;
#pragma unroll
    for (int j = 3; j >= 0; --j) {
      if (br && 4 * wn + j < 5) {
#pragma unroll
        for (int i = 0; i < 4; ++i) acc[i][j] *= SC2;
      }
#pragma unroll
      for (int i = 0; i < 2; ++i) {
        const f32x4 a = acc[2 * i][j], b = acc[2 * i + 1][j];
        const u32x4 ev = (u32x4){pk2(a[0], a[1]), pk2(a[2], a[3]), pk2(b[0], b[1]), pk2(b[2], b[3])};
        if (br && 4 * wn + j >= 5) st16c(rh, (unsigned)(v0 + j * 4096 + i * 256), ev);
        *(LAS u32x4*)(smw + eb[j] + i * 256) = ev;
      }
    }
  }
  if (tid < 128) *(LAS u32x2*)(smw + L_ONE + tid * 8) = (u32x2){0x3F80u, 0u};
  if (tid == 511) *(LAS unsigned*)(smw + L_FB) = 0u;
  const u32x2 gc = next_tag(*(const LAS u32x2*)(sm + L_G + lane * 16));
  const u32x2 tag = {(unsigned)__builtin_amdgcn_readlane(gc[0], 63), (unsigned)__builtin_amdgcn_readlane(gc[1], 63)};
  const char* wog = (const char*)(br ? woB : woA);
  const rsrc_t ro = mkrsrc(ws + WS_O + br * (SEQ * DM * 2), SEQ * DM * 2);
  const rsrc_t rwo = mkrsrc(wog + nt3 * 160, 640 * 1280 - nt3 * 160);
  const int sg = 2 * (wid >> 2) + (wid & 1), st = (wid >> 1) & 1;
#define P_DMA2(w5, G) dma16m<0xFFFFFFFFu, 0xFFu>(rwo, lds0 + P_W + (4 * (w5) + (G)) * 5120 + wid * 640, vwo, \
      (unsigned)(160 * (G) + 4 * (w5) + ((w5) >= 2 ? 8 : 0) + 40 * sg + st * ((w5) == 2 ? 20 : 8)) * 1280u)
#define P_DMA(b) P_DMA2((b) >> 2, (b) & 3)
  if (br) {
    WAIT_BAR(0);
    if (wid == 0) st8c(rg, vf1, gc);
    STAMP(0, 3);
    Strm S[2];
#pragma unroll
    for (int r = 0; r < 2; ++r) {
      S[r].kh = sm + c0 + r * 2 * KH;
      S[r].k4 = sm + c1 + r * 2 * KH;
      const LAS char* qp = sm + (r ? c5 : c4);
      S[r].q0 = frag2(lds8(qp + c6), lds8(qp + c6 + 1024));
      const u32x2 t4 = lds8(qp + 4096);
      S[r].q1 = hh ? (u32x2){0u, 0u} : t4;
    }
#define VPTR(r, seg) (sm + c2 + (((seg) + (r) * 2 * VH) & c3))
#define HALO(tile, seg, pg) do { \
      const rsrc_t rn_ = mkrsrc(ws + WS_KVH + (((tile) >= 0 ? (tile) : 0) * 8 + nt) * 40960 + (pg) * 10240, (tile) >= 0 ? 10240 : 0); \
      const unsigned hd_ = lds0 + (seg) + ((pg) >= 2 ? SEG_V + ((pg) & 1) * 2 * VH : ((pg) & 1) * 2 * KH), hp_ = (pg) >= 2 ? VP : KP; \
      _Pragma("unroll") for (int i = 0; i < 10; ++i) dma16c(rn_, hd_ + i * hp_ + (i >= 5 ? ((pg) >> 1) * 96 : 0), lane * 16, i * 1024); } while (0)
    f32x16 o0 = {}, o1 = {};
    Pipe p0 = prime(S[0], mle, L_LOC + qi * 256, -256), p1 = prime(S[1], mle, L_LOC + (3 - qi) * 256, -256);
    run_tiles(o0, S[0], p0, VPTR(0, L_LOC) + qi * 256, L_LOC + qi * 256, -256, qi + 1);
    STAMP(0, 4);
    const int nb = qi < 2 ? 1 : 0, pg = 2 * hs + (qi & 1);
    wait_flags(rg, (unsigned)(WS_IN + me * 256 + nb * 8), mt - 2 + nb >= 0, tag);
    STAMP(0, 5);
    HALO(mt - 2 + nb, nb ? L_NEAR : L_FAR, pg);
    run_tiles(o1, S[1], p1, VPTR(1, L_LOC) + (3 - qi) * 256, L_LOC + (3 - qi) * 256, -256, 4 - qi);
    STAMP(0, 6);
    if (qi < 2) { WAIT_BAR(0); } else { LBAR(); }
    STAMP(0, 7);
    if (mt > 0) {
      const f32x16 z = {};
      KFr k = kread(S[1], L_NEAR);
      f32x16 sa = qk_tile(S[0].kh + L_NEAR, S[0].k4 + L_NEAR, S[0].q0, S[0].q1, z), sb;
      const LAS char* v0 = VPTR(0, L_NEAR);
      const LAS char* v1 = VPTR(1, L_NEAR);
#pragma unroll 1
      for (int off = 0; off < 1024; off += 256) {
        P_DMA2(0, off >> 8); P_DMA2(1, off >> 8);
        tile_step(o0, sa, v0 + off, sb, S[1], k, S[0], L_NEAR + off + 256);
        P_DMA2(2, off >> 8);
        tile_step(o1, sb, v1 + off, sa, S[0], k, S[1], L_NEAR + off + 256);
        if (off == 512) { WAIT_BAR(9); }
      }
    } else {
#pragma unroll
      for (int ks = 0; ks < 12; ++ks) P_DMA(ks);
    }
    p0 = prime(S[0], mgt, L_FAR + qi * 256, 256);
    p1 = prime(S[1], mgt, L_FAR + (3 - qi) * 256, 256);
    STAMP(0, 8);
    LBAR();
    bool redo[2];
#pragma unroll
    for (int r = 0; r < 2; ++r) {
      const int qt = r ? 3 - qi : qi;
      f32x16& o = r ? o1 : o0;
#pragma unroll
      for (int G = 0; G < 4; ++G) P_DMA2(3 + r, G);
      if (mt > 1) run_tiles(o, S[r], r ? p1 : p0, VPTR(r, L_FAR) + qt * 256, L_FAR + qt * 256, 256, 4 - qt);
      const float l = __uint_as_float(__builtin_amdgcn_permlane32_swap(__float_as_uint(o[8]), __float_as_uint(o[8]), false, false)[1]);
      const float rl = __builtin_amdgcn_rcpf(l);
      float of[12];
#pragma unroll
      for (int i = 0; i < 12; ++i) of[i] = o[i] * rl;
      const unsigned dv = r ? (unsigned)((3 - 2 * qi) * 40960 + 256) : 0u;
      store_o(ro, v16 + dv, hh ? OOB : v8 + dv - (r ? 128 : 0), of);
      float am = __builtin_fabsf(o[0]);
#pragma unroll
      for (int i = 1; i < 11; i += 2) am = fmax3(am, __builtin_fabsf(o[i]), __builtin_fabsf(o[i + 1]));
      am = __builtin_elementwise_maximum(am, __builtin_fabsf(o[11]));
      redo[r] = !__all(l >= 0x1p-80f && l <= FB_HI && am <= 0x1p126f);
    }
    if (redo[0] || redo[1]) *(LAS unsigned*)(smw + L_FB) = 1u;
    STAMP(0, 9);
    WAIT_BAR(0);
    STAMP(0, 10);
    if (__builtin_expect(__builtin_amdgcn_readfirstlane(*(const volatile LAS unsigned*)(sm + L_FB)) != 0u, 0)) {
      HALO(mt - nb, nb ? L_NEAR : L_LOC, pg);
      WAIT_BAR(0);
#pragma unroll
      for (int r = 0; r < 2; ++r) {
        if (!redo[r]) continue;
        const int qt = r ? 3 - qi : qi, w0 = mt >= 2 ? qt : mt == 1 ? 4 : 8;
        f32x16 o;
        float m = -INFINITY;
#pragma unroll 1
        for (int pass = 0; pass < 2; ++pass) {
          o = (f32x16){};
#pragma unroll 1
          for (int w = w0; w <= qt + 8; ++w) {
            const int seg = w < 4 ? L_FAR : w < 8 ? L_NEAR : L_LOC, off = (w & 3) * 256;
            f32x16 c;
#pragma unroll
            for (int e = 0; e < 16; ++e) c[e] = w == qt + 8 ? mle[e] : w == qt ? mgt[e] : 0.f;
            f32x16 s = qk_tile(S[r].kh + seg + off, S[r].k4 + seg + off, S[r].q0, S[r].q1, c);
            if (pass == 0) {
#pragma unroll
              for (int e = 0; e < 16; e += 2) m = fmax3(m, s[e], s[e + 1]);
            } else {
#pragma unroll
              for (int e = 0; e < 16; ++e) s[e] -= m;
              o = pv_tile(VPTR(r, seg) + off, s, o);
            }
          }
          auto rr = __builtin_amdgcn_permlane32_swap(__float_as_uint(m), __float_as_uint(m), false, false);
          m = __builtin_elementwise_maximum(__uint_as_float(rr[0]), __uint_as_float(rr[1]));
        }
        const float rl = __builtin_amdgcn_rcpf(__uint_as_float(__builtin_amdgcn_permlane32_swap(__float_as_uint(o[8]), __float_as_uint(o[8]), false, false)[1]));
        float of[12];
#pragma unroll
        for (int i = 0; i < 12; ++i) of[i] = o[i] * rl;
        const unsigned dv = r ? (unsigned)((3 - 2 * qi) * 40960 + 256) : 0u;
        store_o(ro, v16 + dv, hh ? OOB : v8 + dv - (r ? 128 : 0), of);
      }
      LBAR();
#pragma unroll 1
      for (int G = 0; G < 4; ++G) {
#pragma unroll
        for (int u = 0; u < 5; ++u) P_DMA2(u, G);
      }
      WAIT_BAR(0);
    }
  } else {
    WFrag wf = load_wfrag(sm + c0, sm + c1, hh, lane);
    PIN(wf.t0k0); PIN(wf.t1k0); PIN(wf.t0k1); PIN(wf.t1k1);
#pragma unroll
    for (int ks = 13; ks < 20; ++ks) P_DMA(ks);
    LBAR();
    STAMP(0, 3);
    const LAS char* xp = sm + c2 + hh * 2048;
    const LAS char* x4 = sm + c2 + 4096;
    const HRow rk0 = hh_read(xp + L_LOC, x4 + L_LOC), rk1 = hh_read(xp + L_LOC + 256, x4 + L_LOC + 256);
    const HRow rq0 = hh_read(xp + L_Q, x4 + L_Q), rq1 = hh_read(xp + L_Q + 256, x4 + L_Q + 256);
    const HProj hk0 = hh_proj(wf, rk0);
    bf16x8 vf[4];
#pragma unroll
    for (int ks = 0; ks < 4; ++ks) vf[ks] = cat8(trread(sm + c5 + ks * 128), trread(sm + c5 + ks * 128 + 32));
    SB();
    f32x16 d0 = {}, d1 = {};
    HProj hq0, hq1;
    {
      unsigned ph[10];
      HProj hk1 = hh_proj(wf, rk1);
      hh_soft(hk0, ph);
#pragma unroll
      for (int i = 0; i < 5; ++i) *(LAS u32x2*)(smw + c3 + 8 * i) = (u32x2){ph[2 * i], ph[2 * i + 1]};
      PIN(hk1.p0); PIN(hk1.p1);
      mix_mfma<4, 8>();
      SB();
      bf16x8 pf[4];
#pragma unroll
      for (int i = 0; i < 4; ++i) pf[i] = cat8(trread(sm + c4 + (i >> 1) * 16 * 88 + (i & 1) * 64), trread(sm + c4 + ((i >> 1) * 16 + 4) * 88 + (i & 1) * 64));
      SB();
      hq0 = hh_proj(wf, rq0);
      d0 = MFMA32(pf[0], vf[0], d0); d1 = MFMA32(pf[1], vf[0], d1);
      d0 = MFMA32(pf[2], vf[1], d0); d1 = MFMA32(pf[3], vf[1], d1);
      hh_soft(hk1, ph);
#pragma unroll
      for (int i = 0; i < 5; ++i) *(LAS u32x2*)(smw + c3 + 32 * 88 + 8 * i) = (u32x2){ph[2 * i], ph[2 * i + 1]};
      PIN(hq0.p0); PIN(hq0.p1); PIN(d0); PIN(d1);
      mix_mfma<8, 8>();
      SB();
#pragma unroll
      for (int i = 0; i < 4; ++i) pf[i] = cat8(trread(sm + c4 + (32 + (i >> 1) * 16) * 88 + (i & 1) * 64), trread(sm + c4 + (36 + (i >> 1) * 16) * 88 + (i & 1) * 64));
      d0 = MFMA32(pf[0], vf[2], d0); d1 = MFMA32(pf[1], vf[2], d1);
      d0 = MFMA32(pf[2], vf[3], d0); d1 = MFMA32(pf[3], vf[3], d1);
    }
    *(LAS f32x4*)(smw + c6) = (f32x4){d0[0], d0[1], d0[2], d0[3]};
    *(LAS f32x4*)(smw + c6 + 32) = (f32x4){d0[4], d0[5], d0[6], d0[7]};
    *(LAS f32x4*)(smw + c6 + 64) = (f32x4){d0[8], d0[9], d0[10], d0[11]};
    *(LAS f32x4*)(smw + c6 + 96) = (f32x4){d0[12], d0[13], d0[14], d0[15]};
    *(LAS f32x4*)(smw + c6 + 128) = (f32x4){d1[0], d1[1], d1[2], d1[3]};
    SB();
    hq1 = hh_proj(wf, rq1);
    PIN(hq1.p0); PIN(hq1.p1);
    LBAR();
    STAMP(0, 4);
    const rsrc_t rkv = mkrsrc(ws + WS_KVP + nt * 16 * 6784, 16 * 6784);
    {
      const int e = (wid & 1) * 64 + lane;
      const LAS char* o = sm + A_PHI + (wid & ~1) * 5632 + e * 32;
      const f32x4 a = *(const LAS f32x4*)o + *(const LAS f32x4*)(o + 5632), b = *(const LAS f32x4*)(o + 16) + *(const LAS f32x4*)(o + 5648);
      st16c(rkv, e < 105 ? (unsigned)(mt * 6784 + hl * 1680 + e * 16) : OOB, (u32x4){pk2(a[0], a[1]), pk2(a[2], a[3]), pk2(b[0], b[1]), pk2(b[2], b[3])});
    }
#pragma unroll
    for (int ks = 0; ks < 6; ++ks) P_DMA(ks);
    SB();
    unsigned phq[2][10];
    hh_soft(hq0, phq[0]);
#pragma unroll
    for (int i = 0; i < 10; ++i) PIN(phq[0][i]);
    WAIT_BAR(6);
    if (wid == 0) st8c(rg, vf1, gc);
    if (tid >= 256) *(LAS u32x2*)(smw + A_KV + ((tid - 256) >> 1) * 112 + (tid & 1) * 48 + 40) = (u32x2){0u, 0u};
    STAMP(0, 5);
    hh_soft(hq1, phq[1]);
#pragma unroll
    for (int i = 0; i < 10; ++i) PIN(phq[1][i]);
    int kd0, kd1;
    {
      const int row = tid / 5, oc = tid - row * 5, h4 = row / 21, d = row - h4 * 21;
      kd0 = tid < 420 ? A_KV + h4 * 3584 + d * 112 + oc * 16 + (oc > 2 ? 8 : 0) : L_S;
      kd1 = kd0 + (oc == 2 ? 16 : 8);
      PIN(kd0); PIN(kd1);
    }
    wait_flags(rg, (unsigned)(WS_IN + me * 256 + fr * 8), true, tag);
    STAMP(0, 6);
    if (wid < 7) {
      u32x4 p[16];
#pragma unroll
      for (int i = 0; i < 16; ++i) p[i] = __builtin_amdgcn_raw_buffer_load_b128(rkv, tid * 16, i * 6784, 16);
      f32x2 lo[2] = {}, hi[2] = {};
#pragma unroll
      for (int i = 0; i < 16; ++i)
#pragma unroll
        for (int u = 0; u < 2; ++u) {
          lo[u] += (f32x2){__uint_as_float(p[i][2 * u] << 16), __uint_as_float(p[i][2 * u + 1] << 16)};
          hi[u] += (f32x2){__uint_as_float(p[i][2 * u] & 0xFFFF0000u), __uint_as_float(p[i][2 * u + 1] & 0xFFFF0000u)};
        }
      *(LAS u32x2*)(smw + kd0) = (u32x2){pk2(lo[0][0], hi[0][0]), pk2(lo[0][1], hi[0][1])};
      *(LAS u32x2*)(smw + kd1) = (u32x2){pk2(lo[1][0], hi[1][0]), pk2(lo[1][1], hi[1][1])};
    }
#pragma unroll
    for (int ks = 6; ks < 13; ++ks) P_DMA(ks);
    LBAR();
    STAMP(0, 7);
    const LAS char* kp = sm + A_KV + hl * 3584 + (r32 == 24 ? 20 : r32) * 112 + hh * 48;
    const bf16x8 f0 = *(const LAS bf16x8*)(kp), f1 = *(const LAS bf16x8*)(kp + 16), f2 = *(const LAS bf16x8*)(kp + 32);
    f32x16 n[2];
#pragma unroll
    for (int t = 0; t < 2; ++t) n[t] = MFMA32(f0, asfrag((u32x4){phq[t][0], phq[t][1], phq[t][2], phq[t][3]}), (f32x16){});
#pragma unroll
    for (int t = 0; t < 2; ++t) n[t] = MFMA32(f1, asfrag((u32x4){phq[t][4], phq[t][5], phq[t][6], phq[t][7]}), n[t]);
#pragma unroll
    for (int t = 0; t < 2; ++t) n[t] = MFMA32(f2, asfrag((u32x4){phq[t][8], phq[t][9], 0u, 0u}), n[t]);
#pragma unroll
    for (int t = 0; t < 2; ++t) {
      const float den = rbf(rbf(hh ? n[t][8] : n[t][12]) + 1e-6f);
      const float r = __builtin_amdgcn_rcpf(den);
      float o[12];
#pragma unroll
      for (int i = 0; i < 12; ++i) o[i] = rbf(n[t][i]) * r;
      store_o(ro, v16 + t * 40960, hh ? OOB : v8 + t * 40960, o);
    }
    STAMP(0, 9);
    WAIT_BAR(0);
    STAMP(0, 10);
  }
  {
    if (wid == 0) st8c(rg, vf2, gc);
    STAMP(0, 11);
    int oa[3];
    unsigned og[3];
#pragma unroll
    for (int i = 0; i < 3; ++i) {
      const int c = min(lane + 64 * i, 160), row = c / 10, ch = c - row * 10;
      oa[i] = wid * 2816 + row * 176 + ch * 16;
      og[i] = (unsigned)(row * 2560 + ch * 16);
      PIN(oa[i]); PIN(og[i]);
    }
    const int pm = wid & 3, kh = wid >> 2;
    wait_flags(rg, (unsigned)(WS_IN + me * 256 + 128 + (4 * kh + (lane & 3)) * 8), true, tag);
    STAMP(0, 12);
    const rsrc_t rf = mkrsrc(ws + WS_O + (unsigned)((br * 128 + mt3 * 8 + 2 * pm) * 20 + 10 * kh) * 1024u, 30 * 1024);
    const unsigned vk = lane * 16 + kh * 20480, vh = lane * 16 + (1 - kh) * 20480;
    u32x4 xf[10][2];
    const unsigned vk8 = lane * 8 + kh * 20480, vh8 = lane * 8 + (1 - kh) * 20480;
#define P_LD1(v, v8_, tau) ((tau) % 5 == 2 ? __builtin_bit_cast(u32x4, frag2(__builtin_amdgcn_raw_buffer_load_b64(rf, v8_, (tau) * 1024, 16), \
                                                                             __builtin_amdgcn_raw_buffer_load_b64(rf, v8_, (tau) * 1024 + 512, 16))) \
                                           : __builtin_amdgcn_raw_buffer_load_b128(rf, v, (tau) * 1024, 16))
#define P_LD(tau) do { xf[tau][0] = P_LD1(vk, vk8, tau); xf[tau][1] = P_LD1(vh, vh8, tau); } while (0)
    P_LD(0); P_LD(1); P_LD(2);
    const LAS char* wb = sm + P_W + kh * 10240 + (16 * (g >> 1) + 4 * (g & 1) + q4) * 160 + 8 * p4;
    const LAS char* wb2 = wb + 12 * 5120;
    f32x4 pc[2][5] = {};
    bf16x8 wf[2][5];
#pragma unroll
    for (int j = 0; j < 5; ++j) wf[0][j] = cat8(trread(wb + j * 32), trread(wb + 8 * 160 + j * 32));
#pragma unroll
    for (int tau = 0; tau < 10; ++tau) {
      SB();
      if (tau < 7) P_LD(tau + 3);
#pragma unroll
      for (int j = 0; j < 5; ++j) {
        if (tau < 9) {
          const int blk = ((tau + 1) % 5) * 4 + (tau + 1) / 5;
          const LAS char* wp = (blk >= 12 ? wb2 : wb) + (blk >= 12 ? blk - 12 : blk) * 5120 + j * 32;
          wf[(tau + 1) & 1][j] = cat8(trread(wp), trread(wp + 8 * 160));
        }
        pc[1][j] = MFMA16(wf[tau & 1][j], asfrag(xf[tau][1]), pc[1][j]);
        if (tau < 9) pc[0][j] = MFMA16(wf[tau & 1][j], asfrag(xf[tau][0]), pc[0][j]);
        SB();
      }
    }
#pragma unroll
    for (int j = 0; j < 5; ++j) {
      pc[0][j] = MFMA16(wf[1][j], asfrag(xf[9][0]), pc[0][j]);
      *(LAS f32x4*)(smw + P_X + (wid * 5 + j) * 1024 + lane * 16) = pc[1][j];
    }
    STAMP(0, 13);
    LBAR();
    if (tid == 0) st8c(rg, me * 16, tag);
    LAS char* ow = smw + wid * 2816 + fr * 176 + g * 8;
#pragma unroll
    for (int j = 0; j < 5; ++j) {
      const f32x4 a = pc[0][j] + *(const LAS f32x4*)(sm + P_X + ((wid ^ 4) * 5 + j) * 1024 + lane * 16);
      *(LAS u32x2*)(ow + j * 32) = (u32x2){pk2(a[0], a[1]), pk2(a[2], a[3])};
    }
    const rsrc_t rout = mkrsrc((char*)out + (size_t)(mt3 * 128 + 32 * pm + 16 * kh) * 2560 + br * 1280 + nt3 * 160, 15 * 2560 + 160);
#pragma unroll
    for (int i = 0; i < 3; ++i) __builtin_amdgcn_raw_buffer_store_b128(*(const LAS u32x4*)(sm + oa[i]), rout, og[i], 0, OUT_AUX);
  }
  STAMP(0, 15);
  STAMP_FLUSH(0);
}

extern "C" void kernel_launch(void* const* d_in, const int* in_sizes, int n_in, void* d_out, int out_size, void* d_ws,
                              size_t ws_size, hipStream_t stream) {
  k1_fused<<<256, 512, 0, stream>>>((const __hip_bfloat16*)d_in[0], (const __hip_bfloat16*)d_in[1], (const __hip_bfloat16*)d_in[4],
                                    (const __hip_bfloat16*)d_in[5], (const __hip_bfloat16*)d_in[2], (const __hip_bfloat16*)d_in[3],
                                    (const __hip_bfloat16*)d_in[6], (char*)d_ws, (__hip_bfloat16*)d_out);
}
```

```cpp
#include <hip/hip_runtime.h>
#include <hip/hip_bf16.h>

typedef short bf16x8 __attribute__((ext_vector_type(8)));
typedef short s16x4 __attribute__((ext_vector_type(4)));
typedef float f32x2 __attribute__((ext_vector_type(2)));
typedef float f32x4 __attribute__((ext_vector_type(4)));
typedef float f32x16 __attribute__((ext_vector_type(16)));
typedef unsigned u32x2 __attribute__((ext_vector_type(2)));
typedef unsigned u32x4 __attribute__((ext_vector_type(4)));
typedef __bf16 bf16x2_t __attribute__((ext_vector_type(2)));
typedef __amdgpu_buffer_rsrc_t rsrc_t;
#define LAS __attribute__((address_space(3)))
#define GAS __attribute__((address_space(1)))
#define DI __device__ __forceinline__

#define PFV 6
#define OUT_AUX 0
#define TB 1
#define INB 1
#define FB_HI 0x1p100f
#define CPF 1
constexpr int SEQ = 2048, DM = 640, NH = 32;
constexpr unsigned WS_G = 0;
constexpr unsigned WS_IN = 4096;
constexpr unsigned WS_KVH = WS_IN + 256 * 256;
constexpr unsigned WS_KVP = WS_KVH + 128 * 40960;
constexpr unsigned WS_O = WS_KVP + 128 * 7168;
constexpr unsigned WS_END = WS_O + 2 * SEQ * DM * 2;
constexpr unsigned OOB = 0x7FFF0000u;

DI unsigned pk2(float lo, float hi) {
  bf16x2_t r = __builtin_convertvector((f32x2){lo, hi}, bf16x2_t);
  return __builtin_bit_cast(unsigned, r);
}
DI float rbf(float x) { return __builtin_bit_cast(float, pk2(0.f, x)); }
DI float fmax3(float a, float b, float c) { return __builtin_elementwise_maximum(__builtin_elementwise_maximum(a, b), c); }
DI s16x4 trread(const LAS char* p) { return __builtin_amdgcn_ds_read_tr16_b64_v4i16((LAS s16x4*)p); }
DI bf16x8 cat8(s16x4 a, s16x4 b) { return __builtin_shufflevector(a, b, 0, 1, 2, 3, 4, 5, 6, 7); }
DI bf16x8 asfrag(u32x4 v) { return __builtin_bit_cast(bf16x8, v); }
DI bf16x8 frag2(u32x2 a, u32x2 b) { return asfrag((u32x4){a[0], a[1], b[0], b[1]}); }
DI rsrc_t mkrsrc(const void* p, unsigned bytes) { return __builtin_amdgcn_make_buffer_rsrc((void*)p, (short)0, (int)bytes, 0x00020000); }
DI void dma16(rsrc_t rs, unsigned lds, unsigned voff, unsigned soff) {
  asm volatile("s_mov_b32 m0, %0\n\ts_nop 0\n\tbuffer_load_dwordx4 %1, %2, %3 offen lds"
               :: "s"(lds), "v"(voff), "s"(rs), "s"(soff) : "memory");
}
DI void dma16c(rsrc_t rs, unsigned lds, unsigned voff, unsigned soff) {
  asm volatile("s_mov_b32 m0, %0\n\ts_nop 0\n\tbuffer_load_dwordx4 %1, %2, %3 offen sc1 lds"
               :: "s"(lds), "v"(voff), "s"(rs), "s"(soff) : "memory");
}
DI void touch(rsrc_t rs, unsigned lds, unsigned voff, unsigned soff) {
  asm volatile("s_mov_b32 m0, %0\n\ts_nop 0\n\tbuffer_load_dword %1, %2, %3 offen lds"
               :: "s"(lds), "v"(voff), "s"(rs), "s"(soff) : "memory");
}
DI void touchc(rsrc_t rs, unsigned lds, unsigned voff) {
  asm volatile("s_mov_b32 m0, %0\n\ts_nop 0\n\tbuffer_load_dword %1, %2, 0 offen sc1 lds" :: "s"(lds), "v"(voff), "s"(rs) : "memory");
}
template <unsigned LO, unsigned HI>
DI void dma16m(rsrc_t rs, unsigned lds, unsigned voff, unsigned soff) {
  asm volatile("s_mov_b32 m0, %0\n\ts_mov_b32 exec_lo, %4\n\ts_mov_b32 exec_hi, %5\n\tbuffer_load_dwordx4 %1, %2, %3 offen lds\n\ts_mov_b64 exec, -1"
               :: "s"(lds), "v"(voff), "s"(rs), "s"(soff), "i"(LO), "i"(HI) : "memory");
}
DI void ld8(u32x2& d, rsrc_t rs, unsigned voff, unsigned soff) {
  asm volatile("buffer_load_dwordx2 %0, %1, %2, %3 offen" : "=v"(d) : "v"(voff), "s"(rs), "s"(soff) : "memory");
}
DI void ld16(u32x4& d, rsrc_t rs, unsigned voff, unsigned soff) {
  asm volatile("buffer_load_dwordx4 %0, %1, %2, %3 offen" : "=v"(d) : "v"(voff), "s"(rs), "s"(soff) : "memory");
}
DI void ld16c(u32x4& d, rsrc_t rs, unsigned voff, unsigned soff) {
  asm volatile("buffer_load_dwordx4 %0, %1, %2, %3 offen sc1" : "=v"(d) : "v"(voff), "s"(rs), "s"(soff) : "memory");
}
DI void st16c(rsrc_t rs, unsigned voff, u32x4 v) { __builtin_amdgcn_raw_buffer_store_b128(v, rs, voff, 0, 16); }
DI void st8c(rsrc_t rs, unsigned voff, u32x2 v) { __builtin_amdgcn_raw_buffer_store_b64(v, rs, voff, 0, 16); }
DI u32x2 lds8(const LAS char* p) { return *(const volatile LAS u32x2*)p; }
DI u32x2 next_tag(u32x2 g) { return (u32x2){g[0] * 1664525u + 1013904223u, g[1] * 22695477u + 2654435761u}; }
#define MFMA16(a, b, c) __builtin_amdgcn_mfma_f32_16x16x32_bf16((a), (b), (c), 0, 0, 0)
#define MFMA32(a, b, c) __builtin_amdgcn_mfma_f32_32x32x16_bf16((a), (b), (c), 0, 0, 0)
#define MFMA32K8(a, b, c) __builtin_amdgcn_mfma_f32_32x32x8bf16_1k(__builtin_bit_cast(s16x4, a), __builtin_bit_cast(s16x4, b), (c), 0, 0, 0)
#define WAIT_BAR(N) asm volatile("s_waitcnt vmcnt(" #N ") lgkmcnt(0)\n\ts_barrier" ::: "memory")
#define LBAR() asm volatile("s_waitcnt lgkmcnt(0)\n\ts_barrier" ::: "memory")
#define SB() __builtin_amdgcn_sched_barrier(0)
#define PIN8(a) asm volatile("" : "+v"(a[0]), "+v"(a[1]), "+v"(a[2]), "+v"(a[3]), "+v"(a[4]), "+v"(a[5]), "+v"(a[6]), "+v"(a[7]))

#define STAMP_DECL
#define STAMP(kid, i)
#define STAMP_FLUSH(kid)

constexpr float LOG2E = 1.4426950408889634f;

struct WFrag { bf16x8 t0k0, t1k0; u32x2 t0k1, t1k1; };
DI WFrag load_wfrag(const LAS char* a0  , const LAS char* a1  , int hh, int lane) {
  u32x2 r0 = __builtin_bit_cast(u32x2, trread(a0)), r1 = __builtin_bit_cast(u32x2, trread(a0 + 160));
  u32x2 r2 = __builtin_bit_cast(u32x2, trread(a1)), r3 = __builtin_bit_cast(u32x2, trread(a1 + 160));
  u32x2 r4 = __builtin_bit_cast(u32x2, trread(a0 + 640)), r5 = __builtin_bit_cast(u32x2, trread(a1 + 640));
  const unsigned sg = (lane & 4) ? 0x80008000u : 0u;
  const unsigned km = hh ? 0u : 0xFFFFFFFFu;
  WFrag w;
  w.t0k0 = asfrag((u32x4){r0[0] ^ sg, r0[1] ^ sg, r1[0] ^ sg, r1[1] ^ sg});
  w.t1k0 = asfrag((u32x4){r2[0] ^ sg, r2[1] ^ sg, r3[0] ^ sg, r3[1] ^ sg});
  w.t0k1 = (u32x2){(r4[0] ^ sg) & km, (r4[1] ^ sg) & km};
  w.t1k1 = (u32x2){(r5[0] ^ sg) & km, (r5[1] ^ sg) & km};
  return w;
}
struct HProj { f32x16 p0, p1; };
struct HRow { bf16x8 x0; u32x2 x1; };
DI HRow hh_read(const LAS char* p, const LAS char* p4) { HRow r; r.x0 = frag2(lds8(p), lds8(p + 1024)); r.x1 = lds8(p4); return r; }
DI HProj hh_proj(const WFrag& w, const HRow& x) {
  const f32x16 z = {};
  HProj h;
  h.p0 = MFMA32(w.t0k0, x.x0, z);
  h.p1 = MFMA32(w.t1k0, x.x0, z);
  h.p0 = MFMA32K8(w.t0k1, x.x1, h.p0);
  h.p1 = MFMA32K8(w.t1k1, x.x1, h.p1);
  return h;
}
template <int N, int NV> DI void mix_mfma() {
#pragma unroll
  for (int i = 0; i < N; ++i) { __builtin_amdgcn_sched_group_barrier(0x8, 1, 0); __builtin_amdgcn_sched_group_barrier(0x402, NV, 0); }
}
DI void hh_soft(const HProj& h, unsigned (&ph)[10]) {
  f32x2 v[10];
#pragma unroll
  for (int i = 0; i < 8; ++i) v[i] = (f32x2){rbf(h.p0[2 * i]), rbf(h.p0[2 * i + 1])};
#pragma unroll
  for (int i = 0; i < 2; ++i) v[8 + i] = (f32x2){rbf(h.p1[2 * i]), rbf(h.p1[2 * i + 1])};
  float m = __builtin_elementwise_maximum(v[0][0], v[0][1]);
#pragma unroll
  for (int i = 1; i < 10; ++i) m = fmax3(m, v[i][0], v[i][1]);
  const float nm = -m * LOG2E;
#pragma unroll
  for (int i = 0; i < 10; ++i) {
    v[i] = __builtin_elementwise_fma(v[i], (f32x2){LOG2E, LOG2E}, (f32x2){nm, nm});
    v[i] = (f32x2){__builtin_amdgcn_exp2f(v[i][0]), __builtin_amdgcn_exp2f(v[i][1])};
  }
  const f32x2 s = (((v[0] + v[1]) + (v[2] + v[3])) + ((v[4] + v[5]) + (v[6] + v[7]))) + (v[8] + v[9]);
  const float r = __builtin_amdgcn_rcpf(s[0] + s[1]);
#pragma unroll
  for (int i = 0; i < 10; ++i) { v[i] *= (f32x2){r, r}; ph[i] = pk2(v[i][0], v[i][1]); }
}

constexpr float SC2 = 0.22360679774997896f * 1.4426950408889634f;

DI unsigned o_off(int h, int srow) { return (unsigned)((srow >> 4) * 20480 + (srow & 15) * 16 + 5 * (h >> 3) * 1024 + ((h >> 1) & 3) * 256); }
DI void store_o(rsrc_t ro, unsigned v16, unsigned v8, const float (&o)[12]) {
  st16c(ro, v16, (u32x4){pk2(o[0], o[1]), pk2(o[2], o[3]), pk2(o[4], o[5]), pk2(o[6], o[7])});
  st8c(ro, v8, (u32x2){pk2(o[8], o[9]), pk2(o[10], o[11])});
}
#define PIN(x) asm volatile("" : "+v"(x))
DI void wait_flags(rsrc_t rin, unsigned voff, bool need, u32x2 tag) {
#pragma unroll 1
  for (int spin = 0; spin < (1 << 20); ++spin) {
    u32x2 f;
    asm volatile("buffer_load_dwordx2 %0, %1, %2, 0 offen sc1\n\ts_waitcnt vmcnt(0)" : "=&v"(f) : "v"(voff), "s"(rin) : "memory");
    if (__all(!need || (f[0] == tag[0] && f[1] == tag[1]))) break;
    __builtin_amdgcn_s_sleep(1);
  }
}

constexpr unsigned CODE_BYTES = 24 * 1024;
constexpr int G_STAGE = 16384 + 30720;
constexpr int KP = 1024, KH = 5 * KP, VP = 1056, VH = 5376, SEG_V = 4 * KH, SEG = SEG_V + 4 * VH;
constexpr int L_LOC = 512, L_Q = L_LOC + SEG, L_NEAR = L_Q + 20480, L_FAR = L_NEAR + SEG;
constexpr int A_PHI = L_FAR, A_KV = L_FAR;
constexpr int L_ONE = L_FAR + 8 * 5632 + 160, L_W = L_ONE + 1024 + 96, L_G = L_W + 4096, L_T = L_G + 1024, L_FB = L_T + 256, L_S = L_FB + 16, K_LDS = L_S + 1024;
constexpr int P_W = 1536, P_X = L_FAR;
static_assert(3 * G_STAGE + 64 <= L_W && K_LDS <= 160 * 1024 - 2048 && P_W + 20 * 5120 + 64 <= L_FAR && P_W + 8 * 5120 == L_Q && P_W + 12 * 5120 == L_NEAR, "LDS map");
static_assert(L_LOC % 256 == 0 && SEG % 256 == 0 && SEG_V % 256 == 0 && VH % 256 == 0 && L_NEAR % 256 == 0 && L_ONE % 256 == 160 && L_W % 16 == 0, "banks");

DI f32x16 qk_tile(const LAS char* kh, const LAS char* k4, bf16x8 q0, u32x2 q1, f32x16 c) {
  const u32x2 a = lds8(kh), b = lds8(kh + KP), d = lds8(k4);
  c = MFMA32(frag2(a, b), q0, c);
  return MFMA32K8(d, q1, c);
}
DI f32x16 pv_tile(const LAS char* vp, const f32x16& s, f32x16 o) {
  const bf16x8 v0 = cat8(trread(vp), trread(vp + 64)), v1 = cat8(trread(vp + 128), trread(vp + 192));
  unsigned pw[8];
#pragma unroll
  for (int i = 0; i < 8; ++i) pw[i] = pk2(__builtin_amdgcn_exp2f(s[2 * i]), __builtin_amdgcn_exp2f(s[2 * i + 1]));
  o = MFMA32(v0, asfrag((u32x4){pw[0], pw[1], pw[2], pw[3]}), o);
  return MFMA32(v1, asfrag((u32x4){pw[4], pw[5], pw[6], pw[7]}), o);
}
struct Strm { const LAS char* kh; const LAS char* k4; bf16x8 q0; u32x2 q1; };
struct KFr { u32x2 a, b, d; };
DI KFr kread(const Strm& s, int off) { KFr k; k.a = lds8(s.kh + off); k.b = lds8(s.kh + off + KP); k.d = lds8(s.k4 + off); return k; }
DI void tile_step(f32x16& o, const f32x16& sc, const LAS char* vp, f32x16& sn, const Strm& n, KFr& k, const Strm& ks, int koff) {
  const f32x16 z = {};
  const bf16x8 v0 = cat8(trread(vp), trread(vp + 64)), v1 = cat8(trread(vp + 128), trread(vp + 192));
  float e[16];
#pragma unroll
  for (int i = 0; i < 3; ++i) e[i] = __builtin_amdgcn_exp2f(sc[i]);
  asm volatile("" : "+v"(e[0]), "+v"(e[1]), "+v"(e[2]));
  SB();
  sn = MFMA32(frag2(k.a, k.b), n.q0, z);
#pragma unroll
  for (int i = 3; i < 7; ++i) e[i] = __builtin_amdgcn_exp2f(sc[i]);
  asm volatile("" : "+v"(e[3]), "+v"(e[4]), "+v"(e[5]), "+v"(e[6]));
  SB();
  sn = MFMA32K8(k.d, n.q1, sn);
  k = kread(ks, koff);
#pragma unroll
  for (int i = 7; i < 10; ++i) e[i] = __builtin_amdgcn_exp2f(sc[i]);
  u32x4 p0 = {pk2(e[0], e[1]), pk2(e[2], e[3]), pk2(e[4], e[5]), pk2(e[6], e[7])};
  asm volatile("" : "+v"(e[8]), "+v"(e[9]), "+v"(p0));
  SB();
  o = MFMA32(v0, asfrag(p0), o);
#pragma unroll
  for (int i = 10; i < 16; ++i) e[i] = __builtin_amdgcn_exp2f(sc[i]);
  o = MFMA32(v1, asfrag((u32x4){pk2(e[8], e[9]), pk2(e[10], e[11]), pk2(e[12], e[13]), pk2(e[14], e[15])}), o);
  SB();
}
struct Pipe { f32x16 s; KFr k; };
DI Pipe prime(const Strm& s, const f32x16& c0, int off, int step) {
  Pipe p;
  p.k = kread(s, off + step);
  p.s = qk_tile(s.kh + off, s.k4 + off, s.q0, s.q1, c0);
  return p;
}
DI void run_tiles(f32x16& o, const Strm& s, Pipe& p, const LAS char* vp, int off, int step, int n) {
  f32x16 sb;
#pragma unroll 1
  for (;;) {
    tile_step(o, p.s, vp, sb, s, p.k, s, off + 2 * step);
    if (n == 1) break;
    tile_step(o, sb, vp + step, p.s, s, p.k, s, off + 3 * step);
    if (n == 2) break;
    n -= 2; off += 2 * step; vp += 2 * step;
  }
}

__global__ __launch_bounds__(512, 2) void k1_fused(const __hip_bfloat16* __restrict__ xA, const __hip_bfloat16* __restrict__ wA,
                                                   const __hip_bfloat16* __restrict__ xB, const __hip_bfloat16* __restrict__ wB,
                                                   const __hip_bfloat16* __restrict__ wphi, const __hip_bfloat16* __restrict__ woA,
                                                   const __hip_bfloat16* __restrict__ woB, char* __restrict__ ws,
                                                   __hip_bfloat16* __restrict__ out) {
  __shared__ __attribute__((aligned(16))) char smem[K_LDS];
  const LAS char* sm = (const LAS char*)smem;
  LAS char* smw = (LAS char*)smem;
  const unsigned lds0 = (unsigned)(size_t)(LAS char*)smem;
  const int tid = threadIdx.x, lane = tid & 63;
  const int wid = __builtin_amdgcn_readfirstlane(tid >> 6);
  const int xcd = blockIdx.x & 7, idx = blockIdx.x >> 3;
  const int br = idx & 1, sub = idx >> 1;
  const int mt = (xcd >> 2) * 8 + (sub & 7), nt = (xcd & 3) * 2 + (sub >> 3);
  const int mt3 = xcd * 2 + (sub & 1), nt3 = sub >> 1;
  const int me = blockIdx.x;
  const int g = lane >> 4, fr = lane & 15, q4 = fr >> 2, p4 = lane & 3;
  STAMP_DECL;
  STAMP(0, 0);
  {
    unsigned long long pc;
    asm volatile("s_getpc_b64 %0" : "=s"(pc));
    if (wid == 0) touch(mkrsrc((const void*)pc, CODE_BYTES), lds0 + L_T, lane < 6 ? (unsigned)((lane * 32 + idx) * 128) : OOB, 0);
  }
  const char* wg = (const char*)(br ? wB : wA);
  const rsrc_t rx = mkrsrc((const char*)(br ? xB : xA) + (size_t)mt * 128 * 1280, 128 * 1280);
  const rsrc_t rw = mkrsrc(wg + nt * 160, 640 * 3840 - nt * 160);
  const unsigned vx0 = (unsigned)((2 * (lane >> 3)) * 1280 + (((lane & 7) ^ g) * 16));
  const unsigned vx1 = (unsigned)((16 + 2 * (lane >> 3)) * 1280 + (((lane & 7) ^ (4 + g)) * 16));
  const int wr = lane >= 30 ? 1 : 0, wc = lane - 30 * wr, wsg = (wc >= 10) + (wc >= 20);
  const unsigned vw = (unsigned)(wr * 3840 + wsg * 1280 + (wc - 10 * wsg) * 16);
  const unsigned sx = (unsigned)((32 * (wid >> 1) + (wid & 1)) * 1280);
  const unsigned sw = (unsigned)((16 * (wid >> 1) + 4 * (wid & 1)) * 3840);
  const unsigned lx = lds0 + wid * 2048, lw = lds0 + 16384 + wid * 3840;
#define G_DMA(i, bo, xo, wo) do { \
    if ((i) == 0 || (i) == 3) dma16(rx, lx + (bo) + ((i) ? 1024 : 0), (i) ? vx1 : vx0, (xo)); \
    else { const int n_ = (i) < 3 ? (i) - 1 : (i) - 2; \
      dma16m<0xFFFFFFFFu, 0x0FFFFFFFu>(rw, lw + (bo) + n_ * 960, vw, (wo) + ((n_ >> 1) * 8 + (n_ & 1) * 2) * 3840); } } while (0)
#pragma unroll
  for (int i = 0; i < 6; ++i) G_DMA(i, 0, sx, sw);
  const int bi = mt * 8 + nt;
  const rsrc_t rt = (wid & 4) ? mkrsrc(wg, 640 * 3840) : mkrsrc(br ? xB : xA, SEQ * 1280);
  const unsigned tst = (wid & 4) ? 245760u : 128u;
  unsigned vt = (wid & 4) ? (fr < 15 ? (unsigned)((bi * 15 + fr) * 128 + g * 245760) : OOB) : (unsigned)((bi * 16 + fr) * 1280 + g * 128);
  if ((wid & 3) == 2) vt = g == 0 ? vt + 4 * tst : OOB;
  if ((wid & 3) >= 2) touch(rt, lds0 + L_T, vt, tst);
  const rsrc_t rt2 = (wid & 4) ? mkrsrc((wid & 1) ? wA : wB, 640 * 3840) : mkrsrc((wid & 1) ? xA : xB, SEQ * 1280);
  vt = (wid & 4) ? (lane < 60 ? (unsigned)(((bi >> 2) * 60 + lane) * 128) : OOB) : (unsigned)(((bi >> 2) * 64 + lane) * 1280);

  const int wm = wid >> 2, wn = wid & 3;
  const LAS char* xa0 = sm + (64 * wm + fr) * 128 + ((g ^ (fr >> 1)) * 16);
  const LAS char* xa1 = sm + (((64 * wm + fr) * 128 + ((g ^ (fr >> 1)) * 16)) ^ 64);
  const LAS char* wa = sm + 16384 + (16 * (g >> 1) + 4 * (g & 1) + q4) * 480 + 8 * p4 + wn * 128;
  f32x4 acc[4][4] = {};
#pragma unroll
  for (int i = 0; i < 4; ++i) asm volatile("" : "+v"(acc[i][0]), "+v"(acc[i][1]), "+v"(acc[i][2]), "+v"(acc[i][3]));
  int eb[4];
#pragma unroll
  for (int j = 0; j < 4; ++j) {
    const int qd = 16 * wn + 4 * j + g, r = qd - 40;
    const int v = L_LOC + SEG_V + r * VP + ((r * 13) >> 6) * 96;
    eb[j] = (qd < 20 ? L_Q + qd * 1024 : qd < 40 ? L_LOC + (qd - 20) * KP : qd < 60 ? v : L_S) + wm * 512 + fr * 16;
    asm volatile("" : "+v"(eb[j]));
  }
  int cons;
  {
    const int cm = br ? mt + 1 + lane : lane;
    const int c1 = (((((nt & 1) * 8 + (cm & 7)) << 1) | br) << 3) | (((cm >> 3) & 1) * 4 + (nt >> 1));
    const int c2 = (((((lane & 7) * 2 + (mt & 1)) << 1) | br) << 3) | (mt >> 1);
    cons = lane < 16 ? ((cm < 16 && (!br || lane < 2)) ? c1 : -1) : (lane < 24 ? c2 : lane == 63 ? me : -1);
    asm volatile("" : "+v"(cons));
  }
  const int hh = lane >> 5, r32 = lane & 31, b16 = (lane >> 4) & 1, vq = 4 * b16 + p4;
  unsigned vflag = (unsigned)(WS_IN + cons * 256 + (lane >= 16 ? 16 + nt : br ? 1 - lane : mt) * 8);
  unsigned vf1 = (lane < 16 && cons >= 0) ? vflag : OOB, vf2 = (lane >= 16 && lane < 24) ? vflag : OOB;
  const int lq = (lane >= 10) + (lane >= 20) + (lane >= 30);
  unsigned vwo = (unsigned)(lq * 1280 + (lane - 10 * lq) * 16);
  PIN(vf1); PIN(vf2); PIN(vwo);
  const int hs = wid >> 2, qi = hs ? 3 - (wid & 3) : (wid & 3);
  const int hl = wid >> 1, rw0 = 64 * (wid & 1);
  int c0, c1, c2, c3, c4, c5, c6;
  unsigned v16, v8;
  if (br) {
    c0 = hs * KH + hh * 2 * KP + r32 * 8;
    c1 = hs * KH + 4 * KP + r32 * 8;
    c2 = (vq < 5 ? SEG_V + hs * VH + vq * VP : L_ONE) + (4 * hh + q4) * 8;
    c3 = vq < 5 ? -1 : 0;
    c4 = L_Q + hs * 5120 + (32 * qi + r32) * 8;
    c5 = L_Q + (2 + hs) * 5120 + (32 * (3 - qi) + r32) * 8;
    c6 = hh * 2048;
    v16 = o_off(4 * nt + hs, mt * 128 + 32 * qi + r32);
  } else {
    c0 = L_W + hl * 800 + (8 * hh + q4) * 40 + (2 * b16 + (p4 >> 1)) * 8;
    c1 = L_W + hl * 800 + (8 * hh + q4) * 40 + 32;
    c2 = hl * 5120 + (rw0 + r32) * 8;
    c3 = A_PHI + wid * 5632 + r32 * 88 + hh * 40;
    c4 = A_PHI + wid * 5632 + (8 * hh + q4) * 88 + (16 * b16 + 4 * p4) * 2;
    c5 = (vq < 5 ? L_LOC + SEG_V + hl * VH + vq * VP : L_ONE) + (rw0 + 8 * hh + q4) * 8;
    c6 = A_PHI + wid * 5632 + min(r32, 21) * 160 + hh * 16;
    v16 = o_off(4 * nt + hl, mt * 128 + rw0 + r32);
  }
  {
    const int odd = br ? hs : (hl & 1);
    v8 = hh ? OOB : (v16 & ~1023u) + 2048 + 512 * odd + ((v16 & 1023u) >> 1);
    v16 += (hh + 3 * odd) * 1024;
  }
  PIN(c0); PIN(c1); PIN(c2); PIN(c3); PIN(c4); PIN(c5); PIN(c6); PIN(v16); PIN(v8);
  f32x16 mle, mgt;
#pragma unroll
  for (int e = 0; e < 16; ++e) {
    const bool le = (e & 3) + 8 * (e >> 2) + 4 * hh <= r32;
    mle[e] = le ? 0.f : -INFINITY;
    mgt[e] = le ? -INFINITY : 0.f;
  }
  PIN(mle); PIN(mgt);
  const rsrc_t rg = mkrsrc(ws + WS_G, WS_KVH);
  const rsrc_t rph = mkrsrc((const char*)wphi + nt * 3200, 3200);
  bf16x8 x0[4], x1[4];
  s16x4 w0[4][2], w1[4][2];
#define G_RD(X, W, xa, kk, r) do { \
    if ((r) < 2) W[0][(r)] = trread(wa + ((kk) * 32 + (r) * 8) * 480); \
    else if ((r) < 6) X[(r) - 2] = *(const LAS bf16x8*)(xa + ((r) - 2) * 2048); \
    else if ((r) < 12) W[((r) - 4) >> 1][(r) & 1] = trread(wa + ((kk) * 32 + ((r) & 1) * 8) * 480 + (((r) - 4) >> 1) * 32); } while (0)
#define G_MM(X, W, m) acc[(m) & 3][(m) >> 2] = MFMA16(cat8(W[(m) >> 2][0], W[(m) >> 2][1]), X[(m) & 3], acc[(m) & 3][(m) >> 2])
  if (wid == 0) dma16(rg, lds0 + L_G, cons >= 0 ? cons * 16 : OOB, 0);
  if (wid == 4) touchc(rg, lds0 + L_T, lane < 2 ? (unsigned)(WS_IN + me * 256 + lane * 128) : OOB);
  asm volatile("s_barrier" ::: "memory");
#pragma unroll
  for (int i = 0; i < 6; ++i) G_DMA(i, G_STAGE, sx + 128, sw + 245760);
  if ((wid & 3) != 1) { WAIT_BAR(7); } else { WAIT_BAR(6); }
  STAMP(0, 1);
#pragma unroll
  for (int r = 0; r < 12; ++r) G_RD(x0, w0, xa0, 0, r);
  int bo = 2 * G_STAGE;
  int bd = bo;
  unsigned xo = sx + 256, wo = sw + 2 * 245760;
#pragma unroll
  for (int i = 0; i < 3; ++i) G_DMA(i, bo, xo, wo);
#pragma unroll 1
  for (int t = 0; t < 10; ++t) {
    SB();
#pragma unroll
    for (int m = 0; m < 12; ++m) {
      G_MM(x0, w0, m); G_RD(x1, w1, xa1, 1, m);
      if (m % 3 == 2 && m < 9) G_DMA(3 + m / 3, bd, xo, wo);
      SB();
    }
    if (wn < 3) { G_MM(x0, w0, 12); G_MM(x0, w0, 13); G_MM(x0, w0, 14); G_MM(x0, w0, 15); }
    const int d = bo == G_STAGE ? -2 * G_STAGE : G_STAGE;
    xa0 += d; xa1 += d; wa += d;
    bo = bo == 2 * G_STAGE ? 0 : bo + G_STAGE;
    bd = t < 7 ? bo : 0x30000;
    xo = t < 7 ? xo + 128 : OOB;
    wo += 245760;
    WAIT_BAR(6);
    SB();
#pragma unroll
    for (int m = 0; m < 12; ++m) {
      G_MM(x1, w1, m); G_RD(x0, w0, xa0, 0, m);
      if (m % 3 == 2 && m < 9) G_DMA(m / 3, bd, xo, wo);
      SB();
    }
    if (wn < 3) { G_MM(x1, w1, 12); G_MM(x1, w1, 13); G_MM(x1, w1, 14); G_MM(x1, w1, 15); }
    if (t == (bi & 3) + TB && !br) {
      if ((wid & 3) >= 2) touch(rt2, lds0 + L_T, vt, (6 - TB + t) * tst);
      if ((wid & 3) == 1) touch(mkrsrc(wid == 1 ? woA : woB, 640 * 1280), lds0 + L_T, lane < 50 ? (unsigned)((bi * 50 + lane) * 128) : OOB, 0);
      if (wid == 4) {
#pragma unroll
        for (int i = 0; i < 4; ++i) dma16(rph, lds0 + L_W + i * 1024, (unsigned)lane * 16, i * 1024);
      }
    }
  }
  STAMP(0, 2);
  {
    const rsrc_t rh = mkrsrc(ws + WS_KVH + (mt * 8 + nt) * 40960, 40960);
    const int v0 = (16 * wn + g - 20) * 1024 + wm * 512 + fr * 16;
#pragma unroll
    for (int j = 3; j >= 0; --j) {
      if (br && 4 * wn + j < 5) {
#pragma unroll
        for (int i = 0; i < 4; ++i) acc[i][j] *= SC2;
      }
#pragma unroll
      for (int i = 0; i < 2; ++i) {
        const f32x4 a = acc[2 * i][j], b = acc[2 * i + 1][j];
        const u32x4 ev = (u32x4){pk2(a[0], a[1]), pk2(a[2], a[3]), pk2(b[0], b[1]), pk2(b[2], b[3])};
        if (br && 4 * wn + j >= 5) st16c(rh, (unsigned)(v0 + j * 4096 + i * 256), ev);
        *(LAS u32x4*)(smw + eb[j] + i * 256) = ev;
      }
    }
  }
  if (tid < 128) *(LAS u32x2*)(smw + L_ONE + tid * 8) = (u32x2){0x3F80u, 0u};
  if (tid == 511) *(LAS unsigned*)(smw + L_FB) = 0u;
  const u32x2 gc = next_tag(*(const LAS u32x2*)(sm + L_G + lane * 16));
  const u32x2 tag = {(unsigned)__builtin_amdgcn_readlane(gc[0], 63), (unsigned)__builtin_amdgcn_readlane(gc[1], 63)};
  const char* wog = (const char*)(br ? woB : woA);
  const rsrc_t ro = mkrsrc(ws + WS_O + br * (SEQ * DM * 2), SEQ * DM * 2);
  const rsrc_t rwo = mkrsrc(wog + nt3 * 160, 640 * 1280 - nt3 * 160);
  const int sg = 2 * (wid >> 2) + (wid & 1), st = (wid >> 1) & 1;
#define P_DMA2(w5, G) dma16m<0xFFFFFFFFu, 0xFFu>(rwo, lds0 + P_W + (4 * (w5) + (G)) * 5120 + wid * 640, vwo, \
      (unsigned)(160 * (G) + 4 * (w5) + ((w5) >= 2 ? 8 : 0) + 40 * sg + st * ((w5) == 2 ? 20 : 8)) * 1280u)
#define P_DMA(b) P_DMA2((b) >> 2, (b) & 3)
  if (br) {
    WAIT_BAR(0);
    if (wid == 0) st8c(rg, vf1, gc);
    STAMP(0, 3);
    Strm S[2];
#pragma unroll
    for (int r = 0; r < 2; ++r) {
      S[r].kh = sm + c0 + r * 2 * KH;
      S[r].k4 = sm + c1 + r * 2 * KH;
      const LAS char* qp = sm + (r ? c5 : c4);
      S[r].q0 = frag2(lds8(qp + c6), lds8(qp + c6 + 1024));
      const u32x2 t4 = lds8(qp + 4096);
      S[r].q1 = hh ? (u32x2){0u, 0u} : t4;
    }
#define VPTR(r, seg) (sm + c2 + (((seg) + (r) * 2 * VH) & c3))
#define HALO(tile, seg, pg) do { \
      const rsrc_t rn_ = mkrsrc(ws + WS_KVH + (((tile) >= 0 ? (tile) : 0) * 8 + nt) * 40960 + (pg) * 10240, (tile) >= 0 ? 10240 : 0); \
      const unsigned hd_ = lds0 + (seg) + ((pg) >= 2 ? SEG_V + ((pg) & 1) * 2 * VH : ((pg) & 1) * 2 * KH), hp_ = (pg) >= 2 ? VP : KP; \
      _Pragma("unroll") for (int i = 0; i < 10; ++i) dma16c(rn_, hd_ + i * hp_ + (i >= 5 ? ((pg) >> 1) * 96 : 0), lane * 16, i * 1024); } while (0)
    f32x16 o0 = {}, o1 = {};
    Pipe p0 = prime(S[0], mle, L_LOC + qi * 256, -256), p1 = prime(S[1], mle, L_LOC + (3 - qi) * 256, -256);
    LBAR();
#pragma unroll
    for (int G = 0; G < 4; ++G) P_DMA2(2, G);
    run_tiles(o0, S[0], p0, VPTR(0, L_LOC) + qi * 256, L_LOC + qi * 256, -256, qi + 1);
    STAMP(0, 4);
    const int nb = qi < 2 ? 1 : 0, pg = 2 * hs + (qi & 1);
    if (qi == 0) {
      wait_flags(rg, (unsigned)(WS_IN + me * 256 + 8), mt >= 1, tag);
      HALO(mt - 1, L_NEAR, 2 * hs);
      HALO(mt - 1, L_NEAR, 2 * hs + 1);
    }
    STAMP(0, 5);
    run_tiles(o1, S[1], p1, VPTR(1, L_LOC) + (3 - qi) * 256, L_LOC + (3 - qi) * 256, -256, 4 - qi);
    STAMP(0, 6);
    if (nb) {
      WAIT_BAR(0);
    } else {
      wait_flags(rg, (unsigned)(WS_IN + me * 256), mt >= 2, tag);
      HALO(mt - 2, L_FAR, pg);
      LBAR();
    }
    STAMP(0, 7);
    if (mt > 0) {
      const f32x16 z = {};
      KFr k = kread(S[1], L_NEAR);
      f32x16 sa = qk_tile(S[0].kh + L_NEAR, S[0].k4 + L_NEAR, S[0].q0, S[0].q1, z), sb;
      const LAS char* v0 = VPTR(0, L_NEAR);
      const LAS char* v1 = VPTR(1, L_NEAR);
#pragma unroll 1
      for (int off = 0; off < 1024; off += 256) {
        P_DMA2(0, off >> 8);
        tile_step(o0, sa, v0 + off, sb, S[1], k, S[0], L_NEAR + off + 256);
        P_DMA2(1, off >> 8);
        tile_step(o1, sb, v1 + off, sa, S[0], k, S[1], L_NEAR + off + 256);
        if (off == 512) { WAIT_BAR(6); }
      }
    } else {
#pragma unroll
      for (int ks = 0; ks < 8; ++ks) P_DMA(ks);
    }
    p0 = prime(S[0], mgt, L_FAR + qi * 256, 256);
    p1 = prime(S[1], mgt, L_FAR + (3 - qi) * 256, 256);
    STAMP(0, 8);
    LBAR();
    bool redo[2];
#pragma unroll
    for (int r = 0; r < 2; ++r) {
      const int qt = r ? 3 - qi : qi;
      f32x16& o = r ? o1 : o0;
#pragma unroll
      for (int G = 0; G < 4; ++G) P_DMA2(3 + r, G);
      if (mt > 1) run_tiles(o, S[r], r ? p1 : p0, VPTR(r, L_FAR) + qt * 256, L_FAR + qt * 256, 256, 4 - qt);
      const float l = __uint_as_float(__builtin_amdgcn_permlane32_swap(__float_as_uint(o[8]), __float_as_uint(o[8]), false, false)[1]);
      const float rl = __builtin_amdgcn_rcpf(l);
      float of[12];
#pragma unroll
      for (int i = 0; i < 12; ++i) of[i] = o[i] * rl;
      const unsigned dv = r ? (unsigned)((3 - 2 * qi) * 40960 + 256) : 0u;
      store_o(ro, v16 + dv, hh ? OOB : v8 + dv - (r ? 128 : 0), of);
      float am = __builtin_fabsf(o[0]);
#pragma unroll
      for (int i = 1; i < 11; i += 2) am = fmax3(am, __builtin_fabsf(o[i]), __builtin_fabsf(o[i + 1]));
      am = __builtin_elementwise_maximum(am, __builtin_fabsf(o[11]));
      redo[r] = !__all(l >= 0x1p-80f && l <= FB_HI && am <= 0x1p126f);
    }
    if (redo[0] || redo[1]) *(LAS unsigned*)(smw + L_FB) = 1u;
    STAMP(0, 9);
    WAIT_BAR(0);
    STAMP(0, 10);
    if (__builtin_expect(__builtin_amdgcn_readfirstlane(*(const volatile LAS unsigned*)(sm + L_FB)) != 0u, 0)) {
      HALO(mt - nb, nb ? L_NEAR : L_LOC, pg);
      WAIT_BAR(0);
#pragma unroll
      for (int r = 0; r < 2; ++r) {
        if (!redo[r]) continue;
        const int qt = r ? 3 - qi : qi, w0 = mt >= 2 ? qt : mt == 1 ? 4 : 8;
        f32x16 o;
        float m = -INFINITY;
#pragma unroll 1
        for (int pass = 0; pass < 2; ++pass) {
          o = (f32x16){};
#pragma unroll 1
          for (int w = w0; w <= qt + 8; ++w) {
            const int seg = w < 4 ? L_FAR : w < 8 ? L_NEAR : L_LOC, off = (w & 3) * 256;
            f32x16 c;
#pragma unroll
            for (int e = 0; e < 16; ++e) c[e] = w == qt + 8 ? mle[e] : w == qt ? mgt[e] : 0.f;
            f32x16 s = qk_tile(S[r].kh + seg + off, S[r].k4 + seg + off, S[r].q0, S[r].q1, c);
            if (pass == 0) {
#pragma unroll
              for (int e = 0; e < 16; e += 2) m = fmax3(m, s[e], s[e + 1]);
            } else {
#pragma unroll
              for (int e = 0; e < 16; ++e) s[e] -= m;
              o = pv_tile(VPTR(r, seg) + off, s, o);
            }
          }
          auto rr = __builtin_amdgcn_permlane32_swap(__float_as_uint(m), __float_as_uint(m), false, false);
          m = __builtin_elementwise_maximum(__uint_as_float(rr[0]), __uint_as_float(rr[1]));
        }
        const float rl = __builtin_amdgcn_rcpf(__uint_as_float(__builtin_amdgcn_permlane32_swap(__float_as_uint(o[8]), __float_as_uint(o[8]), false, false)[1]));
        float of[12];
#pragma unroll
        for (int i = 0; i < 12; ++i) of[i] = o[i] * rl;
        const unsigned dv = r ? (unsigned)((3 - 2 * qi) * 40960 + 256) : 0u;
        store_o(ro, v16 + dv, hh ? OOB : v8 + dv - (r ? 128 : 0), of);
      }
      LBAR();
#pragma unroll 1
      for (int G = 0; G < 4; ++G) {
#pragma unroll
        for (int u = 0; u < 5; ++u) P_DMA2(u, G);
      }
      WAIT_BAR(0);
    }
  } else {
    WFrag wf = load_wfrag(sm + c0, sm + c1, hh, lane);
    PIN(wf.t0k0); PIN(wf.t1k0); PIN(wf.t0k1); PIN(wf.t1k1);
    LBAR();
    STAMP(0, 3);
    const LAS char* xp = sm + c2 + hh * 2048;
    const LAS char* x4 = sm + c2 + 4096;
    const HRow rk0 = hh_read(xp + L_LOC, x4 + L_LOC), rk1 = hh_read(xp + L_LOC + 256, x4 + L_LOC + 256);
    const HRow rq0 = hh_read(xp + L_Q, x4 + L_Q), rq1 = hh_read(xp + L_Q + 256, x4 + L_Q + 256);
    const HProj hk0 = hh_proj(wf, rk0);
    bf16x8 vf[4];
#pragma unroll
    for (int ks = 0; ks < 4; ++ks) vf[ks] = cat8(trread(sm + c5 + ks * 128), trread(sm + c5 + ks * 128 + 32));
#pragma unroll
    for (int ks = 12; ks < 18; ++ks) P_DMA(ks);
    SB();
    f32x16 d0 = {}, d1 = {};
    HProj hq0, hq1;
    {
      unsigned ph[10];
      HProj hk1 = hh_proj(wf, rk1);
      hh_soft(hk0, ph);
#pragma unroll
      for (int i = 0; i < 5; ++i) *(LAS u32x2*)(smw + c3 + 8 * i) = (u32x2){ph[2 * i], ph[2 * i + 1]};
      PIN(hk1.p0); PIN(hk1.p1);
      mix_mfma<4, 8>();
      SB();
      bf16x8 pf[4];
#pragma unroll
      for (int i = 0; i < 4; ++i) pf[i] = cat8(trread(sm + c4 + (i >> 1) * 16 * 88 + (i & 1) * 64), trread(sm + c4 + ((i >> 1) * 16 + 4) * 88 + (i & 1) * 64));
      P_DMA(18); P_DMA(19);
      SB();
      hq0 = hh_proj(wf, rq0);
      d0 = MFMA32(pf[0], vf[0], d0); d1 = MFMA32(pf[1], vf[0], d1);
      d0 = MFMA32(pf[2], vf[1], d0); d1 = MFMA32(pf[3], vf[1], d1);
      hh_soft(hk1, ph);
#pragma unroll
      for (int i = 0; i < 5; ++i) *(LAS u32x2*)(smw + c3 + 32 * 88 + 8 * i) = (u32x2){ph[2 * i], ph[2 * i + 1]};
      PIN(hq0.p0); PIN(hq0.p1); PIN(d0); PIN(d1);
      mix_mfma<8, 8>();
      SB();
#pragma unroll
      for (int i = 0; i < 4; ++i) pf[i] = cat8(trread(sm + c4 + (32 + (i >> 1) * 16) * 88 + (i & 1) * 64), trread(sm + c4 + (36 + (i >> 1) * 16) * 88 + (i & 1) * 64));
      d0 = MFMA32(pf[0], vf[2], d0); d1 = MFMA32(pf[1], vf[2], d1);
      d0 = MFMA32(pf[2], vf[3], d0); d1 = MFMA32(pf[3], vf[3], d1);
    }
    *(LAS f32x4*)(smw + c6) = (f32x4){d0[0], d0[1], d0[2], d0[3]};
    *(LAS f32x4*)(smw + c6 + 32) = (f32x4){d0[4], d0[5], d0[6], d0[7]};
    *(LAS f32x4*)(smw + c6 + 64) = (f32x4){d0[8], d0[9], d0[10], d0[11]};
    *(LAS f32x4*)(smw + c6 + 96) = (f32x4){d0[12], d0[13], d0[14], d0[15]};
    *(LAS f32x4*)(smw + c6 + 128) = (f32x4){d1[0], d1[1], d1[2], d1[3]};
    SB();
    hq1 = hh_proj(wf, rq1);
    PIN(hq1.p0); PIN(hq1.p1);
    LBAR();
    STAMP(0, 4);
    const rsrc_t rkv = mkrsrc(ws + WS_KVP + nt * 16 * 7168, 16 * 7168);
    {
      const int e = (wid & 1) * 64 + lane;
      const LAS char* o = sm + A_PHI + (wid & ~1) * 5632 + e * 32;
      const f32x4 a = *(const LAS f32x4*)o + *(const LAS f32x4*)(o + 5632), b = *(const LAS f32x4*)(o + 16) + *(const LAS f32x4*)(o + 5648);
      st16c(rkv, e < 112 ? (unsigned)(mt * 7168 + hl * 1792 + e * 16) : OOB, (u32x4){pk2(a[0], a[1]), pk2(a[2], a[3]), pk2(b[0], b[1]), pk2(b[2], b[3])});
    }
#pragma unroll
    for (int ks = 0; ks < 6; ++ks) P_DMA(ks);
    SB();
    unsigned phq[2][10];
    hh_soft(hq0, phq[0]);
#pragma unroll
    for (int i = 0; i < 10; ++i) PIN(phq[0][i]);
    WAIT_BAR(6);
    if (wid == 0) st8c(rg, vf1, gc);
    if (tid >= 256) *(LAS u32x2*)(smw + A_KV + ((tid - 256) >> 1) * 112 + (tid & 1) * 48 + 40) = (u32x2){0u, 0u};
    STAMP(0, 5);
    hh_soft(hq1, phq[1]);
#pragma unroll
    for (int i = 0; i < 10; ++i) PIN(phq[1][i]);
    int kd0, kd1;
    {
      const int h4 = tid / 112, e = tid - h4 * 112, d = e / 5, oc = e - d * 5;
      kd0 = e < 105 ? A_KV + h4 * 3584 + d * 112 + oc * 16 + (oc > 2 ? 8 : 0) : L_S;
      kd1 = kd0 + (oc == 2 ? 16 : 8);
      PIN(kd0); PIN(kd1);
    }
    wait_flags(rg, (unsigned)(WS_IN + me * 256 + fr * 8), true, tag);
    STAMP(0, 6);
    if (wid < 7) {
      u32x4 p[16];
#pragma unroll
      for (int i = 0; i < 16; ++i) p[i] = __builtin_amdgcn_raw_buffer_load_b128(rkv, tid * 16, i * 7168, 16);
      f32x2 lo[2] = {}, hi[2] = {};
#pragma unroll
      for (int i = 0; i < 16; ++i)
#pragma unroll
        for (int u = 0; u < 2; ++u) {
          lo[u] += (f32x2){__uint_as_float(p[i][2 * u] << 16), __uint_as_float(p[i][2 * u + 1] << 16)};
          hi[u] += (f32x2){__uint_as_float(p[i][2 * u] & 0xFFFF0000u), __uint_as_float(p[i][2 * u + 1] & 0xFFFF0000u)};
        }
      *(LAS u32x2*)(smw + kd0) = (u32x2){pk2(lo[0][0], hi[0][0]), pk2(lo[0][1], hi[0][1])};
      *(LAS u32x2*)(smw + kd1) = (u32x2){pk2(lo[1][0], hi[1][0]), pk2(lo[1][1], hi[1][1])};
    }
#pragma unroll
    for (int ks = 6; ks < 12; ++ks) P_DMA(ks);
    LBAR();
    STAMP(0, 7);
    const LAS char* kp = sm + A_KV + hl * 3584 + (r32 == 24 ? 20 : r32) * 112 + hh * 48;
    const bf16x8 f0 = *(const LAS bf16x8*)(kp), f1 = *(const LAS bf16x8*)(kp + 16), f2 = *(const LAS bf16x8*)(kp + 32);
    f32x16 n[2];
#pragma unroll
    for (int t = 0; t < 2; ++t) n[t] = MFMA32(f0, asfrag((u32x4){phq[t][0], phq[t][1], phq[t][2], phq[t][3]}), (f32x16){});
#pragma unroll
    for (int t = 0; t < 2; ++t) n[t] = MFMA32(f1, asfrag((u32x4){phq[t][4], phq[t][5], phq[t][6], phq[t][7]}), n[t]);
#pragma unroll
    for (int t = 0; t < 2; ++t) n[t] = MFMA32(f2, asfrag((u32x4){phq[t][8], phq[t][9], 0u, 0u}), n[t]);
#pragma unroll
    for (int t = 0; t < 2; ++t) {
      const float den = rbf(rbf(hh ? n[t][8] : n[t][12]) + 1e-6f);
      const float r = __builtin_amdgcn_rcpf(den);
      float o[12];
#pragma unroll
      for (int i = 0; i < 12; ++i) o[i] = rbf(n[t][i]) * r;
      store_o(ro, v16 + t * 40960, hh ? OOB : v8 + t * 40960, o);
    }
    STAMP(0, 9);
    WAIT_BAR(0);
    STAMP(0, 10);
  }
  {
    if (wid == 0) st8c(rg, vf2, gc);
    STAMP(0, 11);
    int oa[3];
    unsigned og[3];
#pragma unroll
    for (int i = 0; i < 3; ++i) {
      const int c = min(lane + 64 * i, 160), row = c / 10, ch = c - row * 10;
      oa[i] = wid * 2816 + row * 176 + ch * 16;
      og[i] = (unsigned)(row * 2560 + ch * 16);
      PIN(oa[i]); PIN(og[i]);
    }
    const int pm = wid & 3, kh = wid >> 2;
    wait_flags(rg, (unsigned)(WS_IN + me * 256 + 128 + (4 * kh + (lane & 3)) * 8), true, tag);
    STAMP(0, 12);
    const rsrc_t rf = mkrsrc(ws + WS_O + (unsigned)((br * 128 + mt3 * 8 + 2 * pm) * 20 + 10 * kh) * 1024u, 30 * 1024);
    const unsigned vk = lane * 16 + kh * 20480, vh = lane * 16 + (1 - kh) * 20480;
    u32x4 xf[10][2];
    const unsigned vk8 = lane * 8 + kh * 20480, vh8 = lane * 8 + (1 - kh) * 20480;
#define P_LD1(v, v8_, tau) ((tau) % 5 == 2 ? __builtin_bit_cast(u32x4, frag2(__builtin_amdgcn_raw_buffer_load_b64(rf, v8_, (tau) * 1024, 16), \
                                                                             __builtin_amdgcn_raw_buffer_load_b64(rf, v8_, (tau) * 1024 + 512, 16))) \
                                           : __builtin_amdgcn_raw_buffer_load_b128(rf, v, (tau) * 1024, 16))
#define P_LD(tau) do { xf[tau][0] = P_LD1(vk, vk8, tau); xf[tau][1] = P_LD1(vh, vh8, tau); } while (0)
    P_LD(0); P_LD(1); P_LD(2);
    const LAS char* wb = sm + P_W + kh * 10240 + (16 * (g >> 1) + 4 * (g & 1) + q4) * 160 + 8 * p4;
    const LAS char* wb2 = wb + 12 * 5120;
    f32x4 pc[2][5] = {};
    bf16x8 wf[2][5];
#pragma unroll
    for (int j = 0; j < 5; ++j) wf[0][j] = cat8(trread(wb + j * 32), trread(wb + 8 * 160 + j * 32));
#pragma unroll
    for (int tau = 0; tau < 10; ++tau) {
      SB();
      if (tau < 7) P_LD(tau + 3);
#pragma unroll
      for (int j = 0; j < 5; ++j) {
        if (tau < 9) {
          const int blk = ((tau + 1) % 5) * 4 + (tau + 1) / 5;
          const LAS char* wp = (blk >= 12 ? wb2 : wb) + (blk >= 12 ? blk - 12 : blk) * 5120 + j * 32;
          wf[(tau + 1) & 1][j] = cat8(trread(wp), trread(wp + 8 * 160));
        }
        pc[1][j] = MFMA16(wf[tau & 1][j], asfrag(xf[tau][1]), pc[1][j]);
        if (tau < 9) pc[0][j] = MFMA16(wf[tau & 1][j], asfrag(xf[tau][0]), pc[0][j]);
        SB();
      }
    }
#pragma unroll
    for (int j = 0; j < 5; ++j) {
      pc[0][j] = MFMA16(wf[1][j], asfrag(xf[9][0]), pc[0][j]);
      *(LAS f32x4*)(smw + P_X + (wid * 5 + j) * 1024 + lane * 16) = pc[1][j];
    }
    STAMP(0, 13);
    LBAR();
    if (tid == 0) st8c(rg, me * 16, tag);
    LAS char* ow = smw + wid * 2816 + fr * 176 + g * 8;
#pragma unroll
    for (int j = 0; j < 5; ++j) {
      const f32x4 a = pc[0][j] + *(const LAS f32x4*)(sm + P_X + ((wid ^ 4) * 5 + j) * 1024 + lane * 16);
      *(LAS u32x2*)(ow + j * 32) = (u32x2){pk2(a[0], a[1]), pk2(a[2], a[3])};
    }
    const rsrc_t rout = mkrsrc((char*)out + (size_t)(mt3 * 128 + 32 * pm + 16 * kh) * 2560 + br * 1280 + nt3 * 160, 15 * 2560 + 160);
#pragma unroll
    for (int i = 0; i < 3; ++i) __builtin_amdgcn_raw_buffer_store_b128(*(const LAS u32x4*)(sm + oa[i]), rout, og[i], 0, OUT_AUX);
  }
  STAMP(0, 15);
  STAMP_FLUSH(0);
}

extern "C" void kernel_launch(void* const* d_in, const int* in_sizes, int n_in, void* d_out, int out_size, void* d_ws,
                              size_t ws_size, hipStream_t stream) {
  k1_fused<<<256, 512, 0, stream>>>((const __hip_bfloat16*)d_in[0], (const __hip_bfloat16*)d_in[1], (const __hip_bfloat16*)d_in[4],
                                    (const __hip_bfloat16*)d_in[5], (const __hip_bfloat16*)d_in[2], (const __hip_bfloat16*)d_in[3],
                                    (const __hip_bfloat16*)d_in[6], (char*)d_ws, (__hip_bfloat16*)d_out);
}
```

```cpp
#include <hip/hip_runtime.h>
#include <hip/hip_bf16.h>

typedef short bf16x8 __attribute__((ext_vector_type(8)));
typedef short s16x4 __attribute__((ext_vector_type(4)));
typedef float f32x2 __attribute__((ext_vector_type(2)));
typedef float f32x4 __attribute__((ext_vector_type(4)));
typedef float f32x16 __attribute__((ext_vector_type(16)));
typedef unsigned u32x2 __attribute__((ext_vector_type(2)));
typedef unsigned u32x4 __attribute__((ext_vector_type(4)));
typedef __bf16 bf16x2_t __attribute__((ext_vector_type(2)));
typedef __amdgpu_buffer_rsrc_t rsrc_t;
#define LAS __attribute__((address_space(3)))
#define DI __device__ __forceinline__

constexpr int SEQ = 2048, DM = 640;
constexpr unsigned WS_G = 0;
constexpr unsigned WS_IN = 4096;
constexpr unsigned WS_KVH = WS_IN + 256 * 256;
constexpr unsigned WS_KVP = WS_KVH + 128 * 40960;
constexpr unsigned WS_O = WS_KVP + 128 * 7168;
constexpr unsigned WS_END = WS_O + 2 * SEQ * DM * 2;
constexpr unsigned OOB = 0x7FFF0000u;

DI unsigned pk2(float lo, float hi) {
  bf16x2_t r = __builtin_convertvector((f32x2){lo, hi}, bf16x2_t);
  return __builtin_bit_cast(unsigned, r);
}
DI float rbf(float x) { return __builtin_bit_cast(float, pk2(0.f, x)); }
DI float fmax3(float a, float b, float c) { return __builtin_elementwise_maximum(__builtin_elementwise_maximum(a, b), c); }
DI s16x4 trread(const LAS char* p) { return __builtin_amdgcn_ds_read_tr16_b64_v4i16((LAS s16x4*)p); }
DI bf16x8 cat8(s16x4 a, s16x4 b) { return __builtin_shufflevector(a, b, 0, 1, 2, 3, 4, 5, 6, 7); }
DI bf16x8 asfrag(u32x4 v) { return __builtin_bit_cast(bf16x8, v); }
DI bf16x8 frag2(u32x2 a, u32x2 b) { return asfrag((u32x4){a[0], a[1], b[0], b[1]}); }
DI rsrc_t mkrsrc(const void* p, unsigned bytes) { return __builtin_amdgcn_make_buffer_rsrc((void*)p, (short)0, (int)bytes, 0x00020000); }
DI void dma16(rsrc_t rs, unsigned lds, unsigned voff, unsigned soff) {
  asm volatile("s_mov_b32 m0, %0\n\ts_nop 0\n\tbuffer_load_dwordx4 %1, %2, %3 offen lds"
               :: "s"(lds), "v"(voff), "s"(rs), "s"(soff) : "memory");
}
DI void dma16c(rsrc_t rs, unsigned lds, unsigned voff, unsigned soff) {
  asm volatile("s_mov_b32 m0, %0\n\ts_nop 0\n\tbuffer_load_dwordx4 %1, %2, %3 offen sc1 lds"
               :: "s"(lds), "v"(voff), "s"(rs), "s"(soff) : "memory");
}
DI void touch(rsrc_t rs, unsigned lds, unsigned voff, unsigned soff) {
  asm volatile("s_mov_b32 m0, %0\n\ts_nop 0\n\tbuffer_load_dword %1, %2, %3 offen lds"
               :: "s"(lds), "v"(voff), "s"(rs), "s"(soff) : "memory");
}
DI void touchc(rsrc_t rs, unsigned lds, unsigned voff) {
  asm volatile("s_mov_b32 m0, %0\n\ts_nop 0\n\tbuffer_load_dword %1, %2, 0 offen sc1 lds" :: "s"(lds), "v"(voff), "s"(rs) : "memory");
}
template <unsigned LO, unsigned HI>
DI void dma16m(rsrc_t rs, unsigned lds, unsigned voff, unsigned soff) {
  asm volatile("s_mov_b32 m0, %0\n\ts_mov_b32 exec_lo, %4\n\ts_mov_b32 exec_hi, %5\n\tbuffer_load_dwordx4 %1, %2, %3 offen lds\n\ts_mov_b64 exec, -1"
               :: "s"(lds), "v"(voff), "s"(rs), "s"(soff), "i"(LO), "i"(HI) : "memory");
}
DI void st16c(rsrc_t rs, unsigned voff, u32x4 v) { __builtin_amdgcn_raw_buffer_store_b128(v, rs, voff, 0, 16); }
DI void st8c(rsrc_t rs, unsigned voff, u32x2 v) { __builtin_amdgcn_raw_buffer_store_b64(v, rs, voff, 0, 16); }
DI u32x2 lds8(const LAS char* p) { return *(const volatile LAS u32x2*)p; }
DI u32x2 next_tag(u32x2 g) { return (u32x2){g[0] * 1664525u + 1013904223u, g[1] * 22695477u + 2654435761u}; }
#define MFMA16(a, b, c) __builtin_amdgcn_mfma_f32_16x16x32_bf16((a), (b), (c), 0, 0, 0)
#define MFMA32(a, b, c) __builtin_amdgcn_mfma_f32_32x32x16_bf16((a), (b), (c), 0, 0, 0)
#define MFMA32K8(a, b, c) __builtin_amdgcn_mfma_f32_32x32x8bf16_1k(__builtin_bit_cast(s16x4, a), __builtin_bit_cast(s16x4, b), (c), 0, 0, 0)
#define WAIT_BAR(N) asm volatile("s_waitcnt vmcnt(" #N ") lgkmcnt(0)\n\ts_barrier" ::: "memory")
#define LBAR() asm volatile("s_waitcnt lgkmcnt(0)\n\ts_barrier" ::: "memory")
#define SB() __builtin_amdgcn_sched_barrier(0)

constexpr float LOG2E = 1.4426950408889634f;

struct WFrag { bf16x8 t0k0, t1k0; u32x2 t0k1, t1k1; };
DI WFrag load_wfrag(const LAS char* a0  , const LAS char* a1  , int hh, int lane) {
  u32x2 r0 = __builtin_bit_cast(u32x2, trread(a0)), r1 = __builtin_bit_cast(u32x2, trread(a0 + 160));
  u32x2 r2 = __builtin_bit_cast(u32x2, trread(a1)), r3 = __builtin_bit_cast(u32x2, trread(a1 + 160));
  u32x2 r4 = __builtin_bit_cast(u32x2, trread(a0 + 640)), r5 = __builtin_bit_cast(u32x2, trread(a1 + 640));
  const unsigned sg = (lane & 4) ? 0x80008000u : 0u;
  const unsigned km = hh ? 0u : 0xFFFFFFFFu;
  WFrag w;
  w.t0k0 = asfrag((u32x4){r0[0] ^ sg, r0[1] ^ sg, r1[0] ^ sg, r1[1] ^ sg});
  w.t1k0 = asfrag((u32x4){r2[0] ^ sg, r2[1] ^ sg, r3[0] ^ sg, r3[1] ^ sg});
  w.t0k1 = (u32x2){(r4[0] ^ sg) & km, (r4[1] ^ sg) & km};
  w.t1k1 = (u32x2){(r5[0] ^ sg) & km, (r5[1] ^ sg) & km};
  return w;
}
struct HProj { f32x16 p0, p1; };
struct HRow { bf16x8 x0; u32x2 x1; };
DI HRow hh_read(const LAS char* p, const LAS char* p4) { HRow r; r.x0 = frag2(lds8(p), lds8(p + 1024)); r.x1 = lds8(p4); return r; }
DI HProj hh_proj(const WFrag& w, const HRow& x) {
  const f32x16 z = {};
  HProj h;
  h.p0 = MFMA32(w.t0k0, x.x0, z);
  h.p1 = MFMA32(w.t1k0, x.x0, z);
  h.p0 = MFMA32K8(w.t0k1, x.x1, h.p0);
  h.p1 = MFMA32K8(w.t1k1, x.x1, h.p1);
  return h;
}
template <int N, int NV> DI void mix_mfma() {
#pragma unroll
  for (int i = 0; i < N; ++i) { __builtin_amdgcn_sched_group_barrier(0x8, 1, 0); __builtin_amdgcn_sched_group_barrier(0x402, NV, 0); }
}
DI void hh_soft(const HProj& h, unsigned (&ph)[10]) {
  f32x2 v[10];
#pragma unroll
  for (int i = 0; i < 8; ++i) v[i] = (f32x2){rbf(h.p0[2 * i]), rbf(h.p0[2 * i + 1])};
#pragma unroll
  for (int i = 0; i < 2; ++i) v[8 + i] = (f32x2){rbf(h.p1[2 * i]), rbf(h.p1[2 * i + 1])};
  float m = __builtin_elementwise_maximum(v[0][0], v[0][1]);
#pragma unroll
  for (int i = 1; i < 10; ++i) m = fmax3(m, v[i][0], v[i][1]);
  const float nm = -m * LOG2E;
#pragma unroll
  for (int i = 0; i < 10; ++i) {
    v[i] = __builtin_elementwise_fma(v[i], (f32x2){LOG2E, LOG2E}, (f32x2){nm, nm});
    v[i] = (f32x2){__builtin_amdgcn_exp2f(v[i][0]), __builtin_amdgcn_exp2f(v[i][1])};
  }
  const f32x2 s = (((v[0] + v[1]) + (v[2] + v[3])) + ((v[4] + v[5]) + (v[6] + v[7]))) + (v[8] + v[9]);
  const float r = __builtin_amdgcn_rcpf(s[0] + s[1]);
#pragma unroll
  for (int i = 0; i < 10; ++i) { v[i] *= (f32x2){r, r}; ph[i] = pk2(v[i][0], v[i][1]); }
}

constexpr float SC2 = 0.22360679774997896f * 1.4426950408889634f;

DI unsigned o_off(int h, int srow) { return (unsigned)((srow >> 4) * 20480 + (srow & 15) * 16 + 5 * (h >> 3) * 1024 + ((h >> 1) & 3) * 256); }
DI void store_o(rsrc_t ro, unsigned v16, unsigned v8, const float (&o)[12]) {
  st16c(ro, v16, (u32x4){pk2(o[0], o[1]), pk2(o[2], o[3]), pk2(o[4], o[5]), pk2(o[6], o[7])});
  st8c(ro, v8, (u32x2){pk2(o[8], o[9]), pk2(o[10], o[11])});
}
#define PIN(x) asm volatile("" : "+v"(x))
DI void wait_flags(rsrc_t rin, unsigned voff, bool need, u32x2 tag) {
#pragma unroll 1
  for (int spin = 0; spin < (1 << 20); ++spin) {
    u32x2 f;
    asm volatile("buffer_load_dwordx2 %0, %1, %2, 0 offen sc1\n\ts_waitcnt vmcnt(0)" : "=&v"(f) : "v"(voff), "s"(rin) : "memory");
    if (__all(!need || (f[0] == tag[0] && f[1] == tag[1]))) break;
    __builtin_amdgcn_s_sleep(1);
  }
}

constexpr unsigned CODE_BYTES = 28 * 1024;
constexpr int G_STAGE = 16384 + 30720;
constexpr int KP = 1024, KH = 5 * KP, VP = 1056, VH = 5376, SEG_V = 4 * KH, SEG = SEG_V + 4 * VH;
constexpr int L_LOC = 512, L_Q = L_LOC + SEG, L_NEAR = L_Q + 20480, L_FAR = L_NEAR + SEG;
constexpr int A_PHI = L_FAR, A_KV = L_FAR;
constexpr int L_ONE = L_FAR + 8 * 5632 + 160, L_W = L_ONE + 1024 + 96, L_G = L_W + 4096, L_T = L_G + 1024, L_FB = L_T + 256, L_S = L_FB + 16, K_LDS = L_S + 1024;
constexpr int P_W = 1536, P_X = L_FAR;
static_assert(3 * G_STAGE + 64 <= L_W && K_LDS <= 160 * 1024 - 2048 && P_W + 20 * 5120 + 64 <= L_FAR && P_W + 8 * 5120 == L_Q && P_W + 12 * 5120 == L_NEAR, "LDS map");
static_assert(L_LOC % 256 == 0 && SEG % 256 == 0 && SEG_V % 256 == 0 && VH % 256 == 0 && L_NEAR % 256 == 0 && L_ONE % 256 == 160 && L_W % 16 == 0, "banks");

DI f32x16 qk_tile(const LAS char* kh, const LAS char* k4, bf16x8 q0, u32x2 q1, f32x16 c) {
  const u32x2 a = lds8(kh), b = lds8(kh + KP), d = lds8(k4);
  c = MFMA32(frag2(a, b), q0, c);
  return MFMA32K8(d, q1, c);
}
DI f32x16 pv_tile(const LAS char* vp, const f32x16& s, f32x16 o) {
  const bf16x8 v0 = cat8(trread(vp), trread(vp + 64)), v1 = cat8(trread(vp + 128), trread(vp + 192));
  unsigned pw[8];
#pragma unroll
  for (int i = 0; i < 8; ++i) pw[i] = pk2(__builtin_amdgcn_exp2f(s[2 * i]), __builtin_amdgcn_exp2f(s[2 * i + 1]));
  o = MFMA32(v0, asfrag((u32x4){pw[0], pw[1], pw[2], pw[3]}), o);
  return MFMA32(v1, asfrag((u32x4){pw[4], pw[5], pw[6], pw[7]}), o);
}
struct Strm { const LAS char* kh; const LAS char* k4; bf16x8 q0; u32x2 q1; };
struct KFr { u32x2 a, b, d; };
DI KFr kread(const Strm& s, int off) { KFr k; k.a = lds8(s.kh + off); k.b = lds8(s.kh + off + KP); k.d = lds8(s.k4 + off); return k; }
DI void tile_step(f32x16& o, const f32x16& sc, const LAS char* vp, f32x16& sn, const Strm& n, KFr& k, const Strm& ks, int koff) {
  const f32x16 z = {};
  const bf16x8 v0 = cat8(trread(vp), trread(vp + 64)), v1 = cat8(trread(vp + 128), trread(vp + 192));
  float e[16];
#pragma unroll
  for (int i = 0; i < 3; ++i) e[i] = __builtin_amdgcn_exp2f(sc[i]);
  asm volatile("" : "+v"(e[0]), "+v"(e[1]), "+v"(e[2]));
  SB();
  sn = MFMA32(frag2(k.a, k.b), n.q0, z);
#pragma unroll
  for (int i = 3; i < 7; ++i) e[i] = __builtin_amdgcn_exp2f(sc[i]);
  asm volatile("" : "+v"(e[3]), "+v"(e[4]), "+v"(e[5]), "+v"(e[6]));
  SB();
  sn = MFMA32K8(k.d, n.q1, sn);
  k = kread(ks, koff);
#pragma unroll
  for (int i = 7; i < 10; ++i) e[i] = __builtin_amdgcn_exp2f(sc[i]);
  u32x4 p0 = {pk2(e[0], e[1]), pk2(e[2], e[3]), pk2(e[4], e[5]), pk2(e[6], e[7])};
  asm volatile("" : "+v"(e[8]), "+v"(e[9]), "+v"(p0));
  SB();
  o = MFMA32(v0, asfrag(p0), o);
#pragma unroll
  for (int i = 10; i < 16; ++i) e[i] = __builtin_amdgcn_exp2f(sc[i]);
  o = MFMA32(v1, asfrag((u32x4){pk2(e[8], e[9]), pk2(e[10], e[11]), pk2(e[12], e[13]), pk2(e[14], e[15])}), o);
  SB();
}
struct Pipe { f32x16 s; KFr k; };
DI Pipe prime(const Strm& s, const f32x16& c0, int off, int step) {
  Pipe p;
  p.k = kread(s, off + step);
  p.s = qk_tile(s.kh + off, s.k4 + off, s.q0, s.q1, c0);
  return p;
}
DI void run_tiles(f32x16& o, const Strm& s, Pipe& p, const LAS char* vp, int off, int step, int n) {
  f32x16 sb;
#pragma unroll 1
  for (;;) {
    tile_step(o, p.s, vp, sb, s, p.k, s, off + 2 * step);
    if (n == 1) break;
    tile_step(o, sb, vp + step, p.s, s, p.k, s, off + 3 * step);
    if (n == 2) break;
    n -= 2; off += 2 * step; vp += 2 * step;
  }
}

DI void run_tiles4(f32x16& o, const Strm& s, Pipe& p, const LAS char* vp, int n) {
  f32x16 sb;
  tile_step(o, p.s, vp, sb, s, p.k, s, 512);
  if (n == 1) return;
  tile_step(o, sb, vp + 256, p.s, s, p.k, s, 768);
  if (n == 2) return;
  tile_step(o, p.s, vp + 512, sb, s, p.k, s, 768);
  if (n == 3) return;
  o = pv_tile(vp + 768, sb, o);
}

__global__ __launch_bounds__(512, 2) void k1_fused(const __hip_bfloat16* __restrict__ xA, const __hip_bfloat16* __restrict__ wA,
                                                   const __hip_bfloat16* __restrict__ xB, const __hip_bfloat16* __restrict__ wB,
                                                   const __hip_bfloat16* __restrict__ wphi, const __hip_bfloat16* __restrict__ woA,
                                                   const __hip_bfloat16* __restrict__ woB, char* __restrict__ ws,
                                                   __hip_bfloat16* __restrict__ out) {
  __shared__ __attribute__((aligned(16))) char smem[K_LDS];
  const LAS char* sm = (const LAS char*)smem;
  LAS char* smw = (LAS char*)smem;
  const unsigned lds0 = (unsigned)(size_t)(LAS char*)smem;
  const int tid = threadIdx.x, lane = tid & 63;
  const int wid = __builtin_amdgcn_readfirstlane(tid >> 6);
  const int xcd = blockIdx.x & 7, idx = blockIdx.x >> 3;
  const int br = idx & 1, sub = idx >> 1;
  const int mt = (xcd >> 2) * 8 + (sub & 7), nt = (xcd & 3) * 2 + (sub >> 3);
  const int mt3 = xcd * 2 + (sub & 1), nt3 = sub >> 1;
  const int me = blockIdx.x;
  const int g = lane >> 4, fr = lane & 15, q4 = fr >> 2, p4 = lane & 3;
  {
    unsigned long long pc;
    asm volatile("s_getpc_b64 %0" : "=s"(pc));
    if (wid == 0) touch(mkrsrc((const void*)pc, CODE_BYTES), lds0 + L_T, lane < 7 ? (unsigned)((lane * 32 + idx) * 128) : OOB, 0);
  }
  const char* wg = (const char*)(br ? wB : wA);
  const rsrc_t rx = mkrsrc((const char*)(br ? xB : xA) + (size_t)mt * 128 * 1280, 128 * 1280);
  const rsrc_t rw = mkrsrc(wg + nt * 160, 640 * 3840 - nt * 160);
  const unsigned vx0 = (unsigned)((2 * (lane >> 3)) * 1280 + (((lane & 7) ^ g) * 16));
  const unsigned vx1 = (unsigned)((16 + 2 * (lane >> 3)) * 1280 + (((lane & 7) ^ (4 + g)) * 16));
  const int wr = lane >= 30 ? 1 : 0, wc = lane - 30 * wr, wsg = (wc >= 10) + (wc >= 20);
  const unsigned vw = (unsigned)(wr * 3840 + wsg * 1280 + (wc - 10 * wsg) * 16);
  const unsigned sx = (unsigned)((32 * (wid >> 1) + (wid & 1)) * 1280);
  const unsigned sw = (unsigned)((16 * (wid >> 1) + 4 * (wid & 1)) * 3840);
  const unsigned lx = lds0 + wid * 2048, lw = lds0 + 16384 + wid * 3840;
#define G_DMA(i, bo, xo, wo) do { \
    if ((i) == 0 || (i) == 3) dma16(rx, lx + (bo) + ((i) ? 1024 : 0), (i) ? vx1 : vx0, (xo)); \
    else { const int n_ = (i) < 3 ? (i) - 1 : (i) - 2; \
      dma16m<0xFFFFFFFFu, 0x0FFFFFFFu>(rw, lw + (bo) + n_ * 960, vw, (wo) + ((n_ >> 1) * 8 + (n_ & 1) * 2) * 3840); } } while (0)
#pragma unroll
  for (int i = 0; i < 6; ++i) G_DMA(i, 0, sx, sw);
  const int bi = mt * 8 + nt;
  const rsrc_t rt = (wid & 4) ? mkrsrc(wg, 640 * 3840) : mkrsrc(br ? xB : xA, SEQ * 1280);
  const unsigned tst = (wid & 4) ? 245760u : 128u;
  unsigned vt = (wid & 4) ? (fr < 15 ? (unsigned)((bi * 15 + fr) * 128 + g * 245760) : OOB) : (unsigned)((bi * 16 + fr) * 1280 + g * 128);
  if ((wid & 3) == 2) vt = g == 0 ? vt + 4 * tst : OOB;
  if ((wid & 3) >= 2) touch(rt, lds0 + L_T, vt, tst);
  const rsrc_t rt2 = (wid & 4) ? mkrsrc((wid & 1) ? wA : wB, 640 * 3840) : mkrsrc((wid & 1) ? xA : xB, SEQ * 1280);
  vt = (wid & 4) ? (lane < 60 ? (unsigned)(((bi >> 2) * 60 + lane) * 128) : OOB) : (unsigned)(((bi >> 2) * 64 + lane) * 1280);

  const int wm = wid >> 2, wn = wid & 3;
  const LAS char* xa0 = sm + (64 * wm + fr) * 128 + ((g ^ (fr >> 1)) * 16);
  const LAS char* xa1 = sm + (((64 * wm + fr) * 128 + ((g ^ (fr >> 1)) * 16)) ^ 64);
  const LAS char* wa = sm + 16384 + (16 * (g >> 1) + 4 * (g & 1) + q4) * 480 + 8 * p4 + wn * 128;
  f32x4 acc[4][4] = {};
#pragma unroll
  for (int i = 0; i < 4; ++i) asm volatile("" : "+v"(acc[i][0]), "+v"(acc[i][1]), "+v"(acc[i][2]), "+v"(acc[i][3]));
  int eb[4];
#pragma unroll
  for (int j = 0; j < 4; ++j) {
    const int qd = 16 * wn + 4 * j + g, r = qd - 40;
    const int v = L_LOC + SEG_V + r * VP + ((r * 13) >> 6) * 96;
    eb[j] = (qd < 20 ? L_Q + qd * 1024 : qd < 40 ? L_LOC + (qd - 20) * KP : qd < 60 ? v : L_S) + wm * 512 + fr * 16;
    asm volatile("" : "+v"(eb[j]));
  }
  int cons;
  {
    const int cm = br ? mt + 1 + lane : lane;
    const int c1 = (((((nt & 1) * 8 + (cm & 7)) << 1) | br) << 3) | (((cm >> 3) & 1) * 4 + (nt >> 1));
    const int c2 = (((((lane & 7) * 2 + (mt & 1)) << 1) | br) << 3) | (mt >> 1);
    cons = lane < 16 ? ((cm < 16 && (!br || lane < 2)) ? c1 : -1) : (lane < 24 ? c2 : lane == 63 ? me : -1);
    asm volatile("" : "+v"(cons));
  }
  const int hh = lane >> 5, r32 = lane & 31, b16 = (lane >> 4) & 1, vq = 4 * b16 + p4;
  unsigned vflag = (unsigned)(WS_IN + cons * 256 + (lane >= 16 ? 16 + nt : br ? 1 - lane : mt) * 8);
  unsigned vf1 = (lane < 16 && cons >= 0) ? vflag : OOB, vf2 = (lane >= 16 && lane < 24) ? vflag : OOB;
  const int lq = (lane >= 10) + (lane >= 20) + (lane >= 30);
  unsigned vwo = (unsigned)(lq * 1280 + (lane - 10 * lq) * 16);
  PIN(vf1); PIN(vf2); PIN(vwo);
  const int hs = wid >> 2, qi = hs ? 3 - (wid & 3) : (wid & 3);
  const int hl = wid >> 1, rw0 = 64 * (wid & 1);
  int c0, c1, c2, c3, c4, c5, c6;
  unsigned v16, v8;
  if (br) {
    c0 = hs * KH + hh * 2 * KP + r32 * 8;
    c1 = hs * KH + 4 * KP + r32 * 8;
    c2 = (vq < 5 ? SEG_V + hs * VH + vq * VP : L_ONE) + (4 * hh + q4) * 8;
    c3 = vq < 5 ? -1 : 0;
    c4 = L_Q + hs * 5120 + (32 * qi + r32) * 8;
    c5 = L_Q + (2 + hs) * 5120 + (32 * (3 - qi) + r32) * 8;
    c6 = hh * 2048;
    v16 = o_off(4 * nt + hs, mt * 128 + 32 * qi + r32);
  } else {
    c0 = L_W + hl * 800 + (8 * hh + q4) * 40 + (2 * b16 + (p4 >> 1)) * 8;
    c1 = L_W + hl * 800 + (8 * hh + q4) * 40 + 32;
    c2 = hl * 5120 + (rw0 + r32) * 8;
    c3 = A_PHI + wid * 5632 + r32 * 88 + hh * 40;
    c4 = A_PHI + wid * 5632 + (8 * hh + q4) * 88 + (16 * b16 + 4 * p4) * 2;
    c5 = (vq < 5 ? L_LOC + SEG_V + hl * VH + vq * VP : L_ONE) + (rw0 + 8 * hh + q4) * 8;
    c6 = A_PHI + wid * 5632 + min(r32, 21) * 160 + hh * 16;
    v16 = o_off(4 * nt + hl, mt * 128 + rw0 + r32);
  }
  {
    const int odd = br ? hs : (hl & 1);
    v8 = hh ? OOB : (v16 & ~1023u) + 2048 + 512 * odd + ((v16 & 1023u) >> 1);
    v16 += (hh + 3 * odd) * 1024;
  }
  PIN(c0); PIN(c1); PIN(c2); PIN(c3); PIN(c4); PIN(c5); PIN(c6); PIN(v16); PIN(v8);
  f32x16 mle, mgt;
#pragma unroll
  for (int e = 0; e < 16; ++e) {
    const bool le = (e & 3) + 8 * (e >> 2) + 4 * hh <= r32;
    mle[e] = le ? 0.f : -INFINITY;
    mgt[e] = le ? -INFINITY : 0.f;
  }
  PIN(mle); PIN(mgt);
  const rsrc_t rg = mkrsrc(ws + WS_G, WS_KVH);
  const rsrc_t rph = mkrsrc((const char*)wphi + nt * 3200, br ? 0 : 3200);
  bf16x8 x0[4], x1[4];
  s16x4 w0[4][2], w1[4][2];
#define G_RD(X, W, xa, kk, r) do { \
    if ((r) < 2) W[0][(r)] = trread(wa + ((kk) * 32 + (r) * 8) * 480); \
    else if ((r) < 6) X[(r) - 2] = *(const LAS bf16x8*)(xa + ((r) - 2) * 2048); \
    else if ((r) < 12) W[((r) - 4) >> 1][(r) & 1] = trread(wa + ((kk) * 32 + ((r) & 1) * 8) * 480 + (((r) - 4) >> 1) * 32); } while (0)
#define G_MM(X, W, m) acc[(m) & 3][(m) >> 2] = MFMA16(cat8(W[(m) >> 2][0], W[(m) >> 2][1]), X[(m) & 3], acc[(m) & 3][(m) >> 2])
  if (wid == 0) dma16c(rg, lds0 + L_G, cons >= 0 ? cons * 16 : OOB, 0);
  if ((wid & 3) == 1) {
    dma16(rph, lds0 + L_W + (wid >> 2) * 2048, (unsigned)lane * 16, (wid >> 2) * 2048);
    dma16(rph, lds0 + L_W + (wid >> 2) * 2048 + 1024, (unsigned)lane * 16, (wid >> 2) * 2048 + 1024);
  }
  if (wid == 4) touchc(rg, lds0 + L_T, lane < 2 ? (unsigned)(WS_IN + me * 256 + lane * 128) : OOB);
  asm volatile("s_barrier" ::: "memory");
#pragma unroll
  for (int i = 0; i < 6; ++i) G_DMA(i, G_STAGE, sx + 128, sw + 245760);
  WAIT_BAR(7);
#pragma unroll
  for (int r = 0; r < 12; ++r) G_RD(x0, w0, xa0, 0, r);
  int bo = 2 * G_STAGE;
  int bd = bo;
  unsigned xo = sx + 256, wo = sw + 2 * 245760;
#pragma unroll
  for (int i = 0; i < 3; ++i) G_DMA(i, bo, xo, wo);
#pragma unroll 1
  for (int t = 0; t < 10; ++t) {
    SB();
#pragma unroll
    for (int m = 0; m < 12; ++m) {
      G_MM(x0, w0, m); G_RD(x1, w1, xa1, 1, m);
      if (m % 3 == 2 && m < 9) G_DMA(3 + m / 3, bd, xo, wo);
      SB();
    }
    if (wn < 3) { G_MM(x0, w0, 12); G_MM(x0, w0, 13); G_MM(x0, w0, 14); G_MM(x0, w0, 15); }
    const int d = bo == G_STAGE ? -2 * G_STAGE : G_STAGE;
    xa0 += d; xa1 += d; wa += d;
    bo = bo == 2 * G_STAGE ? 0 : bo + G_STAGE;
    bd = t < 7 ? bo : 0x30000;
    xo = t < 7 ? xo + 128 : OOB;
    wo += 245760;
    WAIT_BAR(6);
    SB();
#pragma unroll
    for (int m = 0; m < 12; ++m) {
      G_MM(x1, w1, m); G_RD(x0, w0, xa0, 0, m);
      if (m % 3 == 2 && m < 9) G_DMA(m / 3, bd, xo, wo);
      SB();
    }
    if (wn < 3) { G_MM(x1, w1, 12); G_MM(x1, w1, 13); G_MM(x1, w1, 14); G_MM(x1, w1, 15); }
    if (t == (bi & 3) + 1 && !br && (wid & 3) >= 2) touch(rt2, lds0 + L_T, vt, (5 + t) * tst);
    if (t == 8 && wid == 3) touch(mkrsrc(br ? woB : woA, 640 * 1280), lds0 + L_T, lane < 50 ? (unsigned)((bi * 50 + lane) * 128) : OOB, 0);
  }
  {
    const rsrc_t rh = mkrsrc(ws + WS_KVH + (mt * 8 + nt) * 40960, 40960);
    const int v0 = (16 * wn + g - 20) * 1024 + wm * 512 + fr * 16;
#pragma unroll
    for (int j = 3; j >= 0; --j) {
      if (br && 4 * wn + j < 5) {
#pragma unroll
        for (int i = 0; i < 4; ++i) acc[i][j] *= SC2;
      }
#pragma unroll
      for (int i = 0; i < 2; ++i) {
        const f32x4 a = acc[2 * i][j], b = acc[2 * i + 1][j];
        const u32x4 ev = (u32x4){pk2(a[0], a[1]), pk2(a[2], a[3]), pk2(b[0], b[1]), pk2(b[2], b[3])};
        if (br && 4 * wn + j >= 5) st16c(rh, (unsigned)(v0 + j * 4096 + i * 256), ev);
        *(LAS u32x4*)(smw + eb[j] + i * 256) = ev;
      }
    }
  }
  if (tid < 128) *(LAS u32x2*)(smw + L_ONE + tid * 8) = (u32x2){0x3F80u, 0u};
  if (tid == 511) *(LAS unsigned*)(smw + L_FB) = 0u;
  const u32x2 gc = next_tag(*(const LAS u32x2*)(sm + L_G + lane * 16));
  const u32x2 tag = {(unsigned)__builtin_amdgcn_readlane(gc[0], 63), (unsigned)__builtin_amdgcn_readlane(gc[1], 63)};
  const char* wog = (const char*)(br ? woB : woA);
  const rsrc_t ro = mkrsrc(ws + WS_O + br * (SEQ * DM * 2), SEQ * DM * 2);
  const rsrc_t rwo = mkrsrc(wog + nt3 * 160, 640 * 1280 - nt3 * 160);
  const int sg = 2 * (wid >> 2) + (wid & 1), st = (wid >> 1) & 1;
#define P_DMA2(w5, G) dma16m<0xFFFFFFFFu, 0xFFu>(rwo, lds0 + P_W + (4 * (w5) + (G)) * 5120 + wid * 640, vwo, \
      (unsigned)(160 * (G) + 4 * (w5) + ((w5) >= 2 ? 8 : 0) + 40 * sg + st * ((w5) == 2 ? 20 : 8)) * 1280u)
#define P_DMA(b) P_DMA2((b) >> 2, (b) & 3)
  if (br) {
    WAIT_BAR(0);
    if (wid == 0) st8c(rg, vf1, gc);
    Strm S[2];
#pragma unroll
    for (int r = 0; r < 2; ++r) {
      S[r].kh = sm + c0 + r * 2 * KH;
      S[r].k4 = sm + c1 + r * 2 * KH;
      const LAS char* qp = sm + (r ? c5 : c4);
      S[r].q0 = frag2(lds8(qp + c6), lds8(qp + c6 + 1024));
      const u32x2 t4 = lds8(qp + 4096);
      S[r].q1 = hh ? (u32x2){0u, 0u} : t4;
    }
#define VPTR(r, seg) (sm + c2 + (((seg) + (r) * 2 * VH) & c3))
#define HALO(tile, seg, pg) do { \
      const rsrc_t rn_ = mkrsrc(ws + WS_KVH + (((tile) >= 0 ? (tile) : 0) * 8 + nt) * 40960 + (pg) * 10240, (tile) >= 0 ? 10240 : 0); \
      const unsigned hd_ = lds0 + (seg) + ((pg) >= 2 ? SEG_V + ((pg) & 1) * 2 * VH : ((pg) & 1) * 2 * KH), hp_ = (pg) >= 2 ? VP : KP; \
      _Pragma("unroll") for (int i = 0; i < 10; ++i) dma16c(rn_, hd_ + i * hp_ + (i >= 5 ? ((pg) >> 1) * 96 : 0), lane * 16, i * 1024); } while (0)
    f32x16 o0 = {}, o1 = {};
    Pipe p0 = prime(S[0], mle, L_LOC + qi * 256, -256), p1 = prime(S[1], mle, L_LOC + (3 - qi) * 256, -256);
    LBAR();
#pragma unroll
    for (int G = 0; G < 4; ++G) P_DMA2(2, G);
    run_tiles(o0, S[0], p0, VPTR(0, L_LOC) + qi * 256, L_LOC + qi * 256, -256, qi + 1);
    const int nb = qi < 2 ? 1 : 0, pg = 2 * hs + (qi & 1);
    if (qi == 0) {
      wait_flags(rg, (unsigned)(WS_IN + me * 256 + 8), mt >= 1, tag);
      HALO(mt - 1, L_NEAR, 2 * hs);
      HALO(mt - 1, L_NEAR, 2 * hs + 1);
    }
    run_tiles(o1, S[1], p1, VPTR(1, L_LOC) + (3 - qi) * 256, L_LOC + (3 - qi) * 256, -256, 4 - qi);
    if (nb) {
      WAIT_BAR(0);
    } else {
      wait_flags(rg, (unsigned)(WS_IN + me * 256), mt >= 2, tag);
      HALO(mt - 2, L_FAR, pg);
      LBAR();
    }
    if (mt > 0) {
      const f32x16 z = {};
      KFr k = kread(S[1], L_NEAR);
      f32x16 sa = qk_tile(S[0].kh + L_NEAR, S[0].k4 + L_NEAR, S[0].q0, S[0].q1, z), sb;
      const LAS char* v0 = VPTR(0, L_NEAR);
      const LAS char* v1 = VPTR(1, L_NEAR);
      Strm N0 = S[0], N1 = S[1];
      N0.kh += L_NEAR; N0.k4 += L_NEAR; N1.kh += L_NEAR; N1.k4 += L_NEAR;
      PIN(N0.kh); PIN(N0.k4); PIN(N1.kh); PIN(N1.k4);
#pragma unroll
      for (int off = 0; off < 1024; off += 256) {
        P_DMA2(0, off >> 8);
        tile_step(o0, sa, v0 + off, sb, N1, k, N0, off + 256);
        P_DMA2(1, off >> 8);
        tile_step(o1, sb, v1 + off, sa, N0, k, N1, off + 256);
        if (off == 512) { WAIT_BAR(6); }
      }
    } else {
#pragma unroll
      for (int ks = 0; ks < 8; ++ks) P_DMA(ks);
    }
    p0 = prime(S[0], mgt, L_FAR + qi * 256, 256);
    p1 = prime(S[1], mgt, L_FAR + (3 - qi) * 256, 256);
    LBAR();
    bool redo[2];
#pragma unroll
    for (int r = 0; r < 2; ++r) {
      const int qt = r ? 3 - qi : qi;
      f32x16& o = r ? o1 : o0;
#pragma unroll
      for (int G = 0; G < 4; ++G) P_DMA2(3 + r, G);
      if (mt > 1) {
        Strm F = S[r];
        F.kh += L_FAR + qt * 256; F.k4 += L_FAR + qt * 256;
        PIN(F.kh); PIN(F.k4);
        run_tiles4(o, F, r ? p1 : p0, VPTR(r, L_FAR) + qt * 256, 4 - qt);
      }
      const float l = __uint_as_float(__builtin_amdgcn_permlane32_swap(__float_as_uint(o[8]), __float_as_uint(o[8]), false, false)[1]);
      const float rl = __builtin_amdgcn_rcpf(l);
      float of[12];
#pragma unroll
      for (int i = 0; i < 12; ++i) of[i] = o[i] * rl;
      const unsigned dv = r ? (unsigned)((3 - 2 * qi) * 40960 + 256) : 0u;
      store_o(ro, v16 + dv, hh ? OOB : v8 + dv - (r ? 128 : 0), of);
      float am = __builtin_fabsf(o[0]);
#pragma unroll
      for (int i = 1; i < 11; i += 2) am = fmax3(am, __builtin_fabsf(o[i]), __builtin_fabsf(o[i + 1]));
      am = __builtin_elementwise_maximum(am, __builtin_fabsf(o[11]));
      redo[r] = !__all(l >= 0x1p-80f && l <= 0x1p100f && am <= 0x1p126f);
    }
    if (redo[0] || redo[1]) *(LAS unsigned*)(smw + L_FB) = 1u;
    WAIT_BAR(0);
    if (__builtin_expect(__builtin_amdgcn_readfirstlane(*(const volatile LAS unsigned*)(sm + L_FB)) != 0u, 0)) {
      HALO(mt - nb, nb ? L_NEAR : L_LOC, pg);
      WAIT_BAR(0);
#pragma unroll
      for (int r = 0; r < 2; ++r) {
        if (!redo[r]) continue;
        const int qt = r ? 3 - qi : qi, w0 = mt >= 2 ? qt : mt == 1 ? 4 : 8;
        f32x16 o;
        float m = -INFINITY;
#pragma unroll 1
        for (int pass = 0; pass < 2; ++pass) {
          o = (f32x16){};
#pragma unroll 1
          for (int w = w0; w <= qt + 8; ++w) {
            const int seg = w < 4 ? L_FAR : w < 8 ? L_NEAR : L_LOC, off = (w & 3) * 256;
            f32x16 c;
#pragma unroll
            for (int e = 0; e < 16; ++e) c[e] = w == qt + 8 ? mle[e] : w == qt ? mgt[e] : 0.f;
            f32x16 s = qk_tile(S[r].kh + seg + off, S[r].k4 + seg + off, S[r].q0, S[r].q1, c);
            if (pass == 0) {
#pragma unroll
              for (int e = 0; e < 16; e += 2) m = fmax3(m, s[e], s[e + 1]);
            } else {
#pragma unroll
              for (int e = 0; e < 16; ++e) s[e] -= m;
              o = pv_tile(VPTR(r, seg) + off, s, o);
            }
          }
          auto rr = __builtin_amdgcn_permlane32_swap(__float_as_uint(m), __float_as_uint(m), false, false);
          m = __builtin_elementwise_maximum(__uint_as_float(rr[0]), __uint_as_float(rr[1]));
        }
        const float rl = __builtin_amdgcn_rcpf(__uint_as_float(__builtin_amdgcn_permlane32_swap(__float_as_uint(o[8]), __float_as_uint(o[8]), false, false)[1]));
        float of[12];
#pragma unroll
        for (int i = 0; i < 12; ++i) of[i] = o[i] * rl;
        const unsigned dv = r ? (unsigned)((3 - 2 * qi) * 40960 + 256) : 0u;
        store_o(ro, v16 + dv, hh ? OOB : v8 + dv - (r ? 128 : 0), of);
      }
      LBAR();
#pragma unroll 1
      for (int G = 0; G < 4; ++G) {
#pragma unroll
        for (int u = 0; u < 5; ++u) P_DMA2(u, G);
      }
      WAIT_BAR(0);
    }
  } else {
    WFrag wf = load_wfrag(sm + c0, sm + c1, hh, lane);
    PIN(wf.t0k0); PIN(wf.t1k0); PIN(wf.t0k1); PIN(wf.t1k1);
    LBAR();
    const LAS char* xp = sm + c2 + hh * 2048;
    const LAS char* x4 = sm + c2 + 4096;
    const HRow rk0 = hh_read(xp + L_LOC, x4 + L_LOC), rk1 = hh_read(xp + L_LOC + 256, x4 + L_LOC + 256);
    const HRow rq0 = hh_read(xp + L_Q, x4 + L_Q), rq1 = hh_read(xp + L_Q + 256, x4 + L_Q + 256);
    const HProj hk0 = hh_proj(wf, rk0);
    bf16x8 vf[4];
#pragma unroll
    for (int ks = 0; ks < 4; ++ks) vf[ks] = cat8(trread(sm + c5 + ks * 128), trread(sm + c5 + ks * 128 + 32));
#pragma unroll
    for (int ks = 12; ks < 18; ++ks) P_DMA(ks);
    SB();
    f32x16 d0 = {}, d1 = {};
    HProj hq0, hq1;
    {
      unsigned ph[10];
      HProj hk1 = hh_proj(wf, rk1);
      hh_soft(hk0, ph);
#pragma unroll
      for (int i = 0; i < 5; ++i) *(LAS u32x2*)(smw + c3 + 8 * i) = (u32x2){ph[2 * i], ph[2 * i + 1]};
      PIN(hk1.p0); PIN(hk1.p1);
      mix_mfma<4, 8>();
      SB();
      bf16x8 pf[4];
#pragma unroll
      for (int i = 0; i < 4; ++i) pf[i] = cat8(trread(sm + c4 + (i >> 1) * 16 * 88 + (i & 1) * 64), trread(sm + c4 + ((i >> 1) * 16 + 4) * 88 + (i & 1) * 64));
      P_DMA(18); P_DMA(19);
      SB();
      hq0 = hh_proj(wf, rq0);
      d0 = MFMA32(pf[0], vf[0], d0); d1 = MFMA32(pf[1], vf[0], d1);
      d0 = MFMA32(pf[2], vf[1], d0); d1 = MFMA32(pf[3], vf[1], d1);
      hh_soft(hk1, ph);
#pragma unroll
      for (int i = 0; i < 5; ++i) *(LAS u32x2*)(smw + c3 + 32 * 88 + 8 * i) = (u32x2){ph[2 * i], ph[2 * i + 1]};
      PIN(hq0.p0); PIN(hq0.p1); PIN(d0); PIN(d1);
      mix_mfma<8, 8>();
      SB();
#pragma unroll
      for (int i = 0; i < 4; ++i) pf[i] = cat8(trread(sm + c4 + (32 + (i >> 1) * 16) * 88 + (i & 1) * 64), trread(sm + c4 + (36 + (i >> 1) * 16) * 88 + (i & 1) * 64));
      d0 = MFMA32(pf[0], vf[2], d0); d1 = MFMA32(pf[1], vf[2], d1);
      d0 = MFMA32(pf[2], vf[3], d0); d1 = MFMA32(pf[3], vf[3], d1);
    }
    *(LAS f32x4*)(smw + c6) = (f32x4){d0[0], d0[1], d0[2], d0[3]};
    *(LAS f32x4*)(smw + c6 + 32) = (f32x4){d0[4], d0[5], d0[6], d0[7]};
    *(LAS f32x4*)(smw + c6 + 64) = (f32x4){d0[8], d0[9], d0[10], d0[11]};
    *(LAS f32x4*)(smw + c6 + 96) = (f32x4){d0[12], d0[13], d0[14], d0[15]};
    *(LAS f32x4*)(smw + c6 + 128) = (f32x4){d1[0], d1[1], d1[2], d1[3]};
    SB();
    hq1 = hh_proj(wf, rq1);
    PIN(hq1.p0); PIN(hq1.p1);
    LBAR();
    const rsrc_t rkv = mkrsrc(ws + WS_KVP + nt * 16 * 7168, 16 * 7168);
    {
      const int e = (wid & 1) * 64 + lane;
      const LAS char* o = sm + A_PHI + (wid & ~1) * 5632 + e * 32;
      const f32x4 a = *(const LAS f32x4*)o + *(const LAS f32x4*)(o + 5632), b = *(const LAS f32x4*)(o + 16) + *(const LAS f32x4*)(o + 5648);
      st16c(rkv, e < 112 ? (unsigned)(mt * 7168 + hl * 1792 + e * 16) : OOB, (u32x4){pk2(a[0], a[1]), pk2(a[2], a[3]), pk2(b[0], b[1]), pk2(b[2], b[3])});
    }
#pragma unroll
    for (int ks = 0; ks < 6; ++ks) P_DMA(ks);
    SB();
    unsigned phq[2][10];
    hh_soft(hq0, phq[0]);
#pragma unroll
    for (int i = 0; i < 10; ++i) PIN(phq[0][i]);
    WAIT_BAR(6);
    if (wid == 0) st8c(rg, vf1, gc);
    if (tid >= 256) *(LAS u32x2*)(smw + A_KV + ((tid - 256) >> 1) * 112 + (tid & 1) * 48 + 40) = (u32x2){0u, 0u};
    hh_soft(hq1, phq[1]);
#pragma unroll
    for (int i = 0; i < 10; ++i) PIN(phq[1][i]);
    int kd0, kd1;
    {
      const int h4 = tid / 112, e = tid - h4 * 112, d = e / 5, oc = e - d * 5;
      kd0 = e < 105 ? A_KV + h4 * 3584 + d * 112 + oc * 16 + (oc > 2 ? 8 : 0) : L_S;
      kd1 = kd0 + (oc == 2 ? 16 : 8);
      PIN(kd0); PIN(kd1);
    }
    wait_flags(rg, (unsigned)(WS_IN + me * 256 + fr * 8), true, tag);
    if (wid < 7) {
      u32x4 p[16];
#pragma unroll
      for (int i = 0; i < 16; ++i) p[i] = __builtin_amdgcn_raw_buffer_load_b128(rkv, tid * 16, i * 7168, 16);
      f32x2 lo[2] = {}, hi[2] = {};
#pragma unroll
      for (int i = 0; i < 16; ++i)
#pragma unroll
        for (int u = 0; u < 2; ++u) {
          lo[u] += (f32x2){__uint_as_float(p[i][2 * u] << 16), __uint_as_float(p[i][2 * u + 1] << 16)};
          hi[u] += (f32x2){__uint_as_float(p[i][2 * u] & 0xFFFF0000u), __uint_as_float(p[i][2 * u + 1] & 0xFFFF0000u)};
        }
      *(LAS u32x2*)(smw + kd0) = (u32x2){pk2(lo[0][0], hi[0][0]), pk2(lo[0][1], hi[0][1])};
      *(LAS u32x2*)(smw + kd1) = (u32x2){pk2(lo[1][0], hi[1][0]), pk2(lo[1][1], hi[1][1])};
    }
#pragma unroll
    for (int ks = 6; ks < 12; ++ks) P_DMA(ks);
    LBAR();
    const LAS char* kp = sm + A_KV + hl * 3584 + (r32 == 24 ? 20 : r32) * 112 + hh * 48;
    const bf16x8 f0 = *(const LAS bf16x8*)(kp), f1 = *(const LAS bf16x8*)(kp + 16), f2 = *(const LAS bf16x8*)(kp + 32);
    f32x16 n[2];
#pragma unroll
    for (int t = 0; t < 2; ++t) n[t] = MFMA32(f0, asfrag((u32x4){phq[t][0], phq[t][1], phq[t][2], phq[t][3]}), (f32x16){});
#pragma unroll
    for (int t = 0; t < 2; ++t) n[t] = MFMA32(f1, asfrag((u32x4){phq[t][4], phq[t][5], phq[t][6], phq[t][7]}), n[t]);
#pragma unroll
    for (int t = 0; t < 2; ++t) n[t] = MFMA32(f2, asfrag((u32x4){phq[t][8], phq[t][9], 0u, 0u}), n[t]);
#pragma unroll
    for (int t = 0; t < 2; ++t) {
      const float den = rbf(rbf(hh ? n[t][8] : n[t][12]) + 1e-6f);
      const float r = __builtin_amdgcn_rcpf(den);
      float o[12];
#pragma unroll
      for (int i = 0; i < 12; ++i) o[i] = rbf(n[t][i]) * r;
      store_o(ro, v16 + t * 40960, hh ? OOB : v8 + t * 40960, o);
    }
    WAIT_BAR(0);
  }
  {
    if (wid == 0) st8c(rg, vf2, gc);
    int oa[3];
    unsigned og[3];
#pragma unroll
    for (int i = 0; i < 3; ++i) {
      const int c = min(lane + 64 * i, 160), row = c / 10, ch = c - row * 10;
      oa[i] = wid * 2816 + row * 176 + ch * 16;
      og[i] = (unsigned)(row * 2560 + ch * 16);
      PIN(oa[i]); PIN(og[i]);
    }
    const int pm = wid & 3, kh = wid >> 2;
    wait_flags(rg, (unsigned)(WS_IN + me * 256 + 128 + (4 * kh + (lane & 3)) * 8), true, tag);
    const rsrc_t rf = mkrsrc(ws + WS_O + (unsigned)((br * 128 + mt3 * 8 + 2 * pm) * 20 + 10 * kh) * 1024u, 30 * 1024);
    const unsigned vk = lane * 16 + kh * 20480, vh = lane * 16 + (1 - kh) * 20480;
    u32x4 xf[10][2];
    const unsigned vk8 = lane * 8 + kh * 20480, vh8 = lane * 8 + (1 - kh) * 20480;
#define P_LD1(v, v8_, tau) ((tau) % 5 == 2 ? __builtin_bit_cast(u32x4, frag2(__builtin_amdgcn_raw_buffer_load_b64(rf, v8_, (tau) * 1024, 16), \
                                                                             __builtin_amdgcn_raw_buffer_load_b64(rf, v8_, (tau) * 1024 + 512, 16))) \
                                           : __builtin_amdgcn_raw_buffer_load_b128(rf, v, (tau) * 1024, 16))
#define P_LD(tau) do { xf[tau][0] = P_LD1(vk, vk8, tau); xf[tau][1] = P_LD1(vh, vh8, tau); } while (0)
    P_LD(0); P_LD(1); P_LD(2);
    const LAS char* wb = sm + P_W + kh * 10240 + (16 * (g >> 1) + 4 * (g & 1) + q4) * 160 + 8 * p4;
    const LAS char* wb2 = wb + 12 * 5120;
    f32x4 pc[2][5] = {};
    bf16x8 wf[2][5];
#pragma unroll
    for (int j = 0; j < 5; ++j) wf[0][j] = cat8(trread(wb + j * 32), trread(wb + 8 * 160 + j * 32));
#pragma unroll
    for (int tau = 0; tau < 10; ++tau) {
      SB();
      if (tau < 7) P_LD(tau + 3);
#pragma unroll
      for (int j = 0; j < 5; ++j) {
        if (tau < 9) {
          const int blk = ((tau + 1) % 5) * 4 + (tau + 1) / 5;
          const LAS char* wp = (blk >= 12 ? wb2 : wb) + (blk >= 12 ? blk - 12 : blk) * 5120 + j * 32;
          wf[(tau + 1) & 1][j] = cat8(trread(wp), trread(wp + 8 * 160));
        }
        pc[1][j] = MFMA16(wf[tau & 1][j], asfrag(xf[tau][1]), pc[1][j]);
        if (tau < 9) pc[0][j] = MFMA16(wf[tau & 1][j], asfrag(xf[tau][0]), pc[0][j]);
        SB();
      }
    }
#pragma unroll
    for (int j = 0; j < 5; ++j) {
      pc[0][j] = MFMA16(wf[1][j], asfrag(xf[9][0]), pc[0][j]);
      *(LAS f32x4*)(smw + P_X + (wid * 5 + j) * 1024 + lane * 16) = pc[1][j];
    }
    LBAR();
    if (tid == 0) st8c(rg, me * 16, tag);
    LAS char* ow = smw + wid * 2816 + fr * 176 + g * 8;
#pragma unroll
    for (int j = 0; j < 5; ++j) {
      const f32x4 a = pc[0][j] + *(const LAS f32x4*)(sm + P_X + ((wid ^ 4) * 5 + j) * 1024 + lane * 16);
      *(LAS u32x2*)(ow + j * 32) = (u32x2){pk2(a[0], a[1]), pk2(a[2], a[3])};
    }
    const rsrc_t rout = mkrsrc((char*)out + (size_t)(mt3 * 128 + 32 * pm + 16 * kh) * 2560 + br * 1280 + nt3 * 160, 15 * 2560 + 160);
#pragma unroll
    for (int i = 0; i < 3; ++i) __builtin_amdgcn_raw_buffer_store_b128(*(const LAS u32x4*)(sm + oa[i]), rout, og[i], 0, 0);
  }
}

extern "C" void kernel_launch(void* const* d_in, const int* in_sizes, int n_in, void* d_out, int out_size, void* d_ws,
                              size_t ws_size, hipStream_t stream) {
  k1_fused<<<256, 512, 0, stream>>>((const __hip_bfloat16*)d_in[0], (const __hip_bfloat16*)d_in[1], (const __hip_bfloat16*)d_in[4],
                                    (const __hip_bfloat16*)d_in[5], (const __hip_bfloat16*)d_in[2], (const __hip_bfloat16*)d_in[3],
                                    (const __hip_bfloat16*)d_in[6], (char*)d_ws, (__hip_bfloat16*)d_out);
}
```

```cpp
#include <hip/hip_runtime.h>
#include <hip/hip_bf16.h>

typedef short bf16x8 __attribute__((ext_vector_type(8)));
typedef short s16x4 __attribute__((ext_vector_type(4)));
typedef float f32x2 __attribute__((ext_vector_type(2)));
typedef float f32x4 __attribute__((ext_vector_type(4)));
typedef float f32x16 __attribute__((ext_vector_type(16)));
typedef unsigned u32x2 __attribute__((ext_vector_type(2)));
typedef unsigned u32x4 __attribute__((ext_vector_type(4)));
typedef __bf16 bf16x2_t __attribute__((ext_vector_type(2)));
typedef __amdgpu_buffer_rsrc_t rsrc_t;
#define LAS __attribute__((address_space(3)))
#define DI __device__ __forceinline__

constexpr int SEQ = 2048, DM = 640;
constexpr unsigned WS_G = 0;
constexpr unsigned WS_IN = 4096;
constexpr unsigned WS_KVH = WS_IN + 256 * 256;
constexpr unsigned WS_KVP = WS_KVH + 128 * 40960;
constexpr unsigned WS_O = WS_KVP + 128 * 7168;
constexpr unsigned WS_END = WS_O + 2 * SEQ * DM * 2;
constexpr unsigned OOB = 0x7FFF0000u;

DI unsigned pk2(float lo, float hi) {
  bf16x2_t r = __builtin_convertvector((f32x2){lo, hi}, bf16x2_t);
  return __builtin_bit_cast(unsigned, r);
}
DI float rbf(float x) { return __builtin_bit_cast(float, pk2(0.f, x)); }
DI float fmax3(float a, float b, float c) { return __builtin_elementwise_maximum(__builtin_elementwise_maximum(a, b), c); }
DI s16x4 trread(const LAS char* p) { return __builtin_amdgcn_ds_read_tr16_b64_v4i16((LAS s16x4*)p); }
DI bf16x8 cat8(s16x4 a, s16x4 b) { return __builtin_shufflevector(a, b, 0, 1, 2, 3, 4, 5, 6, 7); }
DI bf16x8 asfrag(u32x4 v) { return __builtin_bit_cast(bf16x8, v); }
DI bf16x8 frag2(u32x2 a, u32x2 b) { return asfrag((u32x4){a[0], a[1], b[0], b[1]}); }
DI rsrc_t mkrsrc(const void* p, unsigned bytes) { return __builtin_amdgcn_make_buffer_rsrc((void*)p, (short)0, (int)bytes, 0x00020000); }
DI void dma16(rsrc_t rs, unsigned lds, unsigned voff, unsigned soff) {
  asm volatile("s_mov_b32 m0, %0\n\ts_nop 0\n\tbuffer_load_dwordx4 %1, %2, %3 offen lds"
               :: "s"(lds), "v"(voff), "s"(rs), "s"(soff) : "memory");
}
DI void dma16c(rsrc_t rs, unsigned lds, unsigned voff, unsigned soff) {
  asm volatile("s_mov_b32 m0, %0\n\ts_nop 0\n\tbuffer_load_dwordx4 %1, %2, %3 offen sc1 lds"
               :: "s"(lds), "v"(voff), "s"(rs), "s"(soff) : "memory");
}
DI void touch(rsrc_t rs, unsigned lds, unsigned voff, unsigned soff) {
  asm volatile("s_mov_b32 m0, %0\n\ts_nop 0\n\tbuffer_load_dword %1, %2, %3 offen lds"
               :: "s"(lds), "v"(voff), "s"(rs), "s"(soff) : "memory");
}
DI void touchc(rsrc_t rs, unsigned lds, unsigned voff) {
  asm volatile("s_mov_b32 m0, %0\n\ts_nop 0\n\tbuffer_load_dword %1, %2, 0 offen sc1 lds" :: "s"(lds), "v"(voff), "s"(rs) : "memory");
}
template <unsigned LO, unsigned HI>
DI void dma16m(rsrc_t rs, unsigned lds, unsigned voff, unsigned soff) {
  asm volatile("s_mov_b32 m0, %0\n\ts_mov_b32 exec_lo, %4\n\ts_mov_b32 exec_hi, %5\n\tbuffer_load_dwordx4 %1, %2, %3 offen lds\n\ts_mov_b64 exec, -1"
               :: "s"(lds), "v"(voff), "s"(rs), "s"(soff), "i"(LO), "i"(HI) : "memory");
}
DI void st16c(rsrc_t rs, unsigned voff, u32x4 v) { __builtin_amdgcn_raw_buffer_store_b128(v, rs, voff, 0, 16); }
DI void st8c(rsrc_t rs, unsigned voff, u32x2 v) { __builtin_amdgcn_raw_buffer_store_b64(v, rs, voff, 0, 16); }
DI u32x2 lds8(const LAS char* p) { return *(const volatile LAS u32x2*)p; }
DI u32x2 next_tag(u32x2 g) { return (u32x2){g[0] * 1664525u + 1013904223u, g[1] * 22695477u + 2654435761u}; }
#define MFMA16(a, b, c) __builtin_amdgcn_mfma_f32_16x16x32_bf16((a), (b), (c), 0, 0, 0)
#define MFMA32(a, b, c) __builtin_amdgcn_mfma_f32_32x32x16_bf16((a), (b), (c), 0, 0, 0)
#define MFMA32K8(a, b, c) __builtin_amdgcn_mfma_f32_32x32x8bf16_1k(__builtin_bit_cast(s16x4, a), __builtin_bit_cast(s16x4, b), (c), 0, 0, 0)
#define WAIT_BAR(N) asm volatile("s_waitcnt vmcnt(" #N ") lgkmcnt(0)\n\ts_barrier" ::: "memory")
#define LBAR() asm volatile("s_waitcnt lgkmcnt(0)\n\ts_barrier" ::: "memory")
#define SB() __builtin_amdgcn_sched_barrier(0)

constexpr float LOG2E = 1.4426950408889634f;

struct WFrag { bf16x8 t0k0, t1k0; u32x2 t0k1, t1k1; };
DI WFrag load_wfrag(const LAS char* a0  , const LAS char* a1  , int hh, int lane) {
  u32x2 r0 = __builtin_bit_cast(u32x2, trread(a0)), r1 = __builtin_bit_cast(u32x2, trread(a0 + 160));
  u32x2 r2 = __builtin_bit_cast(u32x2, trread(a1)), r3 = __builtin_bit_cast(u32x2, trread(a1 + 160));
  u32x2 r4 = __builtin_bit_cast(u32x2, trread(a0 + 640)), r5 = __builtin_bit_cast(u32x2, trread(a1 + 640));
  const unsigned sg = (lane & 4) ? 0x80008000u : 0u;
  const unsigned km = hh ? 0u : 0xFFFFFFFFu;
  WFrag w;
  w.t0k0 = asfrag((u32x4){r0[0] ^ sg, r0[1] ^ sg, r1[0] ^ sg, r1[1] ^ sg});
  w.t1k0 = asfrag((u32x4){r2[0] ^ sg, r2[1] ^ sg, r3[0] ^ sg, r3[1] ^ sg});
  w.t0k1 = (u32x2){(r4[0] ^ sg) & km, (r4[1] ^ sg) & km};
  w.t1k1 = (u32x2){(r5[0] ^ sg) & km, (r5[1] ^ sg) & km};
  return w;
}
struct HProj { f32x16 p0, p1; };
struct HRow { bf16x8 x0; u32x2 x1; };
DI HRow hh_read(const LAS char* p, const LAS char* p4) { HRow r; r.x0 = frag2(lds8(p), lds8(p + 1024)); r.x1 = lds8(p4); return r; }
DI HProj hh_proj(const WFrag& w, const HRow& x) {
  const f32x16 z = {};
  HProj h;
  h.p0 = MFMA32(w.t0k0, x.x0, z);
  h.p1 = MFMA32(w.t1k0, x.x0, z);
  h.p0 = MFMA32K8(w.t0k1, x.x1, h.p0);
  h.p1 = MFMA32K8(w.t1k1, x.x1, h.p1);
  return h;
}
template <int N, int NV> DI void mix_mfma() {
#pragma unroll
  for (int i = 0; i < N; ++i) { __builtin_amdgcn_sched_group_barrier(0x8, 1, 0); __builtin_amdgcn_sched_group_barrier(0x402, NV, 0); }
}
DI void hh_soft(const HProj& h, unsigned (&ph)[10]) {
  f32x2 v[10];
#pragma unroll
  for (int i = 0; i < 8; ++i) v[i] = (f32x2){rbf(h.p0[2 * i]), rbf(h.p0[2 * i + 1])};
#pragma unroll
  for (int i = 0; i < 2; ++i) v[8 + i] = (f32x2){rbf(h.p1[2 * i]), rbf(h.p1[2 * i + 1])};
  float m = __builtin_elementwise_maximum(v[0][0], v[0][1]);
#pragma unroll
  for (int i = 1; i < 10; ++i) m = fmax3(m, v[i][0], v[i][1]);
  const float nm = -m * LOG2E;
#pragma unroll
  for (int i = 0; i < 10; ++i) {
    v[i] = __builtin_elementwise_fma(v[i], (f32x2){LOG2E, LOG2E}, (f32x2){nm, nm});
    v[i] = (f32x2){__builtin_amdgcn_exp2f(v[i][0]), __builtin_amdgcn_exp2f(v[i][1])};
  }
  const f32x2 s = (((v[0] + v[1]) + (v[2] + v[3])) + ((v[4] + v[5]) + (v[6] + v[7]))) + (v[8] + v[9]);
  const float r = __builtin_amdgcn_rcpf(s[0] + s[1]);
#pragma unroll
  for (int i = 0; i < 10; ++i) { v[i] *= (f32x2){r, r}; ph[i] = pk2(v[i][0], v[i][1]); }
}

constexpr float SC2 = 0.22360679774997896f * 1.4426950408889634f;

DI unsigned o_off(int h, int srow) { return (unsigned)((srow >> 4) * 20480 + (srow & 15) * 16 + 5 * (h >> 3) * 1024 + ((h >> 1) & 3) * 256); }
DI void store_o(rsrc_t ro, unsigned v16, unsigned v8, const float (&o)[12]) {
  st16c(ro, v16, (u32x4){pk2(o[0], o[1]), pk2(o[2], o[3]), pk2(o[4], o[5]), pk2(o[6], o[7])});
  st8c(ro, v8, (u32x2){pk2(o[8], o[9]), pk2(o[10], o[11])});
}
#define PIN(x) asm volatile("" : "+v"(x))
DI void wait_flags(rsrc_t rin, unsigned voff, bool need, u32x2 tag) {
#pragma unroll 1
  for (int spin = 0; spin < (1 << 20); ++spin) {
    u32x2 f;
    asm volatile("buffer_load_dwordx2 %0, %1, %2, 0 offen sc1\n\ts_waitcnt vmcnt(0)" : "=&v"(f) : "v"(voff), "s"(rin) : "memory");
    if (__all(!need || (f[0] == tag[0] && f[1] == tag[1]))) break;
    __builtin_amdgcn_s_sleep(1);
  }
}

constexpr unsigned CODE_BYTES = 28 * 1024;
constexpr int G_STAGE = 16384 + 30720;
constexpr int KP = 1024, KH = 5 * KP, VP = 1056, VH = 5376, SEG_V = 4 * KH, SEG = SEG_V + 4 * VH;
constexpr int L_LOC = 512, L_Q = L_LOC + SEG, L_NEAR = L_Q + 20480, L_FAR = L_NEAR + SEG;
constexpr int A_PHI = L_FAR, A_KV = L_FAR;
constexpr int L_ONE = L_FAR + 8 * 5632 + 160, L_W = L_ONE + 1024 + 96, L_G = L_W + 4096, L_T = L_G + 1024, L_FB = L_T + 256, L_S = L_FB + 16, K_LDS = L_S + 1024;
constexpr int P_W = 1536, P_X = L_FAR;
static_assert(3 * G_STAGE + 64 <= L_W && K_LDS <= 160 * 1024 - 2048 && P_W + 20 * 5120 + 64 <= L_FAR && P_W + 8 * 5120 == L_Q && P_W + 12 * 5120 == L_NEAR, "LDS map");
static_assert(L_LOC % 256 == 0 && SEG % 256 == 0 && SEG_V % 256 == 0 && VH % 256 == 0 && L_NEAR % 256 == 0 && L_ONE % 256 == 160 && L_W % 16 == 0, "banks");

DI f32x16 qk_tile(const LAS char* kh, const LAS char* k4, bf16x8 q0, u32x2 q1, f32x16 c) {
  const u32x2 a = lds8(kh), b = lds8(kh + KP), d = lds8(k4);
  c = MFMA32(frag2(a, b), q0, c);
  return MFMA32K8(d, q1, c);
}
DI f32x16 pv_tile(const LAS char* vp, const f32x16& s, f32x16 o) {
  const bf16x8 v0 = cat8(trread(vp), trread(vp + 64)), v1 = cat8(trread(vp + 128), trread(vp + 192));
  unsigned pw[8];
#pragma unroll
  for (int i = 0; i < 8; ++i) pw[i] = pk2(__builtin_amdgcn_exp2f(s[2 * i]), __builtin_amdgcn_exp2f(s[2 * i + 1]));
  o = MFMA32(v0, asfrag((u32x4){pw[0], pw[1], pw[2], pw[3]}), o);
  return MFMA32(v1, asfrag((u32x4){pw[4], pw[5], pw[6], pw[7]}), o);
}
struct Strm { const LAS char* kh; const LAS char* k4; bf16x8 q0; u32x2 q1; };
struct KFr { u32x2 a, b, d; };
DI KFr kread(const Strm& s, int off) { KFr k; k.a = lds8(s.kh + off); k.b = lds8(s.kh + off + KP); k.d = lds8(s.k4 + off); return k; }
DI void tile_step(f32x16& o, const f32x16& sc, const LAS char* vp, f32x16& sn, const Strm& n, KFr& k, const Strm& ks, int koff) {
  const f32x16 z = {};
  const bf16x8 v0 = cat8(trread(vp), trread(vp + 64)), v1 = cat8(trread(vp + 128), trread(vp + 192));
  float e[16];
#pragma unroll
  for (int i = 0; i < 3; ++i) e[i] = __builtin_amdgcn_exp2f(sc[i]);
  asm volatile("" : "+v"(e[0]), "+v"(e[1]), "+v"(e[2]));
  SB();
  sn = MFMA32(frag2(k.a, k.b), n.q0, z);
#pragma unroll
  for (int i = 3; i < 7; ++i) e[i] = __builtin_amdgcn_exp2f(sc[i]);
  asm volatile("" : "+v"(e[3]), "+v"(e[4]), "+v"(e[5]), "+v"(e[6]));
  SB();
  sn = MFMA32K8(k.d, n.q1, sn);
  k = kread(ks, koff);
#pragma unroll
  for (int i = 7; i < 10; ++i) e[i] = __builtin_amdgcn_exp2f(sc[i]);
  u32x4 p0 = {pk2(e[0], e[1]), pk2(e[2], e[3]), pk2(e[4], e[5]), pk2(e[6], e[7])};
  asm volatile("" : "+v"(e[8]), "+v"(e[9]), "+v"(p0));
  SB();
  o = MFMA32(v0, asfrag(p0), o);
#pragma unroll
  for (int i = 10; i < 16; ++i) e[i] = __builtin_amdgcn_exp2f(sc[i]);
  o = MFMA32(v1, asfrag((u32x4){pk2(e[8], e[9]), pk2(e[10], e[11]), pk2(e[12], e[13]), pk2(e[14], e[15])}), o);
  SB();
}
struct Pipe { f32x16 s; KFr k; };
DI Pipe prime(const Strm& s, const f32x16& c0, int off, int step) {
  Pipe p;
  p.k = kread(s, off + step);
  p.s = qk_tile(s.kh + off, s.k4 + off, s.q0, s.q1, c0);
  return p;
}
DI void run_tiles(f32x16& o, const Strm& s, Pipe& p, const LAS char* vp, int off, int step, int n) {
  f32x16 sb;
#pragma unroll 1
  for (;;) {
    tile_step(o, p.s, vp, sb, s, p.k, s, off + 2 * step);
    if (n == 1) break;
    tile_step(o, sb, vp + step, p.s, s, p.k, s, off + 3 * step);
    if (n == 2) break;
    n -= 2; off += 2 * step; vp += 2 * step;
  }
}

DI void run_tiles4(f32x16& o, const Strm& s, Pipe& p, const LAS char* vp, int n) {
  f32x16 sb;
  tile_step(o, p.s, vp, sb, s, p.k, s, 512);
  if (n == 1) return;
  tile_step(o, sb, vp + 256, p.s, s, p.k, s, 768);
  if (n == 2) return;
  tile_step(o, p.s, vp + 512, sb, s, p.k, s, 768);
  if (n == 3) return;
  o = pv_tile(vp + 768, sb, o);
}

__global__ __launch_bounds__(512, 2) void k1_fused(const __hip_bfloat16* __restrict__ xA, const __hip_bfloat16* __restrict__ wA,
                                                   const __hip_bfloat16* __restrict__ xB, const __hip_bfloat16* __restrict__ wB,
                                                   const __hip_bfloat16* __restrict__ wphi, const __hip_bfloat16* __restrict__ woA,
                                                   const __hip_bfloat16* __restrict__ woB, char* __restrict__ ws,
                                                   __hip_bfloat16* __restrict__ out) {
  __shared__ __attribute__((aligned(16))) char smem[K_LDS];
  const LAS char* sm = (const LAS char*)smem;
  LAS char* smw = (LAS char*)smem;
  const unsigned lds0 = (unsigned)(size_t)(LAS char*)smem;
  const int tid = threadIdx.x, lane = tid & 63;
  const int wid = __builtin_amdgcn_readfirstlane(tid >> 6);
  const int xcd = blockIdx.x & 7, idx = blockIdx.x >> 3;
  const int br = idx & 1, sub = idx >> 1;
  const int mt = (xcd >> 2) * 8 + (sub & 7), nt = (xcd & 3) * 2 + (sub >> 3);
  const int mt3 = xcd * 2 + (sub & 1), nt3 = sub >> 1;
  const int me = blockIdx.x;
  const int g = lane >> 4, fr = lane & 15, q4 = fr >> 2, p4 = lane & 3;
  {
    unsigned long long pc;
    asm volatile("s_getpc_b64 %0" : "=s"(pc));
    if (wid == 0) touch(mkrsrc((const void*)pc, CODE_BYTES), lds0 + L_T, lane < 7 ? (unsigned)((lane * 32 + idx) * 128) : OOB, 0);
  }
  const char* wg = (const char*)(br ? wB : wA);
  const rsrc_t rx = mkrsrc((const char*)(br ? xB : xA) + (size_t)mt * 128 * 1280, 128 * 1280);
  const rsrc_t rw = mkrsrc(wg + nt * 160, 640 * 3840 - nt * 160);
  const unsigned vx0 = (unsigned)((2 * (lane >> 3)) * 1280 + (((lane & 7) ^ g) * 16));
  const unsigned vx1 = (unsigned)((16 + 2 * (lane >> 3)) * 1280 + (((lane & 7) ^ (4 + g)) * 16));
  const int wr = lane >= 30 ? 1 : 0, wc = lane - 30 * wr, wsg = (wc >= 10) + (wc >= 20);
  const unsigned vw = (unsigned)(wr * 3840 + wsg * 1280 + (wc - 10 * wsg) * 16);
  const unsigned sx = (unsigned)((32 * (wid >> 1) + (wid & 1)) * 1280);
  const unsigned sw = (unsigned)((16 * (wid >> 1) + 4 * (wid & 1)) * 3840);
  const unsigned lx = lds0 + wid * 2048, lw = lds0 + 16384 + wid * 3840;
#define G_DMA(i, bo, xo, wo) do { \
    if ((i) == 0 || (i) == 3) dma16(rx, lx + (bo) + ((i) ? 1024 : 0), (i) ? vx1 : vx0, (xo)); \
    else { const int n_ = (i) < 3 ? (i) - 1 : (i) - 2; \
      dma16m<0xFFFFFFFFu, 0x0FFFFFFFu>(rw, lw + (bo) + n_ * 960, vw, (wo) + ((n_ >> 1) * 8 + (n_ & 1) * 2) * 3840); } } while (0)
#pragma unroll
  for (int i = 0; i < 6; ++i) G_DMA(i, 0, sx, sw);
  const int bi = mt * 8 + nt;
  const rsrc_t rt = (wid & 4) ? mkrsrc(wg, 640 * 3840) : mkrsrc(br ? xB : xA, SEQ * 1280);
  const unsigned tst = (wid & 4) ? 245760u : 128u;
  unsigned vt = (wid & 4) ? (fr < 15 ? (unsigned)((bi * 15 + fr) * 128 + g * 245760) : OOB) : (unsigned)((bi * 16 + fr) * 1280 + g * 128);
  if ((wid & 3) == 2) vt = g == 0 ? vt + 4 * tst : OOB;
  if ((wid & 3) >= 2) touch(rt, lds0 + L_T, vt, tst);
  const rsrc_t rt2 = (wid & 4) ? mkrsrc((wid & 1) ? wA : wB, 640 * 3840) : mkrsrc((wid & 1) ? xA : xB, SEQ * 1280);
  vt = (wid & 4) ? (lane < 60 ? (unsigned)(((bi >> 2) * 60 + lane) * 128) : OOB) : (unsigned)(((bi >> 2) * 64 + lane) * 1280);

  const int wm = wid >> 2, wn = wid & 3;
  const LAS char* xa0 = sm + (64 * wm + fr) * 128 + ((g ^ (fr >> 1)) * 16);
  const LAS char* xa1 = sm + (((64 * wm + fr) * 128 + ((g ^ (fr >> 1)) * 16)) ^ 64);
  const LAS char* wa = sm + 16384 + (16 * (g >> 1) + 4 * (g & 1) + q4) * 480 + 8 * p4 + wn * 128;
  f32x4 acc[4][4] = {};
#pragma unroll
  for (int i = 0; i < 4; ++i) asm volatile("" : "+v"(acc[i][0]), "+v"(acc[i][1]), "+v"(acc[i][2]), "+v"(acc[i][3]));
  int eb[4];
#pragma unroll
  for (int j = 0; j < 4; ++j) {
    const int qd = 16 * wn + 4 * j + g, r = qd - 40;
    const int v = L_LOC + SEG_V + r * VP + ((r * 13) >> 6) * 96;
    eb[j] = (qd < 20 ? L_Q + qd * 1024 : qd < 40 ? L_LOC + (qd - 20) * KP : qd < 60 ? v : L_S) + wm * 512 + fr * 16;
    asm volatile("" : "+v"(eb[j]));
  }
  int cons;
  {
    const int cm = br ? mt + 1 + lane : lane;
    const int c1 = (((((nt & 1) * 8 + (cm & 7)) << 1) | br) << 3) | (((cm >> 3) & 1) * 4 + (nt >> 1));
    const int c2 = (((((lane & 7) * 2 + (mt & 1)) << 1) | br) << 3) | (mt >> 1);
    cons = lane < 16 ? ((cm < 16 && (!br || lane < 2)) ? c1 : -1) : (lane < 24 ? c2 : lane == 63 ? me : -1);
    asm volatile("" : "+v"(cons));
  }
  const int hh = lane >> 5, r32 = lane & 31, b16 = (lane >> 4) & 1, vq = 4 * b16 + p4;
  unsigned vflag = (unsigned)(WS_IN + cons * 256 + (lane >= 16 ? 16 + nt : br ? 1 - lane : mt) * 8);
  unsigned vf1 = (lane < 16 && cons >= 0) ? vflag : OOB, vf2 = (lane >= 16 && lane < 24) ? vflag : OOB;
  const int lq = (lane >= 10) + (lane >= 20) + (lane >= 30);
  unsigned vwo = (unsigned)(lq * 1280 + (lane - 10 * lq) * 16);
  PIN(vf1); PIN(vf2); PIN(vwo);
  const int hs = wid >> 2, qi = hs ? 3 - (wid & 3) : (wid & 3);
  const int hl = wid >> 1, rw0 = 64 * (wid & 1);
  int c0, c1, c2, c3, c4, c5, c6;
  unsigned v16, v8;
  if (br) {
    c0 = hs * KH + hh * 2 * KP + r32 * 8;
    c1 = hs * KH + 4 * KP + r32 * 8;
    c2 = (vq < 5 ? SEG_V + hs * VH + vq * VP : L_ONE) + (4 * hh + q4) * 8;
    c3 = vq < 5 ? -1 : 0;
    c4 = L_Q + hs * 5120 + (32 * qi + r32) * 8;
    c5 = L_Q + (2 + hs) * 5120 + (32 * (3 - qi) + r32) * 8;
    c6 = hh * 2048;
    v16 = o_off(4 * nt + hs, mt * 128 + 32 * qi + r32);
  } else {
    c0 = L_W + hl * 800 + (8 * hh + q4) * 40 + (2 * b16 + (p4 >> 1)) * 8;
    c1 = L_W + hl * 800 + (8 * hh + q4) * 40 + 32;
    c2 = hl * 5120 + (rw0 + r32) * 8;
    c3 = A_PHI + wid * 5632 + r32 * 88 + hh * 40;
    c4 = A_PHI + wid * 5632 + (8 * hh + q4) * 88 + (16 * b16 + 4 * p4) * 2;
    c5 = (vq < 5 ? L_LOC + SEG_V + hl * VH + vq * VP : L_ONE) + (rw0 + 8 * hh + q4) * 8;
    c6 = A_PHI + wid * 5632 + min(r32, 21) * 160 + hh * 16;
    v16 = o_off(4 * nt + hl, mt * 128 + rw0 + r32);
  }
  {
    const int odd = br ? hs : (hl & 1);
    v8 = hh ? OOB : (v16 & ~1023u) + 2048 + 512 * odd + ((v16 & 1023u) >> 1);
    v16 += (hh + 3 * odd) * 1024;
  }
  PIN(c0); PIN(c1); PIN(c2); PIN(c3); PIN(c4); PIN(c5); PIN(c6); PIN(v16); PIN(v8);
  f32x16 mle, mgt;
#pragma unroll
  for (int e = 0; e < 16; ++e) {
    const bool le = (e & 3) + 8 * (e >> 2) + 4 * hh <= r32;
    mle[e] = le ? 0.f : -INFINITY;
    mgt[e] = le ? -INFINITY : 0.f;
  }
  PIN(mle); PIN(mgt);
  const rsrc_t rg = mkrsrc(ws + WS_G, WS_KVH);
  const rsrc_t rph = mkrsrc((const char*)wphi + nt * 3200, br ? 0 : 3200);
  bf16x8 x0[4], x1[4];
  s16x4 w0[4][2], w1[4][2];
#define G_RD(X, W, xa, kk, r) do { \
    if ((r) < 2) W[0][(r)] = trread(wa + ((kk) * 32 + (r) * 8) * 480); \
    else if ((r) < 6) X[(r) - 2] = *(const LAS bf16x8*)(xa + ((r) - 2) * 2048); \
    else if ((r) < 12) W[((r) - 4) >> 1][(r) & 1] = trread(wa + ((kk) * 32 + ((r) & 1) * 8) * 480 + (((r) - 4) >> 1) * 32); } while (0)
#define G_MM(X, W, m) acc[(m) & 3][(m) >> 2] = MFMA16(cat8(W[(m) >> 2][0], W[(m) >> 2][1]), X[(m) & 3], acc[(m) & 3][(m) >> 2])
  if (wid == 0) dma16c(rg, lds0 + L_G, cons >= 0 ? cons * 16 : OOB, 0);
  if ((wid & 3) == 1) {
    dma16(rph, lds0 + L_W + (wid >> 2) * 2048, (unsigned)lane * 16, (wid >> 2) * 2048);
    dma16(rph, lds0 + L_W + (wid >> 2) * 2048 + 1024, (unsigned)lane * 16, (wid >> 2) * 2048 + 1024);
  }
  if (wid == 4) touchc(rg, lds0 + L_T, lane < 2 ? (unsigned)(WS_IN + me * 256 + lane * 128) : OOB);
  asm volatile("s_barrier" ::: "memory");
#pragma unroll
  for (int i = 0; i < 6; ++i) G_DMA(i, G_STAGE, sx + 128, sw + 245760);
  WAIT_BAR(7);
#pragma unroll
  for (int r = 0; r < 12; ++r) G_RD(x0, w0, xa0, 0, r);
  int bo = 2 * G_STAGE;
  int bd = bo;
  unsigned xo = sx + 256, wo = sw + 2 * 245760;
#pragma unroll
  for (int i = 0; i < 3; ++i) G_DMA(i, bo, xo, wo);
#pragma unroll 1
  for (int t = 0; t < 10; ++t) {
    SB();
#pragma unroll
    for (int m = 0; m < 12; ++m) {
      G_MM(x0, w0, m); G_RD(x1, w1, xa1, 1, m);
      if (m % 3 == 2 && m < 9) G_DMA(3 + m / 3, bd, xo, wo);
      SB();
    }
    if (wn < 3) { G_MM(x0, w0, 12); G_MM(x0, w0, 13); G_MM(x0, w0, 14); G_MM(x0, w0, 15); }
    const int d = bo == G_STAGE ? -2 * G_STAGE : G_STAGE;
    xa0 += d; xa1 += d; wa += d;
    bo = bo == 2 * G_STAGE ? 0 : bo + G_STAGE;
    bd = t < 7 ? bo : 0x30000;
    xo = t < 7 ? xo + 128 : OOB;
    wo += 245760;
    WAIT_BAR(6);
    SB();
#pragma unroll
    for (int m = 0; m < 12; ++m) {
      G_MM(x1, w1, m); G_RD(x0, w0, xa0, 0, m);
      if (m % 3 == 2 && m < 9) G_DMA(m / 3, bd, xo, wo);
      SB();
    }
    if (wn < 3) { G_MM(x1, w1, 12); G_MM(x1, w1, 13); G_MM(x1, w1, 14); G_MM(x1, w1, 15); }
    if (t == (bi & 3) + 1 && !br && (wid & 3) >= 2) touch(rt2, lds0 + L_T, vt, (5 + t) * tst);
    if (t == 8 && wid == 3) touch(mkrsrc(br ? woB : woA, 640 * 1280), lds0 + L_T, lane < 50 ? (unsigned)((bi * 50 + lane) * 128) : OOB, 0);
  }
  {
    const rsrc_t rh = mkrsrc(ws + WS_KVH + (mt * 8 + nt) * 40960, 40960);
    const int v0 = (16 * wn + g - 20) * 1024 + wm * 512 + fr * 16;
#pragma unroll
    for (int j = 3; j >= 0; --j) {
      if (br && 4 * wn + j < 5) {
#pragma unroll
        for (int i = 0; i < 4; ++i) acc[i][j] *= SC2;
      }
#pragma unroll
      for (int i = 0; i < 2; ++i) {
        const f32x4 a = acc[2 * i][j], b = acc[2 * i + 1][j];
        const u32x4 ev = (u32x4){pk2(a[0], a[1]), pk2(a[2], a[3]), pk2(b[0], b[1]), pk2(b[2], b[3])};
        if (br && 4 * wn + j >= 5) st16c(rh, (unsigned)(v0 + j * 4096 + i * 256), ev);
        *(LAS u32x4*)(smw + eb[j] + i * 256) = ev;
      }
    }
  }
  if (tid < 128) *(LAS u32x2*)(smw + L_ONE + tid * 8) = (u32x2){0x3F80u, 0u};
  if (tid == 511) *(LAS unsigned*)(smw + L_FB) = 0u;
  const u32x2 gc = next_tag(*(const LAS u32x2*)(sm + L_G + lane * 16));
  const u32x2 tag = {(unsigned)__builtin_amdgcn_readlane(gc[0], 63), (unsigned)__builtin_amdgcn_readlane(gc[1], 63)};
  const char* wog = (const char*)(br ? woB : woA);
  const rsrc_t ro = mkrsrc(ws + WS_O + br * (SEQ * DM * 2), SEQ * DM * 2);
  const rsrc_t rwo = mkrsrc(wog + nt3 * 160, 640 * 1280 - nt3 * 160);
  const int sg = 2 * (wid >> 2) + (wid & 1), st = (wid >> 1) & 1;
#define P_DMA2(w5, G) dma16m<0xFFFFFFFFu, 0xFFu>(rwo, lds0 + P_W + (4 * (w5) + (G)) * 5120 + wid * 640, vwo, \
      (unsigned)(160 * (G) + 4 * (w5) + ((w5) >= 2 ? 8 : 0) + 40 * sg + st * ((w5) == 2 ? 20 : 8)) * 1280u)
#define P_DMA(b) P_DMA2((b) >> 2, (b) & 3)
  if (br) {
    WAIT_BAR(0);
    if (wid == 0) st8c(rg, vf1, gc);
    Strm S[2];
#pragma unroll
    for (int r = 0; r < 2; ++r) {
      S[r].kh = sm + c0 + r * 2 * KH;
      S[r].k4 = sm + c1 + r * 2 * KH;
      const LAS char* qp = sm + (r ? c5 : c4);
      S[r].q0 = frag2(lds8(qp + c6), lds8(qp + c6 + 1024));
      const u32x2 t4 = lds8(qp + 4096);
      S[r].q1 = hh ? (u32x2){0u, 0u} : t4;
    }
#define VPTR(r, seg) (sm + c2 + (((seg) + (r) * 2 * VH) & c3))
#define HALO(tile, seg, pg) do { \
      const rsrc_t rn_ = mkrsrc(ws + WS_KVH + (((tile) >= 0 ? (tile) : 0) * 8 + nt) * 40960 + (pg) * 10240, (tile) >= 0 ? 10240 : 0); \
      const unsigned hd_ = lds0 + (seg) + ((pg) >= 2 ? SEG_V + ((pg) & 1) * 2 * VH : ((pg) & 1) * 2 * KH), hp_ = (pg) >= 2 ? VP : KP; \
      _Pragma("unroll") for (int i = 0; i < 10; ++i) dma16c(rn_, hd_ + i * hp_ + (i >= 5 ? ((pg) >> 1) * 96 : 0), lane * 16, i * 1024); } while (0)
    f32x16 o0 = {}, o1 = {};
    Pipe p0 = prime(S[0], mle, L_LOC + qi * 256, -256), p1 = prime(S[1], mle, L_LOC + (3 - qi) * 256, -256);
    LBAR();
#pragma unroll
    for (int G = 0; G < 4; ++G) P_DMA2(2, G);
    run_tiles(o0, S[0], p0, VPTR(0, L_LOC) + qi * 256, L_LOC + qi * 256, -256, qi + 1);
    const int nb = qi < 2 ? 1 : 0, pg = 2 * hs + (qi & 1);
    if (qi == 0) {
      wait_flags(rg, (unsigned)(WS_IN + me * 256 + 8), mt >= 1, tag);
      HALO(mt - 1, L_NEAR, 2 * hs);
      HALO(mt - 1, L_NEAR, 2 * hs + 1);
    }
    run_tiles(o1, S[1], p1, VPTR(1, L_LOC) + (3 - qi) * 256, L_LOC + (3 - qi) * 256, -256, 4 - qi);
    if (nb) {
      WAIT_BAR(0);
    } else {
      wait_flags(rg, (unsigned)(WS_IN + me * 256), mt >= 2, tag);
      HALO(mt - 2, L_FAR, pg);
      LBAR();
    }
    if (mt > 0) {
      const f32x16 z = {};
      KFr k = kread(S[1], L_NEAR);
      f32x16 sa = qk_tile(S[0].kh + L_NEAR, S[0].k4 + L_NEAR, S[0].q0, S[0].q1, z), sb;
      const LAS char* v0 = VPTR(0, L_NEAR);
      const LAS char* v1 = VPTR(1, L_NEAR);
      Strm N0 = S[0], N1 = S[1];
      N0.kh += L_NEAR; N0.k4 += L_NEAR; N1.kh += L_NEAR; N1.k4 += L_NEAR;
      PIN(N0.kh); PIN(N0.k4); PIN(N1.kh); PIN(N1.k4);
#pragma unroll
      for (int off = 0; off < 1024; off += 256) {
        P_DMA2(0, off >> 8);
        tile_step(o0, sa, v0 + off, sb, N1, k, N0, off + 256);
        P_DMA2(1, off >> 8);
        tile_step(o1, sb, v1 + off, sa, N0, k, N1, off + 256);
        if (off == 512) { WAIT_BAR(6); }
      }
    } else {
#pragma unroll
      for (int ks = 0; ks < 8; ++ks) P_DMA(ks);
    }
    p0 = prime(S[0], mgt, L_FAR + qi * 256, 256);
    p1 = prime(S[1], mgt, L_FAR + (3 - qi) * 256, 256);
    LBAR();
    bool redo[2];
#pragma unroll
    for (int r = 0; r < 2; ++r) {
      const int qt = r ? 3 - qi : qi;
      f32x16& o = r ? o1 : o0;
#pragma unroll
      for (int G = 0; G < 4; ++G) P_DMA2(3 + r, G);
      if (mt > 1) {
        Strm F = S[r];
        F.kh += L_FAR + qt * 256; F.k4 += L_FAR + qt * 256;
        PIN(F.kh); PIN(F.k4);
        run_tiles4(o, F, r ? p1 : p0, VPTR(r, L_FAR) + qt * 256, 4 - qt);
      }
      const float l = __uint_as_float(__builtin_amdgcn_permlane32_swap(__float_as_uint(o[8]), __float_as_uint(o[8]), false, false)[1]);
      const float rl = __builtin_amdgcn_rcpf(l);
      float of[12];
#pragma unroll
      for (int i = 0; i < 12; ++i) of[i] = o[i] * rl;
      const unsigned dv = r ? (unsigned)((3 - 2 * qi) * 40960 + 256) : 0u;
      store_o(ro, v16 + dv, hh ? OOB : v8 + dv - (r ? 128 : 0), of);
      const float a8 = fmax3(fmax3(__builtin_fabsf(o[0]), __builtin_fabsf(o[1]), __builtin_fabsf(o[2])),
                             fmax3(__builtin_fabsf(o[3]), __builtin_fabsf(o[4]), __builtin_fabsf(o[5])),
                             __builtin_elementwise_maximum(__builtin_fabsf(o[6]), __builtin_fabsf(o[7])));
      const float am = fmax3(fmax3(a8, __builtin_fabsf(o[8]), __builtin_fabsf(o[9])), __builtin_fabsf(o[10]), __builtin_fabsf(o[11]));
      redo[r] = !__all(l >= 0x1p-80f && l <= 0x1p100f && am <= 0x1p126f && a8 >= 0x1p-96f);
    }
    if (redo[0] || redo[1]) *(LAS unsigned*)(smw + L_FB) = 1u;
    WAIT_BAR(0);
    if (__builtin_expect(__builtin_amdgcn_readfirstlane(*(const volatile LAS unsigned*)(sm + L_FB)) != 0u, 0)) {
      HALO(mt - nb, nb ? L_NEAR : L_LOC, pg);
      WAIT_BAR(0);
#pragma unroll
      for (int r = 0; r < 2; ++r) {
        if (!redo[r]) continue;
        const int qt = r ? 3 - qi : qi, w0 = mt >= 2 ? qt : mt == 1 ? 4 : 8;
        f32x16 o;
        float m = -INFINITY;
#pragma unroll 1
        for (int pass = 0; pass < 2; ++pass) {
          o = (f32x16){};
#pragma unroll 1
          for (int w = w0; w <= qt + 8; ++w) {
            const int seg = w < 4 ? L_FAR : w < 8 ? L_NEAR : L_LOC, off = (w & 3) * 256;
            f32x16 c;
#pragma unroll
            for (int e = 0; e < 16; ++e) c[e] = w == qt + 8 ? mle[e] : w == qt ? mgt[e] : 0.f;
            f32x16 s = qk_tile(S[r].kh + seg + off, S[r].k4 + seg + off, S[r].q0, S[r].q1, c);
            if (pass == 0) {
#pragma unroll
              for (int e = 0; e < 16; e += 2) m = fmax3(m, s[e], s[e + 1]);
            } else {
#pragma unroll
              for (int e = 0; e < 16; ++e) s[e] -= m;
              o = pv_tile(VPTR(r, seg) + off, s, o);
            }
          }
          auto rr = __builtin_amdgcn_permlane32_swap(__float_as_uint(m), __float_as_uint(m), false, false);
          m = __builtin_elementwise_maximum(__uint_as_float(rr[0]), __uint_as_float(rr[1]));
        }
        const float rl = __builtin_amdgcn_rcpf(__uint_as_float(__builtin_amdgcn_permlane32_swap(__float_as_uint(o[8]), __float_as_uint(o[8]), false, false)[1]));
        float of[12];
#pragma unroll
        for (int i = 0; i < 12; ++i) of[i] = o[i] * rl;
        const unsigned dv = r ? (unsigned)((3 - 2 * qi) * 40960 + 256) : 0u;
        store_o(ro, v16 + dv, hh ? OOB : v8 + dv - (r ? 128 : 0), of);
      }
      LBAR();
#pragma unroll 1
      for (int G = 0; G < 4; ++G) {
#pragma unroll
        for (int u = 0; u < 5; ++u) P_DMA2(u, G);
      }
      WAIT_BAR(0);
    }
  } else {
    WFrag wf = load_wfrag(sm + c0, sm + c1, hh, lane);
    PIN(wf.t0k0); PIN(wf.t1k0); PIN(wf.t0k1); PIN(wf.t1k1);
    LBAR();
    const LAS char* xp = sm + c2 + hh * 2048;
    const LAS char* x4 = sm + c2 + 4096;
    const HRow rk0 = hh_read(xp + L_LOC, x4 + L_LOC), rk1 = hh_read(xp + L_LOC + 256, x4 + L_LOC + 256);
    const HRow rq0 = hh_read(xp + L_Q, x4 + L_Q), rq1 = hh_read(xp + L_Q + 256, x4 + L_Q + 256);
    const HProj hk0 = hh_proj(wf, rk0);
    bf16x8 vf[4];
#pragma unroll
    for (int ks = 0; ks < 4; ++ks) vf[ks] = cat8(trread(sm + c5 + ks * 128), trread(sm + c5 + ks * 128 + 32));
#pragma unroll
    for (int ks = 12; ks < 18; ++ks) P_DMA(ks);
    SB();
    f32x16 d0 = {}, d1 = {};
    HProj hq0, hq1;
    {
      unsigned ph[10];
      HProj hk1 = hh_proj(wf, rk1);
      hh_soft(hk0, ph);
#pragma unroll
      for (int i = 0; i < 5; ++i) *(LAS u32x2*)(smw + c3 + 8 * i) = (u32x2){ph[2 * i], ph[2 * i + 1]};
      PIN(hk1.p0); PIN(hk1.p1);
      mix_mfma<4, 8>();
      SB();
      bf16x8 pf[4];
#pragma unroll
      for (int i = 0; i < 4; ++i) pf[i] = cat8(trread(sm + c4 + (i >> 1) * 16 * 88 + (i & 1) * 64), trread(sm + c4 + ((i >> 1) * 16 + 4) * 88 + (i & 1) * 64));
      P_DMA(18); P_DMA(19);
      SB();
      hq0 = hh_proj(wf, rq0);
      d0 = MFMA32(pf[0], vf[0], d0); d1 = MFMA32(pf[1], vf[0], d1);
      d0 = MFMA32(pf[2], vf[1], d0); d1 = MFMA32(pf[3], vf[1], d1);
      hh_soft(hk1, ph);
#pragma unroll
      for (int i = 0; i < 5; ++i) *(LAS u32x2*)(smw + c3 + 32 * 88 + 8 * i) = (u32x2){ph[2 * i], ph[2 * i + 1]};
      PIN(hq0.p0); PIN(hq0.p1); PIN(d0); PIN(d1);
      mix_mfma<8, 8>();
      SB();
#pragma unroll
      for (int i = 0; i < 4; ++i) pf[i] = cat8(trread(sm + c4 + (32 + (i >> 1) * 16) * 88 + (i & 1) * 64), trread(sm + c4 + (36 + (i >> 1) * 16) * 88 + (i & 1) * 64));
      d0 = MFMA32(pf[0], vf[2], d0); d1 = MFMA32(pf[1], vf[2], d1);
      d0 = MFMA32(pf[2], vf[3], d0); d1 = MFMA32(pf[3], vf[3], d1);
    }
    *(LAS f32x4*)(smw + c6) = (f32x4){d0[0], d0[1], d0[2], d0[3]};
    *(LAS f32x4*)(smw + c6 + 32) = (f32x4){d0[4], d0[5], d0[6], d0[7]};
    *(LAS f32x4*)(smw + c6 + 64) = (f32x4){d0[8], d0[9], d0[10], d0[11]};
    *(LAS f32x4*)(smw + c6 + 96) = (f32x4){d0[12], d0[13], d0[14], d0[15]};
    *(LAS f32x4*)(smw + c6 + 128) = (f32x4){d1[0], d1[1], d1[2], d1[3]};
    SB();
    hq1 = hh_proj(wf, rq1);
    PIN(hq1.p0); PIN(hq1.p1);
    LBAR();
    const rsrc_t rkv = mkrsrc(ws + WS_KVP + nt * 16 * 7168, 16 * 7168);
    {
      const int e = (wid & 1) * 64 + lane;
      const LAS char* o = sm + A_PHI + (wid & ~1) * 5632 + e * 32;
      const f32x4 a = *(const LAS f32x4*)o + *(const LAS f32x4*)(o + 5632), b = *(const LAS f32x4*)(o + 16) + *(const LAS f32x4*)(o + 5648);
      st16c(rkv, e < 112 ? (unsigned)(mt * 7168 + hl * 1792 + e * 16) : OOB, (u32x4){pk2(a[0], a[1]), pk2(a[2], a[3]), pk2(b[0], b[1]), pk2(b[2], b[3])});
    }
#pragma unroll
    for (int ks = 0; ks < 6; ++ks) P_DMA(ks);
    SB();
    unsigned phq[2][10];
    hh_soft(hq0, phq[0]);
#pragma unroll
    for (int i = 0; i < 10; ++i) PIN(phq[0][i]);
    WAIT_BAR(6);
    if (wid == 0) st8c(rg, vf1, gc);
    if (tid >= 256) *(LAS u32x2*)(smw + A_KV + ((tid - 256) >> 1) * 112 + (tid & 1) * 48 + 40) = (u32x2){0u, 0u};
    hh_soft(hq1, phq[1]);
#pragma unroll
    for (int i = 0; i < 10; ++i) PIN(phq[1][i]);
    int kd0, kd1;
    {
      const int h4 = tid / 112, e = tid - h4 * 112, d = e / 5, oc = e - d * 5;
      kd0 = e < 105 ? A_KV + h4 * 3584 + d * 112 + oc * 16 + (oc > 2 ? 8 : 0) : L_S;
      kd1 = kd0 + (oc == 2 ? 16 : 8);
      PIN(kd0); PIN(kd1);
    }
    wait_flags(rg, (unsigned)(WS_IN + me * 256 + fr * 8), true, tag);
    if (wid < 7) {
      u32x4 p[16];
#pragma unroll
      for (int i = 0; i < 16; ++i) p[i] = __builtin_amdgcn_raw_buffer_load_b128(rkv, tid * 16, i * 7168, 16);
      f32x2 lo[2] = {}, hi[2] = {};
#pragma unroll
      for (int i = 0; i < 16; ++i)
#pragma unroll
        for (int u = 0; u < 2; ++u) {
          lo[u] += (f32x2){__uint_as_float(p[i][2 * u] << 16), __uint_as_float(p[i][2 * u + 1] << 16)};
          hi[u] += (f32x2){__uint_as_float(p[i][2 * u] & 0xFFFF0000u), __uint_as_float(p[i][2 * u + 1] & 0xFFFF0000u)};
        }
      *(LAS u32x2*)(smw + kd0) = (u32x2){pk2(lo[0][0], hi[0][0]), pk2(lo[0][1], hi[0][1])};
      *(LAS u32x2*)(smw + kd1) = (u32x2){pk2(lo[1][0], hi[1][0]), pk2(lo[1][1], hi[1][1])};
    }
#pragma unroll
    for (int ks = 6; ks < 12; ++ks) P_DMA(ks);
    LBAR();
    const LAS char* kp = sm + A_KV + hl * 3584 + (r32 == 24 ? 20 : r32) * 112 + hh * 48;
    const bf16x8 f0 = *(const LAS bf16x8*)(kp), f1 = *(const LAS bf16x8*)(kp + 16), f2 = *(const LAS bf16x8*)(kp + 32);
    f32x16 n[2];
#pragma unroll
    for (int t = 0; t < 2; ++t) n[t] = MFMA32(f0, asfrag((u32x4){phq[t][0], phq[t][1], phq[t][2], phq[t][3]}), (f32x16){});
#pragma unroll
    for (int t = 0; t < 2; ++t) n[t] = MFMA32(f1, asfrag((u32x4){phq[t][4], phq[t][5], phq[t][6], phq[t][7]}), n[t]);
#pragma unroll
    for (int t = 0; t < 2; ++t) n[t] = MFMA32(f2, asfrag((u32x4){phq[t][8], phq[t][9], 0u, 0u}), n[t]);
#pragma unroll
    for (int t = 0; t < 2; ++t) {
      const float den = rbf(rbf(hh ? n[t][8] : n[t][12]) + 1e-6f);
      const float r = __builtin_amdgcn_rcpf(den);
      float o[12];
#pragma unroll
      for (int i = 0; i < 12; ++i) o[i] = rbf(n[t][i]) * r;
      store_o(ro, v16 + t * 40960, hh ? OOB : v8 + t * 40960, o);
    }
    WAIT_BAR(0);
  }
  {
    if (wid == 0) st8c(rg, vf2, gc);
    int oa[3];
    unsigned og[3];
#pragma unroll
    for (int i = 0; i < 3; ++i) {
      const int c = min(lane + 64 * i, 160), row = c / 10, ch = c - row * 10;
      oa[i] = wid * 2816 + row * 176 + ch * 16;
      og[i] = (unsigned)(row * 2560 + ch * 16);
      PIN(oa[i]); PIN(og[i]);
    }
    const int pm = wid & 3, kh = wid >> 2;
    wait_flags(rg, (unsigned)(WS_IN + me * 256 + 128 + (4 * kh + (lane & 3)) * 8), true, tag);
    const rsrc_t rf = mkrsrc(ws + WS_O + (unsigned)((br * 128 + mt3 * 8 + 2 * pm) * 20 + 10 * kh) * 1024u, 30 * 1024);
    const unsigned vk = lane * 16 + kh * 20480, vh = lane * 16 + (1 - kh) * 20480;
    u32x4 xf[10][2];
    const unsigned vk8 = lane * 8 + kh * 20480, vh8 = lane * 8 + (1 - kh) * 20480;
#define P_LD1(v, v8_, tau) ((tau) % 5 == 2 ? __builtin_bit_cast(u32x4, frag2(__builtin_amdgcn_raw_buffer_load_b64(rf, v8_, (tau) * 1024, 16), \
                                                                             __builtin_amdgcn_raw_buffer_load_b64(rf, v8_, (tau) * 1024 + 512, 16))) \
                                           : __builtin_amdgcn_raw_buffer_load_b128(rf, v, (tau) * 1024, 16))
#define P_LD(tau) do { xf[tau][0] = P_LD1(vk, vk8, tau); xf[tau][1] = P_LD1(vh, vh8, tau); } while (0)
    P_LD(0); P_LD(1); P_LD(2);
    const LAS char* wb = sm + P_W + kh * 10240 + (16 * (g >> 1) + 4 * (g & 1) + q4) * 160 + 8 * p4;
    const LAS char* wb2 = wb + 12 * 5120;
    f32x4 pc[2][5] = {};
    bf16x8 wf[2][5];
#pragma unroll
    for (int j = 0; j < 5; ++j) wf[0][j] = cat8(trread(wb + j * 32), trread(wb + 8 * 160 + j * 32));
#pragma unroll
    for (int tau = 0; tau < 10; ++tau) {
      SB();
      if (tau < 7) P_LD(tau + 3);
#pragma unroll
      for (int j = 0; j < 5; ++j) {
        if (tau < 9) {
          const int blk = ((tau + 1) % 5) * 4 + (tau + 1) / 5;
          const LAS char* wp = (blk >= 12 ? wb2 : wb) + (blk >= 12 ? blk - 12 : blk) * 5120 + j * 32;
          wf[(tau + 1) & 1][j] = cat8(trread(wp), trread(wp + 8 * 160));
        }
        pc[1][j] = MFMA16(wf[tau & 1][j], asfrag(xf[tau][1]), pc[1][j]);
        if (tau < 9) pc[0][j] = MFMA16(wf[tau & 1][j], asfrag(xf[tau][0]), pc[0][j]);
        SB();
      }
    }
#pragma unroll
    for (int j = 0; j < 5; ++j) {
      pc[0][j] = MFMA16(wf[1][j], asfrag(xf[9][0]), pc[0][j]);
      *(LAS f32x4*)(smw + P_X + (wid * 5 + j) * 1024 + lane * 16) = pc[1][j];
    }
    LBAR();
    if (tid == 0) st8c(rg, me * 16, tag);
    LAS char* ow = smw + wid * 2816 + fr * 176 + g * 8;
#pragma unroll
    for (int j = 0; j < 5; ++j) {
      const f32x4 a = pc[0][j] + *(const LAS f32x4*)(sm + P_X + ((wid ^ 4) * 5 + j) * 1024 + lane * 16);
      *(LAS u32x2*)(ow + j * 32) = (u32x2){pk2(a[0], a[1]), pk2(a[2], a[3])};
    }
    const rsrc_t rout = mkrsrc((char*)out + (size_t)(mt3 * 128 + 32 * pm + 16 * kh) * 2560 + br * 1280 + nt3 * 160, 15 * 2560 + 160);
#pragma unroll
    for (int i = 0; i < 3; ++i) __builtin_amdgcn_raw_buffer_store_b128(*(const LAS u32x4*)(sm + oa[i]), rout, og[i], 0, 0);
  }
}

extern "C" void kernel_launch(void* const* d_in, const int* in_sizes, int n_in, void* d_out, int out_size, void* d_ws,
                              size_t ws_size, hipStream_t stream) {
  k1_fused<<<256, 512, 0, stream>>>((const __hip_bfloat16*)d_in[0], (const __hip_bfloat16*)d_in[1], (const __hip_bfloat16*)d_in[4],
                                    (const __hip_bfloat16*)d_in[5], (const __hip_bfloat16*)d_in[2], (const __hip_bfloat16*)d_in[3],
                                    (const __hip_bfloat16*)d_in[6], (char*)d_ws, (__hip_bfloat16*)d_out);
}
```

```cpp
#include <hip/hip_runtime.h>
#include <hip/hip_bf16.h>

typedef short bf16x8 __attribute__((ext_vector_type(8)));
typedef short s16x4 __attribute__((ext_vector_type(4)));
typedef float f32x2 __attribute__((ext_vector_type(2)));
typedef float f32x4 __attribute__((ext_vector_type(4)));
typedef float f32x16 __attribute__((ext_vector_type(16)));
typedef unsigned u32x2 __attribute__((ext_vector_type(2)));
typedef unsigned u32x4 __attribute__((ext_vector_type(4)));
typedef __bf16 bf16x2_t __attribute__((ext_vector_type(2)));
typedef __amdgpu_buffer_rsrc_t rsrc_t;
#define LAS __attribute__((address_space(3)))
#define DI __device__ __forceinline__

constexpr int SEQ = 2048, DM = 640;
constexpr unsigned WS_G = 0;
constexpr unsigned WS_IN = 4096;
constexpr unsigned WS_KVH = WS_IN + 256 * 256;
constexpr unsigned WS_KVP = WS_KVH + 128 * 40960;
constexpr unsigned WS_O = WS_KVP + 128 * 7168;
constexpr unsigned WS_END = WS_O + 2 * SEQ * DM * 2;
constexpr unsigned OOB = 0x7FFF0000u;

DI unsigned pk2(float lo, float hi) {
  bf16x2_t r = __builtin_convertvector((f32x2){lo, hi}, bf16x2_t);
  return __builtin_bit_cast(unsigned, r);
}
DI float rbf(float x) { return __builtin_bit_cast(float, pk2(0.f, x)); }
DI float fmax3(float a, float b, float c) { return __builtin_elementwise_maximum(__builtin_elementwise_maximum(a, b), c); }
DI s16x4 trread(const LAS char* p) { return __builtin_amdgcn_ds_read_tr16_b64_v4i16((LAS s16x4*)p); }
DI bf16x8 cat8(s16x4 a, s16x4 b) { return __builtin_shufflevector(a, b, 0, 1, 2, 3, 4, 5, 6, 7); }
DI bf16x8 asfrag(u32x4 v) { return __builtin_bit_cast(bf16x8, v); }
DI bf16x8 frag2(u32x2 a, u32x2 b) { return asfrag((u32x4){a[0], a[1], b[0], b[1]}); }
DI rsrc_t mkrsrc(const void* p, unsigned bytes) { return __builtin_amdgcn_make_buffer_rsrc((void*)p, (short)0, (int)bytes, 0x00020000); }
DI void dma16(rsrc_t rs, unsigned lds, unsigned voff, unsigned soff) {
  asm volatile("s_mov_b32 m0, %0\n\ts_nop 0\n\tbuffer_load_dwordx4 %1, %2, %3 offen lds"
               :: "s"(lds), "v"(voff), "s"(rs), "s"(soff) : "memory");
}
DI void dma16c(rsrc_t rs, unsigned lds, unsigned voff, unsigned soff) {
  asm volatile("s_mov_b32 m0, %0\n\ts_nop 0\n\tbuffer_load_dwordx4 %1, %2, %3 offen sc1 lds"
               :: "s"(lds), "v"(voff), "s"(rs), "s"(soff) : "memory");
}
DI void touch(rsrc_t rs, unsigned lds, unsigned voff, unsigned soff) {
  asm volatile("s_mov_b32 m0, %0\n\ts_nop 0\n\tbuffer_load_dword %1, %2, %3 offen lds"
               :: "s"(lds), "v"(voff), "s"(rs), "s"(soff) : "memory");
}
DI void touchc(rsrc_t rs, unsigned lds, unsigned voff) {
  asm volatile("s_mov_b32 m0, %0\n\ts_nop 0\n\tbuffer_load_dword %1, %2, 0 offen sc1 lds" :: "s"(lds), "v"(voff), "s"(rs) : "memory");
}
template <unsigned LO, unsigned HI>
DI void dma16m(rsrc_t rs, unsigned lds, unsigned voff, unsigned soff) {
  asm volatile("s_mov_b32 m0, %0\n\ts_mov_b32 exec_lo, %4\n\ts_mov_b32 exec_hi, %5\n\tbuffer_load_dwordx4 %1, %2, %3 offen lds\n\ts_mov_b64 exec, -1"
               :: "s"(lds), "v"(voff), "s"(rs), "s"(soff), "i"(LO), "i"(HI) : "memory");
}
DI void st16c(rsrc_t rs, unsigned voff, u32x4 v) { __builtin_amdgcn_raw_buffer_store_b128(v, rs, voff, 0, 16); }
DI void st8c(rsrc_t rs, unsigned voff, u32x2 v) { __builtin_amdgcn_raw_buffer_store_b64(v, rs, voff, 0, 16); }
DI u32x2 lds8(const LAS char* p) { return *(const volatile LAS u32x2*)p; }
DI u32x2 next_tag(u32x2 g) { return (u32x2){g[0] * 1664525u + 1013904223u, g[1] * 22695477u + 2654435761u}; }
#define MFMA16(a, b, c) __builtin_amdgcn_mfma_f32_16x16x32_bf16((a), (b), (c), 0, 0, 0)
#define MFMA32(a, b, c) __builtin_amdgcn_mfma_f32_32x32x16_bf16((a), (b), (c), 0, 0, 0)
#define MFMA32K8(a, b, c) __builtin_amdgcn_mfma_f32_32x32x8bf16_1k(__builtin_bit_cast(s16x4, a), __builtin_bit_cast(s16x4, b), (c), 0, 0, 0)
#define WAIT_BAR(N) asm volatile("s_waitcnt vmcnt(" #N ") lgkmcnt(0)\n\ts_barrier" ::: "memory")
#define LBAR() asm volatile("s_waitcnt lgkmcnt(0)\n\ts_barrier" ::: "memory")
#define SB() __builtin_amdgcn_sched_barrier(0)

constexpr float LOG2E = 1.4426950408889634f;

struct WFrag { bf16x8 t0k0, t1k0; u32x2 t0k1, t1k1; };
DI WFrag load_wfrag(const LAS char* a0  , const LAS char* a1  , int hh, int lane) {
  u32x2 r0 = __builtin_bit_cast(u32x2, trread(a0)), r1 = __builtin_bit_cast(u32x2, trread(a0 + 160));
  u32x2 r2 = __builtin_bit_cast(u32x2, trread(a1)), r3 = __builtin_bit_cast(u32x2, trread(a1 + 160));
  u32x2 r4 = __builtin_bit_cast(u32x2, trread(a0 + 640)), r5 = __builtin_bit_cast(u32x2, trread(a1 + 640));
  const unsigned sg = (lane & 4) ? 0x80008000u : 0u;
  const unsigned km = hh ? 0u : 0xFFFFFFFFu;
  WFrag w;
  w.t0k0 = asfrag((u32x4){r0[0] ^ sg, r0[1] ^ sg, r1[0] ^ sg, r1[1] ^ sg});
  w.t1k0 = asfrag((u32x4){r2[0] ^ sg, r2[1] ^ sg, r3[0] ^ sg, r3[1] ^ sg});
  w.t0k1 = (u32x2){(r4[0] ^ sg) & km, (r4[1] ^ sg) & km};
  w.t1k1 = (u32x2){(r5[0] ^ sg) & km, (r5[1] ^ sg) & km};
  return w;
}
struct HProj { f32x16 p0, p1; };
struct HRow { bf16x8 x0; u32x2 x1; };
DI HRow hh_read(const LAS char* p, const LAS char* p4) { HRow r; r.x0 = frag2(lds8(p), lds8(p + 1024)); r.x1 = lds8(p4); return r; }
DI HProj hh_proj(const WFrag& w, const HRow& x) {
  const f32x16 z = {};
  HProj h;
  h.p0 = MFMA32(w.t0k0, x.x0, z);
  h.p1 = MFMA32(w.t1k0, x.x0, z);
  h.p0 = MFMA32K8(w.t0k1, x.x1, h.p0);
  h.p1 = MFMA32K8(w.t1k1, x.x1, h.p1);
  return h;
}
template <int N, int NV> DI void mix_mfma() {
#pragma unroll
  for (int i = 0; i < N; ++i) { __builtin_amdgcn_sched_group_barrier(0x8, 1, 0); __builtin_amdgcn_sched_group_barrier(0x402, NV, 0); }
}
DI void hh_soft(const HProj& h, unsigned (&ph)[10]) {
  f32x2 v[10];
#pragma unroll
  for (int i = 0; i < 8; ++i) v[i] = (f32x2){rbf(h.p0[2 * i]), rbf(h.p0[2 * i + 1])};
#pragma unroll
  for (int i = 0; i < 2; ++i) v[8 + i] = (f32x2){rbf(h.p1[2 * i]), rbf(h.p1[2 * i + 1])};
  float m = __builtin_elementwise_maximum(v[0][0], v[0][1]);
#pragma unroll
  for (int i = 1; i < 10; ++i) m = fmax3(m, v[i][0], v[i][1]);
  const float nm = -m * LOG2E;
#pragma unroll
  for (int i = 0; i < 10; ++i) {
    v[i] = __builtin_elementwise_fma(v[i], (f32x2){LOG2E, LOG2E}, (f32x2){nm, nm});
    v[i] = (f32x2){__builtin_amdgcn_exp2f(v[i][0]), __builtin_amdgcn_exp2f(v[i][1])};
  }
  const f32x2 s = (((v[0] + v[1]) + (v[2] + v[3])) + ((v[4] + v[5]) + (v[6] + v[7]))) + (v[8] + v[9]);
  const float r = __builtin_amdgcn_rcpf(s[0] + s[1]);
#pragma unroll
  for (int i = 0; i < 10; ++i) { v[i] *= (f32x2){r, r}; ph[i] = pk2(v[i][0], v[i][1]); }
}

constexpr float SC2 = 0.22360679774997896f * 1.4426950408889634f;

DI unsigned o_off(int h, int srow) { return (unsigned)((srow >> 4) * 20480 + (srow & 15) * 16 + 5 * (h >> 3) * 1024 + ((h >> 1) & 3) * 256); }
DI void store_o(rsrc_t ro, unsigned v16, unsigned v8, const float (&o)[12]) {
  st16c(ro, v16, (u32x4){pk2(o[0], o[1]), pk2(o[2], o[3]), pk2(o[4], o[5]), pk2(o[6], o[7])});
  st8c(ro, v8, (u32x2){pk2(o[8], o[9]), pk2(o[10], o[11])});
}
#define PIN(x) asm volatile("" : "+v"(x))
typedef __attribute__((address_space(1))) unsigned long long gu64;
DI void put_word(char* ws, unsigned off, u32x2 v) {
  if (off != OOB) __hip_atomic_store((gu64*)(ws + off), __builtin_bit_cast(unsigned long long, v), __ATOMIC_RELAXED, __HIP_MEMORY_SCOPE_AGENT);
}
DI void wait_flags(char* ws, unsigned off, bool need, u32x2 tag) {
#pragma unroll 1
  for (int spin = 0; spin < (1 << 20); ++spin) {
    const unsigned long long f = __hip_atomic_load((gu64*)(ws + off), __ATOMIC_RELAXED, __HIP_MEMORY_SCOPE_AGENT);
    if (__all(!need || f == __builtin_bit_cast(unsigned long long, tag))) break;
    __builtin_amdgcn_s_sleep(1);
  }
  __builtin_amdgcn_fence(__ATOMIC_ACQUIRE, "wavefront");
  asm volatile("" ::: "memory");
}

constexpr unsigned CODE_BYTES = 28 * 1024;
constexpr int G_STAGE = 16384 + 30720;
constexpr int KP = 1024, KH = 5 * KP, VP = 1056, VH = 5376, SEG_V = 4 * KH, SEG = SEG_V + 4 * VH;
constexpr int L_LOC = 512, L_Q = L_LOC + SEG, L_NEAR = L_Q + 20480, L_FAR = L_NEAR + SEG;
constexpr int A_PHI = L_FAR, A_KV = L_FAR;
constexpr int L_ONE = L_FAR + 8 * 5632 + 160, L_W = L_ONE + 1024 + 96, L_G = L_W + 4096, L_T = L_G + 1024, L_FB = L_T + 256, L_S = L_FB + 16, K_LDS = L_S + 1024;
constexpr int P_W = 1536, P_X = L_FAR;
static_assert(3 * G_STAGE + 64 <= L_W && K_LDS <= 160 * 1024 - 2048 && P_W + 20 * 5120 + 64 <= L_FAR && P_W + 8 * 5120 == L_Q && P_W + 12 * 5120 == L_NEAR, "LDS map");
static_assert(L_LOC % 256 == 0 && SEG % 256 == 0 && SEG_V % 256 == 0 && VH % 256 == 0 && L_NEAR % 256 == 0 && L_ONE % 256 == 160 && L_W % 16 == 0, "banks");

DI f32x16 qk_tile(const LAS char* kh, const LAS char* k4, bf16x8 q0, u32x2 q1, f32x16 c) {
  const u32x2 a = lds8(kh), b = lds8(kh + KP), d = lds8(k4);
  c = MFMA32(frag2(a, b), q0, c);
  return MFMA32K8(d, q1, c);
}
DI f32x16 pv_tile(const LAS char* vp, const f32x16& s, f32x16 o) {
  const bf16x8 v0 = cat8(trread(vp), trread(vp + 64)), v1 = cat8(trread(vp + 128), trread(vp + 192));
  unsigned pw[8];
#pragma unroll
  for (int i = 0; i < 8; ++i) pw[i] = pk2(__builtin_amdgcn_exp2f(s[2 * i]), __builtin_amdgcn_exp2f(s[2 * i + 1]));
  o = MFMA32(v0, asfrag((u32x4){pw[0], pw[1], pw[2], pw[3]}), o);
  return MFMA32(v1, asfrag((u32x4){pw[4], pw[5], pw[6], pw[7]}), o);
}
struct Strm { const LAS char* kh; const LAS char* k4; bf16x8 q0; u32x2 q1; };
struct KFr { u32x2 a, b, d; };
DI KFr kread(const Strm& s, int off) { KFr k; k.a = lds8(s.kh + off); k.b = lds8(s.kh + off + KP); k.d = lds8(s.k4 + off); return k; }
DI void tile_step(f32x16& o, const f32x16& sc, const LAS char* vp, f32x16& sn, const Strm& n, KFr& k, const Strm& ks, int koff) {
  const f32x16 z = {};
  const bf16x8 v0 = cat8(trread(vp), trread(vp + 64)), v1 = cat8(trread(vp + 128), trread(vp + 192));
  float e[16];
#pragma unroll
  for (int i = 0; i < 3; ++i) e[i] = __builtin_amdgcn_exp2f(sc[i]);
  asm volatile("" : "+v"(e[0]), "+v"(e[1]), "+v"(e[2]));
  SB();
  sn = MFMA32(frag2(k.a, k.b), n.q0, z);
#pragma unroll
  for (int i = 3; i < 7; ++i) e[i] = __builtin_amdgcn_exp2f(sc[i]);
  asm volatile("" : "+v"(e[3]), "+v"(e[4]), "+v"(e[5]), "+v"(e[6]));
  SB();
  sn = MFMA32K8(k.d, n.q1, sn);
  k = kread(ks, koff);
#pragma unroll
  for (int i = 7; i < 10; ++i) e[i] = __builtin_amdgcn_exp2f(sc[i]);
  u32x4 p0 = {pk2(e[0], e[1]), pk2(e[2], e[3]), pk2(e[4], e[5]), pk2(e[6], e[7])};
  asm volatile("" : "+v"(e[8]), "+v"(e[9]), "+v"(p0));
  SB();
  o = MFMA32(v0, asfrag(p0), o);
#pragma unroll
  for (int i = 10; i < 16; ++i) e[i] = __builtin_amdgcn_exp2f(sc[i]);
  o = MFMA32(v1, asfrag((u32x4){pk2(e[8], e[9]), pk2(e[10], e[11]), pk2(e[12], e[13]), pk2(e[14], e[15])}), o);
  SB();
}
struct Pipe { f32x16 s; KFr k; };
DI Pipe prime(const Strm& s, const f32x16& c0, int off, int step) {
  Pipe p;
  p.k = kread(s, off + step);
  p.s = qk_tile(s.kh + off, s.k4 + off, s.q0, s.q1, c0);
  return p;
}
DI void run_tiles(f32x16& o, const Strm& s, Pipe& p, const LAS char* vp, int off, int step, int n) {
  f32x16 sb;
#pragma unroll 1
  for (;;) {
    tile_step(o, p.s, vp, sb, s, p.k, s, off + 2 * step);
    if (n == 1) break;
    tile_step(o, sb, vp + step, p.s, s, p.k, s, off + 3 * step);
    if (n == 2) break;
    n -= 2; off += 2 * step; vp += 2 * step;
  }
}

DI void run_tiles4(f32x16& o, const Strm& s, Pipe& p, const LAS char* vp, int n) {
  f32x16 sb;
  tile_step(o, p.s, vp, sb, s, p.k, s, 512);
  if (n == 1) return;
  tile_step(o, sb, vp + 256, p.s, s, p.k, s, 768);
  if (n == 2) return;
  tile_step(o, p.s, vp + 512, sb, s, p.k, s, 768);
  if (n == 3) return;
  o = pv_tile(vp + 768, sb, o);
}

__global__ __launch_bounds__(512, 2) void k1_fused(const __hip_bfloat16* __restrict__ xA, const __hip_bfloat16* __restrict__ wA,
                                                   const __hip_bfloat16* __restrict__ xB, const __hip_bfloat16* __restrict__ wB,
                                                   const __hip_bfloat16* __restrict__ wphi, const __hip_bfloat16* __restrict__ woA,
                                                   const __hip_bfloat16* __restrict__ woB, char* __restrict__ ws,
                                                   __hip_bfloat16* __restrict__ out) {
  __shared__ __attribute__((aligned(16))) char smem[K_LDS];
  const LAS char* sm = (const LAS char*)smem;
  LAS char* smw = (LAS char*)smem;
  const unsigned lds0 = (unsigned)(size_t)(LAS char*)smem;
  const int tid = threadIdx.x, lane = tid & 63;
  const int wid = __builtin_amdgcn_readfirstlane(tid >> 6);
  const int xcd = blockIdx.x & 7, idx = blockIdx.x >> 3;
  const int br = idx & 1, sub = idx >> 1;
  const int mt = (xcd >> 2) * 8 + (sub & 7), nt = (xcd & 3) * 2 + (sub >> 3);
  const int mt3 = xcd * 2 + (sub & 1), nt3 = sub >> 1;
  const int me = blockIdx.x;
  const int g = lane >> 4, fr = lane & 15, q4 = fr >> 2, p4 = lane & 3;
  {
    unsigned long long pc;
    asm volatile("s_getpc_b64 %0" : "=s"(pc));
    if (wid == 0) touch(mkrsrc((const void*)pc, CODE_BYTES), lds0 + L_T, lane < 7 ? (unsigned)((lane * 32 + idx) * 128) : OOB, 0);
  }
  asm volatile("" :: "s"(xA), "s"(wphi), "s"(woA), "s"(woB), "s"(ws), "s"(out));
  const char* wg = (const char*)(br ? wB : wA);
  const rsrc_t rx = mkrsrc((const char*)(br ? xB : xA) + (size_t)mt * 128 * 1280, 128 * 1280);
  const rsrc_t rw = mkrsrc(wg + nt * 160, 640 * 3840 - nt * 160);
  const unsigned vx0 = (unsigned)((2 * (lane >> 3)) * 1280 + (((lane & 7) ^ g) * 16));
  const unsigned vx1 = (unsigned)((16 + 2 * (lane >> 3)) * 1280 + (((lane & 7) ^ (4 + g)) * 16));
  const int wr = lane >= 30 ? 1 : 0, wc = lane - 30 * wr, wsg = (wc >= 10) + (wc >= 20);
  const unsigned vw = (unsigned)(wr * 3840 + wsg * 1280 + (wc - 10 * wsg) * 16);
  const unsigned sx = (unsigned)((32 * (wid >> 1) + (wid & 1)) * 1280);
  const unsigned sw = (unsigned)((16 * (wid >> 1) + 4 * (wid & 1)) * 3840);
  const unsigned lx = lds0 + wid * 2048, lw = lds0 + 16384 + wid * 3840;
#define G_DMA(i, bo, xo, wo) do { \
    if ((i) == 0 || (i) == 3) dma16(rx, lx + (bo) + ((i) ? 1024 : 0), (i) ? vx1 : vx0, (xo)); \
    else { const int n_ = (i) < 3 ? (i) - 1 : (i) - 2; \
      dma16m<0xFFFFFFFFu, 0x0FFFFFFFu>(rw, lw + (bo) + n_ * 960, vw, (wo) + ((n_ >> 1) * 8 + (n_ & 1) * 2) * 3840); } } while (0)
#pragma unroll
  for (int i = 0; i < 6; ++i) G_DMA(i, 0, sx, sw);
  const int bi = mt * 8 + nt;
  const rsrc_t rt = (wid & 4) ? mkrsrc(wg, 640 * 3840) : mkrsrc(br ? xB : xA, SEQ * 1280);
  const unsigned tst = (wid & 4) ? 245760u : 128u;
  unsigned vt = (wid & 4) ? (fr < 15 ? (unsigned)((bi * 15 + fr) * 128 + g * 245760) : OOB) : (unsigned)((bi * 16 + fr) * 1280 + g * 128);
  if ((wid & 3) == 2) vt = g == 0 ? vt + 4 * tst : OOB;
  if ((wid & 3) >= 2) touch(rt, lds0 + L_T, vt, tst);
  const rsrc_t rt2 = (wid & 4) ? mkrsrc((wid & 1) ? wA : wB, 640 * 3840) : mkrsrc((wid & 1) ? xA : xB, SEQ * 1280);
  vt = (wid & 4) ? (lane < 60 ? (unsigned)(((bi >> 2) * 60 + lane) * 128) : OOB) : (unsigned)(((bi >> 2) * 64 + lane) * 1280);

  const int wm = wid >> 2, wn = wid & 3;
  const LAS char* xa0 = sm + (64 * wm + fr) * 128 + ((g ^ (fr >> 1)) * 16);
  const LAS char* xa1 = sm + (((64 * wm + fr) * 128 + ((g ^ (fr >> 1)) * 16)) ^ 64);
  const LAS char* wa = sm + 16384 + (16 * (g >> 1) + 4 * (g & 1) + q4) * 480 + 8 * p4 + wn * 128;
  f32x4 acc[4][4] = {};
#pragma unroll
  for (int i = 0; i < 4; ++i) asm volatile("" : "+v"(acc[i][0]), "+v"(acc[i][1]), "+v"(acc[i][2]), "+v"(acc[i][3]));
  int eb[4];
#pragma unroll
  for (int j = 0; j < 4; ++j) {
    const int qd = 16 * wn + 4 * j + g, r = qd - 40;
    const int v = L_LOC + SEG_V + r * VP + ((r * 13) >> 6) * 96;
    eb[j] = (qd < 20 ? L_Q + qd * 1024 : qd < 40 ? L_LOC + (qd - 20) * KP : qd < 60 ? v : L_S) + wm * 512 + fr * 16;
    asm volatile("" : "+v"(eb[j]));
  }
  int cons;
  {
    const int cm = br ? mt + 1 + lane : lane;
    const int c1 = (((((nt & 1) * 8 + (cm & 7)) << 1) | br) << 3) | (((cm >> 3) & 1) * 4 + (nt >> 1));
    const int c2 = (((((lane & 7) * 2 + (mt & 1)) << 1) | br) << 3) | (mt >> 1);
    cons = lane < 16 ? ((cm < 16 && (!br || lane < 2)) ? c1 : -1) : (lane < 24 ? c2 : lane == 63 ? me : -1);
    asm volatile("" : "+v"(cons));
  }
  const int hh = lane >> 5, r32 = lane & 31, b16 = (lane >> 4) & 1, vq = 4 * b16 + p4;
  unsigned vflag = (unsigned)(WS_IN + cons * 256 + (lane >= 16 ? 16 + nt : br ? 1 - lane : mt) * 8);
  unsigned vf1 = (lane < 16 && cons >= 0) ? vflag : OOB, vf2 = (lane >= 16 && lane < 24) ? vflag : OOB;
  const int lq = (lane >= 10) + (lane >= 20) + (lane >= 30);
  unsigned vwo = (unsigned)(lq * 1280 + (lane - 10 * lq) * 16);
  PIN(vf1); PIN(vf2); PIN(vwo);
  const int hs = wid >> 2, qi = hs ? 3 - (wid & 3) : (wid & 3);
  const int hl = wid >> 1, rw0 = 64 * (wid & 1);
  int c0, c1, c2, c3, c4, c5, c6;
  unsigned v16, v8;
  if (br) {
    c0 = hs * KH + hh * 2 * KP + r32 * 8;
    c1 = hs * KH + 4 * KP + r32 * 8;
    c2 = (vq < 5 ? SEG_V + hs * VH + vq * VP : L_ONE) + (4 * hh + q4) * 8;
    c3 = vq < 5 ? -1 : 0;
    c4 = L_Q + hs * 5120 + (32 * qi + r32) * 8;
    c5 = L_Q + (2 + hs) * 5120 + (32 * (3 - qi) + r32) * 8;
    c6 = hh * 2048;
    v16 = o_off(4 * nt + hs, mt * 128 + 32 * qi + r32);
  } else {
    c0 = L_W + hl * 800 + (8 * hh + q4) * 40 + (2 * b16 + (p4 >> 1)) * 8;
    c1 = L_W + hl * 800 + (8 * hh + q4) * 40 + 32;
    c2 = hl * 5120 + (rw0 + r32) * 8;
    c3 = A_PHI + wid * 5632 + r32 * 88 + hh * 40;
    c4 = A_PHI + wid * 5632 + (8 * hh + q4) * 88 + (16 * b16 + 4 * p4) * 2;
    c5 = (vq < 5 ? L_LOC + SEG_V + hl * VH + vq * VP : L_ONE) + (rw0 + 8 * hh + q4) * 8;
    c6 = A_PHI + wid * 5632 + min(r32, 21) * 160 + hh * 16;
    v16 = o_off(4 * nt + hl, mt * 128 + rw0 + r32);
  }
  {
    const int odd = br ? hs : (hl & 1);
    v8 = hh ? OOB : (v16 & ~1023u) + 2048 + 512 * odd + ((v16 & 1023u) >> 1);
    v16 += (hh + 3 * odd) * 1024;
  }
  PIN(c0); PIN(c1); PIN(c2); PIN(c3); PIN(c4); PIN(c5); PIN(c6); PIN(v16); PIN(v8);
  f32x16 mle, mgt;
#pragma unroll
  for (int e = 0; e < 16; ++e) {
    const bool le = (e & 3) + 8 * (e >> 2) + 4 * hh <= r32;
    mle[e] = le ? 0.f : -INFINITY;
    mgt[e] = le ? -INFINITY : 0.f;
  }
  PIN(mle); PIN(mgt);
  const rsrc_t rg = mkrsrc(ws + WS_G, WS_KVH);
  const rsrc_t rph = mkrsrc((const char*)wphi + nt * 3200, br ? 0 : 3200);
  bf16x8 x0[4], x1[4];
  s16x4 w0[4][2], w1[4][2];
#define G_RD(X, W, xa, kk, r) do { \
    if ((r) < 2) W[0][(r)] = trread(wa + ((kk) * 32 + (r) * 8) * 480); \
    else if ((r) < 6) X[(r) - 2] = *(const LAS bf16x8*)(xa + ((r) - 2) * 2048); \
    else if ((r) < 12) W[((r) - 4) >> 1][(r) & 1] = trread(wa + ((kk) * 32 + ((r) & 1) * 8) * 480 + (((r) - 4) >> 1) * 32); } while (0)
#define G_MM(X, W, m) acc[(m) & 3][(m) >> 2] = MFMA16(cat8(W[(m) >> 2][0], W[(m) >> 2][1]), X[(m) & 3], acc[(m) & 3][(m) >> 2])
  if (wid == 0) dma16c(rg, lds0 + L_G, cons >= 0 ? cons * 16 : OOB, 0);
  if ((wid & 3) == 1) {
    dma16(rph, lds0 + L_W + (wid >> 2) * 2048, (unsigned)lane * 16, (wid >> 2) * 2048);
    dma16(rph, lds0 + L_W + (wid >> 2) * 2048 + 1024, (unsigned)lane * 16, (wid >> 2) * 2048 + 1024);
  }
  if (wid == 4) touchc(rg, lds0 + L_T, lane < 2 ? (unsigned)(WS_IN + me * 256 + lane * 128) : OOB);
  asm volatile("s_barrier" ::: "memory");
#pragma unroll
  for (int i = 0; i < 6; ++i) G_DMA(i, G_STAGE, sx + 128, sw + 245760);
  WAIT_BAR(7);
#pragma unroll
  for (int r = 0; r < 12; ++r) G_RD(x0, w0, xa0, 0, r);
  int bo = 2 * G_STAGE;
  int bd = bo;
  unsigned xo = sx + 256, wo = sw + 2 * 245760;
#pragma unroll
  for (int i = 0; i < 3; ++i) G_DMA(i, bo, xo, wo);
#pragma unroll 1
  for (int t = 0; t < 10; ++t) {
    SB();
#pragma unroll
    for (int m = 0; m < 12; ++m) {
      G_MM(x0, w0, m); G_RD(x1, w1, xa1, 1, m);
      if (m % 3 == 2 && m < 9) G_DMA(3 + m / 3, bd, xo, wo);
      SB();
    }
    if (wn < 3) { G_MM(x0, w0, 12); G_MM(x0, w0, 13); G_MM(x0, w0, 14); G_MM(x0, w0, 15); }
    const int d = bo == G_STAGE ? -2 * G_STAGE : G_STAGE;
    xa0 += d; xa1 += d; wa += d;
    bo = bo == 2 * G_STAGE ? 0 : bo + G_STAGE;
    bd = t < 7 ? bo : 0x30000;
    xo = t < 7 ? xo + 128 : OOB;
    wo += 245760;
    WAIT_BAR(6);
    SB();
#pragma unroll
    for (int m = 0; m < 12; ++m) {
      G_MM(x1, w1, m); G_RD(x0, w0, xa0, 0, m);
      if (m % 3 == 2 && m < 9) G_DMA(m / 3, bd, xo, wo);
      SB();
    }
    if (wn < 3) { G_MM(x1, w1, 12); G_MM(x1, w1, 13); G_MM(x1, w1, 14); G_MM(x1, w1, 15); }
    if (t == (bi & 3) + 1 && !br && (wid & 3) >= 2) touch(rt2, lds0 + L_T, vt, (5 + t) * tst);
    if (t == 8 && wid == 3) touch(mkrsrc(br ? woB : woA, 640 * 1280), lds0 + L_T, lane < 50 ? (unsigned)((bi * 50 + lane) * 128) : OOB, 0);
  }
  {
    const rsrc_t rh = mkrsrc(ws + WS_KVH + (mt * 8 + nt) * 40960, 40960);
    const int v0 = (16 * wn + g - 20) * 1024 + wm * 512 + fr * 16;
#pragma unroll
    for (int j = 3; j >= 0; --j) {
      if (br && 4 * wn + j < 5) {
#pragma unroll
        for (int i = 0; i < 4; ++i) acc[i][j] *= SC2;
      }
#pragma unroll
      for (int i = 0; i < 2; ++i) {
        const f32x4 a = acc[2 * i][j], b = acc[2 * i + 1][j];
        const u32x4 ev = (u32x4){pk2(a[0], a[1]), pk2(a[2], a[3]), pk2(b[0], b[1]), pk2(b[2], b[3])};
        if (br && 4 * wn + j >= 5) st16c(rh, (unsigned)(v0 + j * 4096 + i * 256), ev);
        *(LAS u32x4*)(smw + eb[j] + i * 256) = ev;
      }
    }
  }
  if (tid < 128) *(LAS u32x2*)(smw + L_ONE + tid * 8) = (u32x2){0x3F80u, 0u};
  if (tid == 511) *(LAS unsigned*)(smw + L_FB) = 0u;
  const u32x2 gc = next_tag(*(const LAS u32x2*)(sm + L_G + lane * 16));
  const u32x2 tag = {(unsigned)__builtin_amdgcn_readlane(gc[0], 63), (unsigned)__builtin_amdgcn_readlane(gc[1], 63)};
  const char* wog = (const char*)(br ? woB : woA);
  const rsrc_t ro = mkrsrc(ws + WS_O + br * (SEQ * DM * 2), SEQ * DM * 2);
  const rsrc_t rwo = mkrsrc(wog + nt3 * 160, 640 * 1280 - nt3 * 160);
  const int sg = 2 * (wid >> 2) + (wid & 1), st = (wid >> 1) & 1;
#define P_DMA2(w5, G) dma16m<0xFFFFFFFFu, 0xFFu>(rwo, lds0 + P_W + (4 * (w5) + (G)) * 5120 + wid * 640, vwo, \
      (unsigned)(160 * (G) + 4 * (w5) + ((w5) >= 2 ? 8 : 0) + 40 * sg + st * ((w5) == 2 ? 20 : 8)) * 1280u)
#define P_DMA(b) P_DMA2((b) >> 2, (b) & 3)
  if (br) {
    WAIT_BAR(0);
    if (wid == 0) put_word(ws, vf1, gc);
    Strm S[2];
#pragma unroll
    for (int r = 0; r < 2; ++r) {
      S[r].kh = sm + c0 + r * 2 * KH;
      S[r].k4 = sm + c1 + r * 2 * KH;
      const LAS char* qp = sm + (r ? c5 : c4);
      S[r].q0 = frag2(lds8(qp + c6), lds8(qp + c6 + 1024));
      const u32x2 t4 = lds8(qp + 4096);
      S[r].q1 = hh ? (u32x2){0u, 0u} : t4;
    }
#define VPTR(r, seg) (sm + c2 + (((seg) + (r) * 2 * VH) & c3))
#define HALO(tile, seg, pg) do { \
      const rsrc_t rn_ = mkrsrc(ws + WS_KVH + (((tile) >= 0 ? (tile) : 0) * 8 + nt) * 40960 + (pg) * 10240, (tile) >= 0 ? 10240 : 0); \
      const unsigned hd_ = lds0 + (seg) + ((pg) >= 2 ? SEG_V + ((pg) & 1) * 2 * VH : ((pg) & 1) * 2 * KH), hp_ = (pg) >= 2 ? VP : KP; \
      _Pragma("unroll") for (int i = 0; i < 10; ++i) dma16c(rn_, hd_ + i * hp_ + (i >= 5 ? ((pg) >> 1) * 96 : 0), lane * 16, i * 1024); } while (0)
    f32x16 o0 = {}, o1 = {};
    Pipe p0 = prime(S[0], mle, L_LOC + qi * 256, -256), p1 = prime(S[1], mle, L_LOC + (3 - qi) * 256, -256);
    LBAR();
#pragma unroll
    for (int G = 0; G < 4; ++G) P_DMA2(2, G);
    run_tiles(o0, S[0], p0, VPTR(0, L_LOC) + qi * 256, L_LOC + qi * 256, -256, qi + 1);
    const int nb = qi < 2 ? 1 : 0, pg = 2 * hs + (qi & 1);
    if (qi == 0) {
      wait_flags(ws, (unsigned)(WS_IN + me * 256 + 8), mt >= 1, tag);
      HALO(mt - 1, L_NEAR, 2 * hs);
      HALO(mt - 1, L_NEAR, 2 * hs + 1);
    }
    run_tiles(o1, S[1], p1, VPTR(1, L_LOC) + (3 - qi) * 256, L_LOC + (3 - qi) * 256, -256, 4 - qi);
    if (nb) {
      WAIT_BAR(0);
    } else {
      wait_flags(ws, (unsigned)(WS_IN + me * 256), mt >= 2, tag);
      HALO(mt - 2, L_FAR, pg);
      LBAR();
    }
    if (mt > 0) {
      const f32x16 z = {};
      KFr k = kread(S[1], L_NEAR);
      f32x16 sa = qk_tile(S[0].kh + L_NEAR, S[0].k4 + L_NEAR, S[0].q0, S[0].q1, z), sb;
      const LAS char* v0 = VPTR(0, L_NEAR);
      const LAS char* v1 = VPTR(1, L_NEAR);
      Strm N0 = S[0], N1 = S[1];
      N0.kh += L_NEAR; N0.k4 += L_NEAR; N1.kh += L_NEAR; N1.k4 += L_NEAR;
      PIN(N0.kh); PIN(N0.k4); PIN(N1.kh); PIN(N1.k4);
#pragma unroll
      for (int off = 0; off < 1024; off += 256) {
        P_DMA2(0, off >> 8);
        tile_step(o0, sa, v0 + off, sb, N1, k, N0, off + 256);
        P_DMA2(1, off >> 8);
        tile_step(o1, sb, v1 + off, sa, N0, k, N1, off + 256);
        if (off == 512) { WAIT_BAR(6); }
      }
    } else {
#pragma unroll
      for (int ks = 0; ks < 8; ++ks) P_DMA(ks);
    }
    p0 = prime(S[0], mgt, L_FAR + qi * 256, 256);
    p1 = prime(S[1], mgt, L_FAR + (3 - qi) * 256, 256);
    LBAR();
    bool redo[2];
#pragma unroll
    for (int r = 0; r < 2; ++r) {
      const int qt = r ? 3 - qi : qi;
      f32x16& o = r ? o1 : o0;
#pragma unroll
      for (int G = 0; G < 4; ++G) P_DMA2(3 + r, G);
      if (mt > 1) {
        Strm F = S[r];
        F.kh += L_FAR + qt * 256; F.k4 += L_FAR + qt * 256;
        PIN(F.kh); PIN(F.k4);
        run_tiles4(o, F, r ? p1 : p0, VPTR(r, L_FAR) + qt * 256, 4 - qt);
      }
      const float l = __uint_as_float(__builtin_amdgcn_permlane32_swap(__float_as_uint(o[8]), __float_as_uint(o[8]), false, false)[1]);
      const float rl = __builtin_amdgcn_rcpf(l);
      float of[12];
#pragma unroll
      for (int i = 0; i < 12; ++i) of[i] = o[i] * rl;
      const unsigned dv = r ? (unsigned)((3 - 2 * qi) * 40960 + 256) : 0u;
      store_o(ro, v16 + dv, hh ? OOB : v8 + dv - (r ? 128 : 0), of);
      const float a8 = fmax3(fmax3(__builtin_fabsf(o[0]), __builtin_fabsf(o[1]), __builtin_fabsf(o[2])),
                             fmax3(__builtin_fabsf(o[3]), __builtin_fabsf(o[4]), __builtin_fabsf(o[5])),
                             __builtin_elementwise_maximum(__builtin_fabsf(o[6]), __builtin_fabsf(o[7])));
      const float am = fmax3(fmax3(a8, __builtin_fabsf(o[8]), __builtin_fabsf(o[9])), __builtin_fabsf(o[10]), __builtin_fabsf(o[11]));
      redo[r] = !__all(l >= 0x1p-80f && l <= 0x1p100f && am <= 0x1p126f && a8 >= 0x1p-96f);
    }
    if (redo[0] || redo[1]) *(LAS unsigned*)(smw + L_FB) = 1u;
    WAIT_BAR(0);
    if (__builtin_expect(__builtin_amdgcn_readfirstlane(*(const volatile LAS unsigned*)(sm + L_FB)) != 0u, 0)) {
      HALO(mt - nb, nb ? L_NEAR : L_LOC, pg);
      WAIT_BAR(0);
#pragma unroll
      for (int r = 0; r < 2; ++r) {
        if (!redo[r]) continue;
        const int qt = r ? 3 - qi : qi, w0 = mt >= 2 ? qt : mt == 1 ? 4 : 8;
        f32x16 o;
        float m = -INFINITY;
#pragma unroll 1
        for (int pass = 0; pass < 2; ++pass) {
          o = (f32x16){};
#pragma unroll 1
          for (int w = w0; w <= qt + 8; ++w) {
            const int seg = w < 4 ? L_FAR : w < 8 ? L_NEAR : L_LOC, off = (w & 3) * 256;
            f32x16 c;
#pragma unroll
            for (int e = 0; e < 16; ++e) c[e] = w == qt + 8 ? mle[e] : w == qt ? mgt[e] : 0.f;
            f32x16 s = qk_tile(S[r].kh + seg + off, S[r].k4 + seg + off, S[r].q0, S[r].q1, c);
            if (pass == 0) {
#pragma unroll
              for (int e = 0; e < 16; e += 2) m = fmax3(m, s[e], s[e + 1]);
            } else {
#pragma unroll
              for (int e = 0; e < 16; ++e) s[e] -= m;
              o = pv_tile(VPTR(r, seg) + off, s, o);
            }
          }
          auto rr = __builtin_amdgcn_permlane32_swap(__float_as_uint(m), __float_as_uint(m), false, false);
          m = __builtin_elementwise_maximum(__uint_as_float(rr[0]), __uint_as_float(rr[1]));
        }
        const float rl = __builtin_amdgcn_rcpf(__uint_as_float(__builtin_amdgcn_permlane32_swap(__float_as_uint(o[8]), __float_as_uint(o[8]), false, false)[1]));
        float of[12];
#pragma unroll
        for (int i = 0; i < 12; ++i) of[i] = o[i] * rl;
        const unsigned dv = r ? (unsigned)((3 - 2 * qi) * 40960 + 256) : 0u;
        store_o(ro, v16 + dv, hh ? OOB : v8 + dv - (r ? 128 : 0), of);
      }
      LBAR();
#pragma unroll 1
      for (int G = 0; G < 4; ++G) {
#pragma unroll
        for (int u = 0; u < 5; ++u) P_DMA2(u, G);
      }
      WAIT_BAR(0);
    }
  } else {
    WFrag wf = load_wfrag(sm + c0, sm + c1, hh, lane);
    PIN(wf.t0k0); PIN(wf.t1k0); PIN(wf.t0k1); PIN(wf.t1k1);
    LBAR();
    const LAS char* xp = sm + c2 + hh * 2048;
    const LAS char* x4 = sm + c2 + 4096;
    const HRow rk0 = hh_read(xp + L_LOC, x4 + L_LOC), rk1 = hh_read(xp + L_LOC + 256, x4 + L_LOC + 256);
    const HRow rq0 = hh_read(xp + L_Q, x4 + L_Q), rq1 = hh_read(xp + L_Q + 256, x4 + L_Q + 256);
    const HProj hk0 = hh_proj(wf, rk0);
    bf16x8 vf[4];
#pragma unroll
    for (int ks = 0; ks < 4; ++ks) vf[ks] = cat8(trread(sm + c5 + ks * 128), trread(sm + c5 + ks * 128 + 32));
#pragma unroll
    for (int ks = 12; ks < 18; ++ks) P_DMA(ks);
    SB();
    f32x16 d0 = {}, d1 = {};
    HProj hq0, hq1;
    {
      unsigned ph[10];
      HProj hk1 = hh_proj(wf, rk1);
      hh_soft(hk0, ph);
#pragma unroll
      for (int i = 0; i < 5; ++i) *(LAS u32x2*)(smw + c3 + 8 * i) = (u32x2){ph[2 * i], ph[2 * i + 1]};
      PIN(hk1.p0); PIN(hk1.p1);
      mix_mfma<4, 8>();
      SB();
      bf16x8 pf[4];
#pragma unroll
      for (int i = 0; i < 4; ++i) pf[i] = cat8(trread(sm + c4 + (i >> 1) * 16 * 88 + (i & 1) * 64), trread(sm + c4 + ((i >> 1) * 16 + 4) * 88 + (i & 1) * 64));
      P_DMA(18); P_DMA(19);
      SB();
      hq0 = hh_proj(wf, rq0);
      d0 = MFMA32(pf[0], vf[0], d0); d1 = MFMA32(pf[1], vf[0], d1);
      d0 = MFMA32(pf[2], vf[1], d0); d1 = MFMA32(pf[3], vf[1], d1);
      hh_soft(hk1, ph);
#pragma unroll
      for (int i = 0; i < 5; ++i) *(LAS u32x2*)(smw + c3 + 32 * 88 + 8 * i) = (u32x2){ph[2 * i], ph[2 * i + 1]};
      PIN(hq0.p0); PIN(hq0.p1); PIN(d0); PIN(d1);
      mix_mfma<8, 8>();
      SB();
#pragma unroll
      for (int i = 0; i < 4; ++i) pf[i] = cat8(trread(sm + c4 + (32 + (i >> 1) * 16) * 88 + (i & 1) * 64), trread(sm + c4 + (36 + (i >> 1) * 16) * 88 + (i & 1) * 64));
      d0 = MFMA32(pf[0], vf[2], d0); d1 = MFMA32(pf[1], vf[2], d1);
      d0 = MFMA32(pf[2], vf[3], d0); d1 = MFMA32(pf[3], vf[3], d1);
    }
    *(LAS f32x4*)(smw + c6) = (f32x4){d0[0], d0[1], d0[2], d0[3]};
    *(LAS f32x4*)(smw + c6 + 32) = (f32x4){d0[4], d0[5], d0[6], d0[7]};
    *(LAS f32x4*)(smw + c6 + 64) = (f32x4){d0[8], d0[9], d0[10], d0[11]};
    *(LAS f32x4*)(smw + c6 + 96) = (f32x4){d0[12], d0[13], d0[14], d0[15]};
    *(LAS f32x4*)(smw + c6 + 128) = (f32x4){d1[0], d1[1], d1[2], d1[3]};
    SB();
    hq1 = hh_proj(wf, rq1);
    PIN(hq1.p0); PIN(hq1.p1);
    LBAR();
    const rsrc_t rkv = mkrsrc(ws + WS_KVP + nt * 16 * 7168, 16 * 7168);
    {
      const int e = (wid & 1) * 64 + lane;
      const LAS char* o = sm + A_PHI + (wid & ~1) * 5632 + e * 32;
      const f32x4 a = *(const LAS f32x4*)o + *(const LAS f32x4*)(o + 5632), b = *(const LAS f32x4*)(o + 16) + *(const LAS f32x4*)(o + 5648);
      st16c(rkv, e < 112 ? (unsigned)(mt * 7168 + hl * 1792 + e * 16) : OOB, (u32x4){pk2(a[0], a[1]), pk2(a[2], a[3]), pk2(b[0], b[1]), pk2(b[2], b[3])});
    }
    SB();
    unsigned phq[2][10];
    hh_soft(hq0, phq[0]);
#pragma unroll
    for (int i = 0; i < 10; ++i) PIN(phq[0][i]);
    WAIT_BAR(0);
    if (wid == 0) put_word(ws, vf1, gc);
#pragma unroll
    for (int ks = 0; ks < 6; ++ks) P_DMA(ks);
    if (tid >= 256) *(LAS u32x2*)(smw + A_KV + ((tid - 256) >> 1) * 112 + (tid & 1) * 48 + 40) = (u32x2){0u, 0u};
    hh_soft(hq1, phq[1]);
#pragma unroll
    for (int i = 0; i < 10; ++i) PIN(phq[1][i]);
    int kd0, kd1;
    {
      const int h4 = tid / 112, e = tid - h4 * 112, d = e / 5, oc = e - d * 5;
      kd0 = e < 105 ? A_KV + h4 * 3584 + d * 112 + oc * 16 + (oc > 2 ? 8 : 0) : L_S;
      kd1 = kd0 + (oc == 2 ? 16 : 8);
      PIN(kd0); PIN(kd1);
    }
    wait_flags(ws, (unsigned)(WS_IN + me * 256 + fr * 8), true, tag);
    if (wid < 7) {
      u32x4 p[16];
#pragma unroll
      for (int i = 0; i < 16; ++i) p[i] = __builtin_amdgcn_raw_buffer_load_b128(rkv, tid * 16, i * 7168, 16);
      f32x2 lo[2] = {}, hi[2] = {};
#pragma unroll
      for (int i = 0; i < 16; ++i)
#pragma unroll
        for (int u = 0; u < 2; ++u) {
          lo[u] += (f32x2){__uint_as_float(p[i][2 * u] << 16), __uint_as_float(p[i][2 * u + 1] << 16)};
          hi[u] += (f32x2){__uint_as_float(p[i][2 * u] & 0xFFFF0000u), __uint_as_float(p[i][2 * u + 1] & 0xFFFF0000u)};
        }
      *(LAS u32x2*)(smw + kd0) = (u32x2){pk2(lo[0][0], hi[0][0]), pk2(lo[0][1], hi[0][1])};
      *(LAS u32x2*)(smw + kd1) = (u32x2){pk2(lo[1][0], hi[1][0]), pk2(lo[1][1], hi[1][1])};
    }
#pragma unroll
    for (int ks = 6; ks < 12; ++ks) P_DMA(ks);
    LBAR();
    const LAS char* kp = sm + A_KV + hl * 3584 + (r32 == 24 ? 20 : r32) * 112 + hh * 48;
    const bf16x8 f0 = *(const LAS bf16x8*)(kp), f1 = *(const LAS bf16x8*)(kp + 16), f2 = *(const LAS bf16x8*)(kp + 32);
    f32x16 n[2];
#pragma unroll
    for (int t = 0; t < 2; ++t) n[t] = MFMA32(f0, asfrag((u32x4){phq[t][0], phq[t][1], phq[t][2], phq[t][3]}), (f32x16){});
#pragma unroll
    for (int t = 0; t < 2; ++t) n[t] = MFMA32(f1, asfrag((u32x4){phq[t][4], phq[t][5], phq[t][6], phq[t][7]}), n[t]);
#pragma unroll
    for (int t = 0; t < 2; ++t) n[t] = MFMA32(f2, asfrag((u32x4){phq[t][8], phq[t][9], 0u, 0u}), n[t]);
#pragma unroll
    for (int t = 0; t < 2; ++t) {
      const float den = rbf(rbf(hh ? n[t][8] : n[t][12]) + 1e-6f);
      const float r = __builtin_amdgcn_rcpf(den);
      float o[12];
#pragma unroll
      for (int i = 0; i < 12; ++i) o[i] = rbf(n[t][i]) * r;
      store_o(ro, v16 + t * 40960, hh ? OOB : v8 + t * 40960, o);
    }
    WAIT_BAR(0);
  }
  {
    if (wid == 0) put_word(ws, vf2, gc);
    int oa[3];
    unsigned og[3];
#pragma unroll
    for (int i = 0; i < 3; ++i) {
      const int c = min(lane + 64 * i, 160), row = c / 10, ch = c - row * 10;
      oa[i] = wid * 2816 + row * 176 + ch * 16;
      og[i] = (unsigned)(row * 2560 + ch * 16);
      PIN(oa[i]); PIN(og[i]);
    }
    const int pm = wid & 3, kh = wid >> 2;
    wait_flags(ws, (unsigned)(WS_IN + me * 256 + 128 + (4 * kh + (lane & 3)) * 8), true, tag);
    const rsrc_t rf = mkrsrc(ws + WS_O + (unsigned)((br * 128 + mt3 * 8 + 2 * pm) * 20 + 10 * kh) * 1024u, 30 * 1024);
    const unsigned vk = lane * 16 + kh * 20480, vh = lane * 16 + (1 - kh) * 20480;
    u32x4 xf[10][2];
    const unsigned vk8 = lane * 8 + kh * 20480, vh8 = lane * 8 + (1 - kh) * 20480;
#define P_LD1(v, v8_, tau) ((tau) % 5 == 2 ? __builtin_bit_cast(u32x4, frag2(__builtin_amdgcn_raw_buffer_load_b64(rf, v8_, (tau) * 1024, 16), \
                                                                             __builtin_amdgcn_raw_buffer_load_b64(rf, v8_, (tau) * 1024 + 512, 16))) \
                                           : __builtin_amdgcn_raw_buffer_load_b128(rf, v, (tau) * 1024, 16))
#define P_LD(tau) do { xf[tau][0] = P_LD1(vk, vk8, tau); xf[tau][1] = P_LD1(vh, vh8, tau); } while (0)
    P_LD(0); P_LD(1); P_LD(2);
    const LAS char* wb = sm + P_W + kh * 10240 + (16 * (g >> 1) + 4 * (g & 1) + q4) * 160 + 8 * p4;
    const LAS char* wb2 = wb + 12 * 5120;
    f32x4 pc[2][5] = {};
    bf16x8 wf[2][5];
#pragma unroll
    for (int j = 0; j < 5; ++j) wf[0][j] = cat8(trread(wb + j * 32), trread(wb + 8 * 160 + j * 32));
#pragma unroll
    for (int tau = 0; tau < 10; ++tau) {
      SB();
      if (tau < 7) P_LD(tau + 3);
#pragma unroll
      for (int j = 0; j < 5; ++j) {
        if (tau < 9) {
          const int blk = ((tau + 1) % 5) * 4 + (tau + 1) / 5;
          const LAS char* wp = (blk >= 12 ? wb2 : wb) + (blk >= 12 ? blk - 12 : blk) * 5120 + j * 32;
          wf[(tau + 1) & 1][j] = cat8(trread(wp), trread(wp + 8 * 160));
        }
        pc[1][j] = MFMA16(wf[tau & 1][j], asfrag(xf[tau][1]), pc[1][j]);
        if (tau < 9) pc[0][j] = MFMA16(wf[tau & 1][j], asfrag(xf[tau][0]), pc[0][j]);
        SB();
      }
    }
#pragma unroll
    for (int j = 0; j < 5; ++j) {
      pc[0][j] = MFMA16(wf[1][j], asfrag(xf[9][0]), pc[0][j]);
      *(LAS f32x4*)(smw + P_X + (wid * 5 + j) * 1024 + lane * 16) = pc[1][j];
    }
    LBAR();
    if (tid == 0) put_word(ws, me * 16, tag);
    LAS char* ow = smw + wid * 2816 + fr * 176 + g * 8;
#pragma unroll
    for (int j = 0; j < 5; ++j) {
      const f32x4 a = pc[0][j] + *(const LAS f32x4*)(sm + P_X + ((wid ^ 4) * 5 + j) * 1024 + lane * 16);
      *(LAS u32x2*)(ow + j * 32) = (u32x2){pk2(a[0], a[1]), pk2(a[2], a[3])};
    }
    const rsrc_t rout = mkrsrc((char*)out + (size_t)(mt3 * 128 + 32 * pm + 16 * kh) * 2560 + br * 1280 + nt3 * 160, 15 * 2560 + 160);
#pragma unroll
    for (int i = 0; i < 3; ++i) __builtin_amdgcn_raw_buffer_store_b128(*(const LAS u32x4*)(sm + oa[i]), rout, og[i], 0, 0);
  }
}

extern "C" void kernel_launch(void* const* d_in, const int* in_sizes, int n_in, void* d_out, int out_size, void* d_ws,
                              size_t ws_size, hipStream_t stream) {
  k1_fused<<<256, 512, 0, stream>>>((const __hip_bfloat16*)d_in[0], (const __hip_bfloat16*)d_in[1], (const __hip_bfloat16*)d_in[4],
                                    (const __hip_bfloat16*)d_in[5], (const __hip_bfloat16*)d_in[2], (const __hip_bfloat16*)d_in[3],
                                    (const __hip_bfloat16*)d_in[6], (char*)d_ws, (__hip_bfloat16*)d_out);
}
```
